```python
import torch

def _softmax(x, axis=-1):
    return torch.softmax(x, dim=axis)
def _gelu(x):
    return torch.nn.functional.gelu(x, approximate="tanh")
def _silu(x):
    return torch.nn.functional.silu(x)
def _sigmoid(x):
    return torch.sigmoid(x)
def _layernorm(x, axis=-1, eps=1e-5):
    m = x.mean(dim=axis, keepdim=True)
    v = x.var(dim=axis, keepdim=True, unbiased=False)
    return (x - m) / torch.sqrt(v + eps)
def _scatter_add(tgt, idx, src, axis=0):
    return tgt.clone().index_add_(axis, idx, src)
def _segment_sum(src, seg, n):
    out = torch.zeros((n,) + src.shape[1:], dtype=src.dtype)
    return out.index_add_(0, seg, src)
def _pad(x, pw):
    flat = tuple(v for pair in reversed(pw) for v in pair)
    return torch.nn.functional.pad(x, flat)

kernel_name = 'interleave_layers__linear_kernel_hedgehog__dense_activation_gelu__prompt'

def setup_inputs():

    _w = (lambda a: (lambda t: t.to(torch.bfloat16) if t.is_floating_point() else t)(torch.as_tensor(a)))
    def _A_setup():
        B, S, D, H = 1, 2048, 448, 16
        Dh = D // H
        return {
            "x": _w((lambda *s: torch.randn(*s, dtype=torch.bfloat16) * 0.05)(B, S, D)),
            "wqkv": _w((lambda *s: torch.randn(*s, dtype=torch.bfloat16) * 0.4)(D, 3 * D)),
            "w_phi": _w((lambda *s: torch.randn(*s, dtype=torch.bfloat16) * 0.4)(H, Dh, Dh) * (Dh ** -0.5)),
            "wo": _w((lambda *s: torch.randn(*s, dtype=torch.bfloat16) * 0.05)(D, D)),
        }
    _A_in = _A_setup()

    def _B_setup():
        B, S, D, F = 1, 2048, 448, 1792
        return {
            "x": _w((lambda *s: torch.randn(*s, dtype=torch.bfloat16) * 0.05)(B, S, D)),
            "w1": _w((lambda *s: torch.randn(*s, dtype=torch.bfloat16) * 1.6)(D, F) * (D ** -0.5)),
            "b1": _w((lambda *s: torch.randn(*s, dtype=torch.bfloat16) * 1.6)(F) * 0.01),
            "w2": _w((lambda *s: torch.randn(*s, dtype=torch.bfloat16) * 1.6)(F, D) * (F ** -0.5)),
            "b2": _w((lambda *s: torch.randn(*s, dtype=torch.bfloat16) * 1.6)(D) * 0.01),
        }
    _B_in = _B_setup()

    return {
        "A_x": _A_in["x"],
        "A_wqkv": _A_in["wqkv"],
        "A_w_phi": _A_in["w_phi"],
        "A_wo": _A_in["wo"],
        "B_x": _B_in["x"],
        "B_w1": _B_in["w1"],
        "B_b1": _B_in["b1"],
        "B_w2": _B_in["w2"],
        "B_b2": _B_in["b2"],
    }

def reference(A_x, A_wqkv, A_w_phi, A_wo, B_x, B_w1, B_b1, B_w2, B_b2):
    def _A_ref(x, wqkv, w_phi, wo):
        B, S, D = x.shape
        H = 16; Dh = D // H
        qkv = (lambda x, *d: x.permute(*d))((x @ wqkv).reshape(B, S, 3, H, Dh), 2, 0, 3, 1, 4)
        q, k, v = qkv[0], qkv[1], qkv[2]
        pq = torch.einsum("bhsd,hdf->bhsf", q, w_phi)
        pk = torch.einsum("bhsd,hdf->bhsf", k, w_phi)
        phi_q = torch.concatenate([_softmax(pq, axis=-1), _softmax(-pq, axis=-1)], axis=-1)
        phi_k = torch.concatenate([_softmax(pk, axis=-1), _softmax(-pk, axis=-1)], axis=-1)
        kv = torch.einsum("bhsf,bhsd->bhfd", phi_k, v)
        z  = torch.einsum("bhsf->bhf", phi_k)
        num = torch.einsum("bhqf,bhfd->bhqd", phi_q, kv)
        den = torch.einsum("bhqf,bhf->bhq", phi_q, z)[..., None] + 1e-6
        o = num / den
        return (lambda x, *d: x.permute(*d))(o, 0, 2, 1, 3).reshape(B, S, D) @ wo

    def _B_ref(x, w1, b1, w2, b2):
        h = torch.einsum("bsd,df->bsf", x, w1) + b1
        h = _gelu(h)
        return torch.einsum("bsf,fd->bsd", h, w2) + b2

    x = A_x
    x = x + _A_ref(_layernorm(x, axis=-1), A_wqkv, A_w_phi, A_wo)
    x = x + _B_ref(_layernorm(x, axis=-1), B_w1, B_b1, B_w2, B_b2)
    x = x + _A_ref(_layernorm(x, axis=-1), A_wqkv, A_w_phi, A_wo)
    x = x + _B_ref(_layernorm(x, axis=-1), B_w1, B_b1, B_w2, B_b2)
    return x
```

```cpp
#include <hip/hip_runtime.h>
#include <hip/hip_bf16.h>
#include <stdint.h>

typedef __attribute__((ext_vector_type(8))) short bf16x8;
typedef __attribute__((ext_vector_type(4))) short s16x4;
typedef __attribute__((ext_vector_type(16))) float f32x16;
typedef __attribute__((ext_vector_type(4))) float f32x4;
typedef __attribute__((ext_vector_type(2))) float f32x2;
typedef __attribute__((ext_vector_type(4))) unsigned u32x4;
typedef __attribute__((ext_vector_type(2))) unsigned u32x2;
typedef __attribute__((ext_vector_type(2))) __bf16 bf16x2_t;

#define DI __device__ __forceinline__
#define MFMA32(a, b, c) __builtin_amdgcn_mfma_f32_32x32x16_bf16((a), (b), (c), 0, 0, 0)

namespace {

constexpr int S_ = 2048, D_ = 448, H_ = 16, DH_ = 28, F_ = 1792;
constexpr int NTB = S_ / 32;
constexpr int KSD = D_ / 16;
constexpr int KSF = F_ / 16;
constexpr int NNT = D_ / 32;
constexpr int NFT = F_ / 32;

constexpr size_t XF_BYTES   = (size_t)NTB * KSD * 1024;
constexpr size_t ST_BYTES   = (size_t)NNT * S_ * 8;
constexpr size_t OFF_XF     = 0;
constexpr size_t OFF_ST     = OFF_XF + 4 * XF_BYTES;
constexpr size_t OFF_WQKV   = OFF_ST + 4 * ST_BYTES;
constexpr size_t OFF_CSQKV  = OFF_WQKV + (size_t)H_ * 3 * KSD * 1024;
constexpr size_t OFF_WO     = OFF_CSQKV + (size_t)H_ * 3 * 32 * 4;
constexpr size_t OFF_W1     = OFF_WO + (size_t)H_ * 2 * NNT * 1024;
constexpr size_t OFF_CS1    = OFF_W1 + (size_t)NFT * KSD * 1024;
constexpr size_t OFF_W2     = OFF_CS1 + (size_t)F_ * 4;
constexpr size_t OFF_PHIQ   = OFF_W2 + (size_t)NNT * KSF * 1024;
constexpr size_t OFF_KV     = OFF_PHIQ + (size_t)NTB * H_ * 4 * 1024;
constexpr size_t OFF_HF     = OFF_KV + (size_t)2 * H_ * 2048 * 4;
constexpr size_t WS_TOTAL   = OFF_HF + (size_t)NTB * KSF * 1024;

DI unsigned pack2(float a, float b) {
  f32x2 v = {a, b};
  bf16x2_t r = __builtin_convertvector(v, bf16x2_t);
  return __builtin_bit_cast(unsigned, r);
}
DI float bflo(unsigned u) { return __uint_as_float(u << 16); }
DI float bfhi(unsigned u) { return __uint_as_float(u & 0xffff0000u); }
DI float bf1(unsigned short u) { return __uint_as_float(((unsigned)u) << 16); }
DI bf16x8 asfrag(u32x4 v) { return __builtin_bit_cast(bf16x8, v); }
DI u32x4 pack8(const f32x16& x, int sp) {
  u32x4 p;
  p[0] = pack2(x[8 * sp + 0], x[8 * sp + 1]);
  p[1] = pack2(x[8 * sp + 2], x[8 * sp + 3]);
  p[2] = pack2(x[8 * sp + 4], x[8 * sp + 5]);
  p[3] = pack2(x[8 * sp + 6], x[8 * sp + 7]);
  return p;
}
DI float xor32(float v) { return __shfl_xor(v, 32, 64); }
DI f32x16 zero16() {
  f32x16 z;
#pragma unroll
  for (int i = 0; i < 16; ++i) z[i] = 0.f;
  return z;
}
DI void glds16(const void* g, void* l) {
  __builtin_amdgcn_global_load_lds((const __attribute__((address_space(1))) unsigned*)g,
                                   (__attribute__((address_space(3))) unsigned*)l, 16, 0, 0);
}

__global__ __launch_bounds__(256) void k_prep(const unsigned short* __restrict__ x,
                                              const unsigned short* __restrict__ wqkv,
                                              const unsigned short* __restrict__ wphi,
                                              const unsigned short* __restrict__ wo,
                                              const unsigned short* __restrict__ w1,
                                              const unsigned short* __restrict__ w2,
                                              char* __restrict__ ws) {
  __shared__ float red[4][32];
  const int b = blockIdx.x, tid = threadIdx.x, w = tid >> 6, l = tid & 63, r = l & 31, hh = l >> 5;
  ((float*)(ws + OFF_KV))[b * 256 + tid] = 0.f;

  if (b < 32) {
    const int h = b >> 1, p = b & 1;
    u32x4 bfr[2];
#pragma unroll
    for (int s2 = 0; s2 < 2; ++s2) {
      unsigned short v[8];
#pragma unroll
      for (int j = 0; j < 8; ++j) {
        const int e = 16 * s2 + 8 * hh + j;
        v[j] = (e < DH_ && r < DH_) ? wphi[(h * DH_ + e) * DH_ + r] : (unsigned short)0;
      }
#pragma unroll
      for (int j = 0; j < 4; ++j) bfr[s2][j] = (unsigned)v[2 * j] | ((unsigned)v[2 * j + 1] << 16);
    }
    float cs = 0.f;
    u32x4* dst = (u32x4*)(ws + OFF_WQKV) + (size_t)(h * 3 + p) * KSD * 64;
    for (int blk = w; blk < 14; blk += 4) {
      const unsigned short* row = wqkv + (size_t)(32 * blk + r) * (3 * D_) + p * D_ + DH_ * h;
      f32x16 acc = zero16();
#pragma unroll
      for (int s2 = 0; s2 < 2; ++s2) {
        const int e0 = 16 * s2 + 8 * hh;
        u32x2 lo = *(const u32x2*)(row + e0);
        u32x2 hi = {0u, 0u};
        if (e0 + 4 < DH_) hi = *(const u32x2*)(row + e0 + 4);
        u32x4 a = {lo[0], lo[1], hi[0], hi[1]};
        acc = MFMA32(asfrag(a), asfrag(bfr[s2]), acc);
      }
#pragma unroll
      for (int sp = 0; sp < 2; ++sp) {
        u32x4 pk = pack8(acc, sp);
        dst[(2 * blk + sp) * 64 + l] = pk;
#pragma unroll
        for (int j = 0; j < 4; ++j) cs += bflo(pk[j]) + bfhi(pk[j]);
      }
    }
    cs += xor32(cs);
    if (hh == 0) red[w][r] = cs;
    __syncthreads();
    if (tid < 32)
      ((float*)(ws + OFF_CSQKV))[(h * 3 + p) * 32 + tid] = red[0][tid] + red[1][tid] + red[2][tid] + red[3][tid];
  } else if (b < 48) {
    const int h = b - 32;
    float cs = 0.f;
    u32x4* dst = (u32x4*)(ws + OFF_WQKV) + (size_t)(h * 3 + 2) * KSD * 64;
#pragma unroll
    for (int i = 0; i < 7; ++i) {
      const int s = w * 7 + i;
      unsigned short v[8];
#pragma unroll
      for (int j = 0; j < 8; ++j) {
        const int k = 16 * s + 8 * (j >> 2) + 4 * hh + (j & 3);
        v[j] = (r < DH_) ? wqkv[(size_t)k * (3 * D_) + 2 * D_ + DH_ * h + r] : (unsigned short)0;
      }
      u32x4 pk;
#pragma unroll
      for (int j = 0; j < 4; ++j) {
        pk[j] = (unsigned)v[2 * j] | ((unsigned)v[2 * j + 1] << 16);
        cs += bf1(v[2 * j]) + bf1(v[2 * j + 1]);
      }
      dst[s * 64 + l] = pk;
    }
    cs += xor32(cs);
    if (hh == 0) red[w][r] = cs;
    __syncthreads();
    if (tid < 32)
      ((float*)(ws + OFF_CSQKV))[(h * 3 + 2) * 32 + tid] = red[0][tid] + red[1][tid] + red[2][tid] + red[3][tid];
  } else if (b < 64) {
    const int h = b - 48;
    u32x4* dst = (u32x4*)(ws + OFF_WO) + (size_t)h * 28 * 64;
#pragma unroll
    for (int i = 0; i < 7; ++i) {
      const int fi = w * 7 + i, ks = fi / NNT, nt = fi % NNT;
      unsigned short v[8];
#pragma unroll
      for (int j = 0; j < 8; ++j) {
        const int dd = 16 * ks + 8 * (j >> 2) + 4 * hh + (j & 3);
        v[j] = (dd < DH_) ? wo[(size_t)(DH_ * h + dd) * D_ + 32 * nt + r] : (unsigned short)0;
      }
      u32x4 pk;
#pragma unroll
      for (int j = 0; j < 4; ++j) pk[j] = (unsigned)v[2 * j] | ((unsigned)v[2 * j + 1] << 16);
      dst[fi * 64 + l] = pk;
    }
  } else if (b < 120) {
    const int ft = b - 64;
    float cs = 0.f;
    u32x4* dst = (u32x4*)(ws + OFF_W1) + (size_t)ft * KSD * 64;
#pragma unroll
    for (int i = 0; i < 7; ++i) {
      const int s = w * 7 + i;
      unsigned short v[8];
#pragma unroll
      for (int j = 0; j < 8; ++j) {
        const int k = 16 * s + 8 * (j >> 2) + 4 * hh + (j & 3);
        v[j] = w1[(size_t)k * F_ + 32 * ft + r];
      }
      u32x4 pk;
#pragma unroll
      for (int j = 0; j < 4; ++j) {
        pk[j] = (unsigned)v[2 * j] | ((unsigned)v[2 * j + 1] << 16);
        cs += bf1(v[2 * j]) + bf1(v[2 * j + 1]);
      }
      dst[s * 64 + l] = pk;
    }
    cs += xor32(cs);
    if (hh == 0) red[w][r] = cs;
    __syncthreads();
    if (tid < 32)
      ((float*)(ws + OFF_CS1))[ft * 32 + tid] = red[0][tid] + red[1][tid] + red[2][tid] + red[3][tid];
  } else if (b < 176) {
    const int bb = b - 120, nt = bb >> 2, qd = bb & 3;
    u32x4* dst = (u32x4*)(ws + OFF_W2) + (size_t)nt * KSF * 64;
#pragma unroll
    for (int i = 0; i < 7; ++i) {
      const int s = qd * 28 + w * 7 + i;
      unsigned short v[8];
#pragma unroll
      for (int j = 0; j < 8; ++j) {
        const int k = 16 * s + 8 * (j >> 2) + 4 * hh + (j & 3);
        v[j] = w2[(size_t)k * D_ + 32 * nt + r];
      }
      u32x4 pk;
#pragma unroll
      for (int j = 0; j < 4; ++j) pk[j] = (unsigned)v[2 * j] | ((unsigned)v[2 * j + 1] << 16);
      dst[s * 64 + l] = pk;
    }
  } else if (b < 240) {
    const int tb = b - 176;
    const int tok = tb * 32 + r;
    u32x4* dst = (u32x4*)(ws + OFF_XF) + (size_t)tb * KSD * 64;
    f32x2* st = (f32x2*)(ws + OFF_ST);
    for (int nt = w; nt < NNT; nt += 4) {
      float s1 = 0.f, s2 = 0.f;
#pragma unroll
      for (int sp = 0; sp < 2; ++sp) {
        const int s = 2 * nt + sp;
        const unsigned short* p = x + (size_t)tok * D_ + 16 * s + 4 * hh;
        u32x2 lo = *(const u32x2*)p;
        u32x2 hi = *(const u32x2*)(p + 8);
        u32x4 pk = {lo[0], lo[1], hi[0], hi[1]};
        dst[s * 64 + l] = pk;
#pragma unroll
        for (int j = 0; j < 4; ++j) {
          float a = bflo(pk[j]), c = bfhi(pk[j]);
          s1 += a + c;
          s2 += a * a + c * c;
        }
      }
      s1 += xor32(s1);
      s2 += xor32(s2);
      if (hh == 0) st[nt * S_ + tok] = f32x2{s1, s2};
    }
  }
}

DI void row_stats(const f32x2* __restrict__ st, int tok, float& mean, float& rstd) {
  float s1 = 0.f, s2 = 0.f;
#pragma unroll
  for (int nt = 0; nt < NNT; ++nt) {
    f32x2 v = st[nt * S_ + tok];
    s1 += v[0];
    s2 += v[1];
  }
  mean = s1 * (1.f / D_);
  float var = fmaxf(s2 * (1.f / D_) - mean * mean, 0.f);
  rstd = rsqrtf(var + 1e-5f);
}

constexpr float LOG2E = 1.4426950408889634f;

DI void dual_softmax(const f32x16& acc, const float* __restrict__ cs, float mean, float rstd, int hh,
                     f32x16& pos, f32x16& neg) {
  f32x16 y;
  float mx = -3.0e38f, mn = 3.0e38f;
#pragma unroll
  for (int g = 0; g < 4; ++g) {
    f32x4 c = *(const f32x4*)(cs + 8 * g + 4 * hh);
#pragma unroll
    for (int i = 0; i < 4; ++i) {
      float v = (acc[4 * g + i] - mean * c[i]) * rstd;
      y[4 * g + i] = v;
      const bool valid = !(g == 3 && hh == 1);
      mx = valid ? fmaxf(mx, v) : mx;
      mn = valid ? fminf(mn, v) : mn;
    }
  }
  mx = fmaxf(mx, xor32(mx));
  mn = fminf(mn, xor32(mn));
  float sp = 0.f, sn = 0.f;
#pragma unroll
  for (int g = 0; g < 4; ++g) {
#pragma unroll
    for (int i = 0; i < 4; ++i) {
      const bool valid = !(g == 3 && hh == 1);
      float ep = __builtin_amdgcn_exp2f((y[4 * g + i] - mx) * LOG2E);
      float en = __builtin_amdgcn_exp2f((mn - y[4 * g + i]) * LOG2E);
      ep = valid ? ep : 0.f;
      en = valid ? en : 0.f;
      pos[4 * g + i] = ep;
      neg[4 * g + i] = en;
      sp += ep;
      sn += en;
    }
  }
  sp += xor32(sp);
  sn += xor32(sn);
  const float ip = 1.f / sp, in = 1.f / sn;
#pragma unroll
  for (int i = 0; i < 16; ++i) {
    pos[i] *= ip;
    neg[i] *= in;
  }
}

constexpr int K1_W    = 0;
constexpr int K1_IMG  = K1_W + 84 * 1024;
constexpr int K1_RED  = K1_IMG + 16 * 1024;
constexpr int K1_MS   = K1_RED + 32 * 1024;
constexpr int K1_LDS  = K1_MS + 4 * 256;

__global__ __launch_bounds__(256, 1) void k_qkv(const u32x4* __restrict__ xf, const f32x2* __restrict__ st,
                                                const u32x4* __restrict__ wqkv_t,
                                                const float* __restrict__ cs_qkv,
                                                u32x4* __restrict__ phiq, float* __restrict__ kvacc) {
  __shared__ __attribute__((aligned(16))) char smem[K1_LDS];
  const int bid = blockIdx.x, xcd = bid & 7, bi = bid >> 3;
  const int t = 2 * xcd + (bi >> 4), h = bi & 15;
  const int tid = threadIdx.x, w = __builtin_amdgcn_readfirstlane(tid >> 6), l = tid & 63, r = l & 31, hh = l >> 5;
  const int tb = t * 4 + w;

  {
    const u32x4* src = wqkv_t + (size_t)(h * 84 + w * 21) * 64 + l;
    char* dst = smem + K1_W + w * 21 * 1024;
#pragma unroll
    for (int i = 0; i < 21; ++i) glds16(src + i * 64, dst + i * 1024);
  }
  u32x4 xr[KSD];
#pragma unroll
  for (int s = 0; s < KSD; ++s) xr[s] = xf[(size_t)(tb * KSD + s) * 64 + l];
  float mean, rstd;
  row_stats(st, tb * 32 + r, mean, rstd);
  float* ms = (float*)(smem + K1_MS) + w * 64;
  if (hh == 0) { ms[r] = mean; ms[32 + r] = rstd; }
  __syncthreads();

  const u32x4* wl = (const u32x4*)(smem + K1_W) + l;
  f32x16 aq = zero16(), ak = zero16(), av = zero16();
#pragma unroll
  for (int s = 0; s < KSD; ++s) {
    ak = MFMA32(asfrag(wl[(KSD + s) * 64]), asfrag(xr[s]), ak);
    av = MFMA32(asfrag(xr[s]), asfrag(wl[(2 * KSD + s) * 64]), av);
    aq = MFMA32(asfrag(wl[s * 64]), asfrag(xr[s]), aq);
  }

  const float* cs = cs_qkv + h * 96;
  char* img = smem + K1_IMG + w * 4096;
  {
    f32x16 pos, neg;
    dual_softmax(ak, cs + 32, mean, rstd, hh, pos, neg);
#pragma unroll
    for (int g = 0; g < 4; ++g) {
      const int ch = (2 * g + hh) ^ ((r >> 1) & 7);
      u32x2 a = {pack2(pos[4 * g], pos[4 * g + 1]), pack2(pos[4 * g + 2], pos[4 * g + 3])};
      u32x2 c = {pack2(neg[4 * g], neg[4 * g + 1]), pack2(neg[4 * g + 2], neg[4 * g + 3])};
      *(u32x2*)(img + r * 64 + ch * 8) = a;
      *(u32x2*)(img + 2048 + r * 64 + ch * 8) = c;
    }
  }
  u32x4 vfr[2];
  {
    const float csv = cs[64 + r];
    f32x16 vn;
#pragma unroll
    for (int g = 0; g < 4; ++g) {
      f32x4 m4 = *(const f32x4*)(ms + 8 * g + 4 * hh);
      f32x4 r4 = *(const f32x4*)(ms + 32 + 8 * g + 4 * hh);
#pragma unroll
      for (int i = 0; i < 4; ++i) {
        float v = (av[4 * g + i] - m4[i] * csv) * r4[i];
        vn[4 * g + i] = (r == DH_) ? 1.f : v;
      }
    }
    vfr[0] = pack8(vn, 0);
    vfr[1] = pack8(vn, 1);
  }
  f32x16 kv0 = zero16(), kv1 = zero16();
  {
    const int i16 = l & 15, q4 = i16 >> 2, p4 = i16 & 3, blk = (l >> 4) & 1;
#pragma unroll
    for (int s = 0; s < 2; ++s) {
      const int t0 = 16 * s + 4 * hh + q4, t1 = t0 + 8;
      const int c0 = (4 * blk + p4) ^ ((t0 >> 1) & 7), c1 = (4 * blk + p4) ^ ((t1 >> 1) & 7);
      typedef __attribute__((address_space(3))) s16x4* lp;
      s16x4 a0 = __builtin_amdgcn_ds_read_tr16_b64_v4i16((lp)(img + t0 * 64 + c0 * 8));
      s16x4 a1 = __builtin_amdgcn_ds_read_tr16_b64_v4i16((lp)(img + t1 * 64 + c1 * 8));
      s16x4 b0 = __builtin_amdgcn_ds_read_tr16_b64_v4i16((lp)(img + 2048 + t0 * 64 + c0 * 8));
      s16x4 b1 = __builtin_amdgcn_ds_read_tr16_b64_v4i16((lp)(img + 2048 + t1 * 64 + c1 * 8));
      bf16x8 pa = __builtin_shufflevector(a0, a1, 0, 1, 2, 3, 4, 5, 6, 7);
      bf16x8 pb = __builtin_shufflevector(b0, b1, 0, 1, 2, 3, 4, 5, 6, 7);
      kv0 = MFMA32(pa, asfrag(vfr[s]), kv0);
      kv1 = MFMA32(pb, asfrag(vfr[s]), kv1);
    }
  }
  {
    f32x4* red = (f32x4*)(smem + K1_RED) + w * 512 + l;
#pragma unroll
    for (int g = 0; g < 4; ++g) {
      red[g * 64] = f32x4{kv0[4 * g], kv0[4 * g + 1], kv0[4 * g + 2], kv0[4 * g + 3]};
      red[(4 + g) * 64] = f32x4{kv1[4 * g], kv1[4 * g + 1], kv1[4 * g + 2], kv1[4 * g + 3]};
    }
  }
  {
    f32x16 pos, neg;
    dual_softmax(aq, cs, mean, rstd, hh, pos, neg);
    u32x4* dst = phiq + (size_t)((tb * H_ + h) * 4) * 64 + l;
    dst[0] = pack8(pos, 0);
    dst[64] = pack8(pos, 1);
    dst[128] = pack8(neg, 0);
    dst[192] = pack8(neg, 1);
  }
  __syncthreads();
  {
    const float* red = (const float*)(smem + K1_RED);
    float* dst = kvacc + h * 2048;
#pragma unroll
    for (int i = 0; i < 8; ++i) {
      const int idx = w * 512 + i * 64 + l;
      float v = red[idx] + red[2048 + idx] + red[4096 + idx] + red[6144 + idx];
      atomicAdd(dst + idx, v);
    }
  }
}

template <bool FINAL, bool BIAS>
DI void resid_epilogue(char* smem, const f32x16 (&acc)[2][2], int w, int l, int tp, int ns,
                       const u32x4* __restrict__ xin, const unsigned short* __restrict__ bias,
                       u32x4* __restrict__ xout, f32x2* __restrict__ stout,
                       unsigned short* __restrict__ out) {
  const int r = l & 31, hh = l >> 5;
  f32x4* red = (f32x4*)smem;
#pragma unroll
  for (int a = 0; a < 2; ++a)
#pragma unroll
    for (int b = 0; b < 2; ++b)
#pragma unroll
      for (int g = 0; g < 4; ++g)
        red[((w * 4 + a * 2 + b) * 4 + g) * 64 + l] =
            f32x4{acc[a][b][4 * g], acc[a][b][4 * g + 1], acc[a][b][4 * g + 2], acc[a][b][4 * g + 3]};
  const int tb = 2 * tp + (w >> 1), nt = 2 * ns + (w & 1);
  u32x4 xo[2];
  xo[0] = xin[(size_t)(tb * KSD + 2 * nt) * 64 + l];
  xo[1] = xin[(size_t)(tb * KSD + 2 * nt + 1) * 64 + l];
  u32x2 bs[4];
  if (BIAS) {
#pragma unroll
    for (int g = 0; g < 4; ++g) bs[g] = *(const u32x2*)(bias + 32 * nt + 8 * g + 4 * hh);
  }
  __syncthreads();
  f32x16 v;
#pragma unroll
  for (int g = 0; g < 4; ++g) {
    f32x4 s = red[((0 * 4 + w) * 4 + g) * 64 + l];
    s += red[((1 * 4 + w) * 4 + g) * 64 + l];
    s += red[((2 * 4 + w) * 4 + g) * 64 + l];
    s += red[((3 * 4 + w) * 4 + g) * 64 + l];
    const unsigned x0 = xo[g >> 1][(g & 1) * 2], x1 = xo[g >> 1][(g & 1) * 2 + 1];
    if (BIAS) {
      s[0] += bflo(bs[g][0]); s[1] += bfhi(bs[g][0]); s[2] += bflo(bs[g][1]); s[3] += bfhi(bs[g][1]);
    }
    v[4 * g + 0] = s[0] + bflo(x0);
    v[4 * g + 1] = s[1] + bfhi(x0);
    v[4 * g + 2] = s[2] + bflo(x1);
    v[4 * g + 3] = s[3] + bfhi(x1);
  }
  u32x4 p0 = pack8(v, 0), p1 = pack8(v, 1);
  if (FINAL) {
    unsigned short* o = out + (size_t)(tb * 32 + r) * D_ + 32 * nt + 4 * hh;
    *(u32x2*)(o + 0) = u32x2{p0[0], p0[1]};
    *(u32x2*)(o + 8) = u32x2{p0[2], p0[3]};
    *(u32x2*)(o + 16) = u32x2{p1[0], p1[1]};
    *(u32x2*)(o + 24) = u32x2{p1[2], p1[3]};
  } else {
    xout[(size_t)(tb * KSD + 2 * nt) * 64 + l] = p0;
    xout[(size_t)(tb * KSD + 2 * nt + 1) * 64 + l] = p1;
    float s1 = 0.f, s2 = 0.f;
#pragma unroll
    for (int j = 0; j < 4; ++j) {
      float a = bflo(p0[j]), c = bfhi(p0[j]), d = bflo(p1[j]), e = bfhi(p1[j]);
      s1 += (a + c) + (d + e);
      s2 += (a * a + c * c) + (d * d + e * e);
    }
    s1 += xor32(s1);
    s2 += xor32(s2);
    if (hh == 0) stout[nt * S_ + tb * 32 + r] = f32x2{s1, s2};
  }
}

__global__ __launch_bounds__(256, 1) void k_attn_out(const u32x4* __restrict__ phiq,
                                                     const float* __restrict__ kvacc,
                                                     const u32x4* __restrict__ wo_t,
                                                     const u32x4* __restrict__ xin,
                                                     u32x4* __restrict__ xout, f32x2* __restrict__ stout) {
  __shared__ __attribute__((aligned(16))) char smem[64 * 1024];
  const int bid = blockIdx.x, xcd = bid & 7, bi = bid >> 3;
  const int tp = 4 * xcd + bi / 7, ns = bi % 7;
  const int tid = threadIdx.x, w = __builtin_amdgcn_readfirstlane(tid >> 6), l = tid & 63, r = l & 31;
  f32x16 acc[2][2];
#pragma unroll
  for (int a = 0; a < 2; ++a)
#pragma unroll
    for (int b = 0; b < 2; ++b) acc[a][b] = zero16();
#pragma unroll
  for (int hi = 0; hi < 4; ++hi) {
    const int hd = 4 * w + hi;
    u32x4 kvf[4];
    {
      const f32x4* kp = (const f32x4*)(kvacc + hd * 2048) + l;
#pragma unroll
      for (int i = 0; i < 4; ++i) {
        f32x4 a = kp[(2 * i) * 64], b = kp[(2 * i + 1) * 64];
        kvf[i] = u32x4{pack2(a[0], a[1]), pack2(a[2], a[3]), pack2(b[0], b[1]), pack2(b[2], b[3])};
      }
    }
    u32x4 wf[2][2];
#pragma unroll
    for (int sp = 0; sp < 2; ++sp)
#pragma unroll
      for (int nt = 0; nt < 2; ++nt) wf[sp][nt] = wo_t[(size_t)((hd * 2 + sp) * NNT + 2 * ns + nt) * 64 + l];
#pragma unroll
    for (int a = 0; a < 2; ++a) {
      const u32x4* pq = phiq + (size_t)(((2 * tp + a) * H_ + hd) * 4) * 64 + l;
      f32x16 o = zero16();
#pragma unroll
      for (int i = 0; i < 4; ++i) o = MFMA32(asfrag(kvf[i]), asfrag(pq[i * 64]), o);
      const float den = __shfl(o[12], 32 + r, 64) + 1e-6f;
      const float inv = 1.f / den;
#pragma unroll
      for (int i = 0; i < 16; ++i) o[i] *= inv;
      u32x4 of0 = pack8(o, 0), of1 = pack8(o, 1);
#pragma unroll
      for (int nt = 0; nt < 2; ++nt) {
        acc[a][nt] = MFMA32(asfrag(wf[0][nt]), asfrag(of0), acc[a][nt]);
        acc[a][nt] = MFMA32(asfrag(wf[1][nt]), asfrag(of1), acc[a][nt]);
      }
    }
  }
  resid_epilogue<false, false>(smem, acc, w, l, tp, ns, xin, nullptr, xout, stout, nullptr);
}

constexpr int K3_X  = 0;
constexpr int K3_MS = 112 * 1024;
constexpr int K3_LDS = K3_MS + 1024;

__global__ __launch_bounds__(256, 1) void k_ffn1(const u32x4* __restrict__ xf, const f32x2* __restrict__ st,
                                                 const u32x4* __restrict__ w1_t, const float* __restrict__ cs1,
                                                 const unsigned short* __restrict__ b1,
                                                 u32x4* __restrict__ hf) {
  __shared__ __attribute__((aligned(16))) char smem[K3_LDS];
  const int bid = blockIdx.x, xcd = bid & 7, bi = bid >> 3;
  const int tt = 2 * xcd + bi / 14, fg = bi % 14;
  const int tid = threadIdx.x, w = __builtin_amdgcn_readfirstlane(tid >> 6), l = tid & 63, r = l & 31, hh = l >> 5;
  const int ft = 4 * fg + w;
  {
    const u32x4* src = xf + (size_t)((tt * 4 + w) * KSD) * 64 + l;
    char* dst = smem + K3_X + w * KSD * 1024;
#pragma unroll
    for (int i = 0; i < KSD; ++i) glds16(src + i * 64, dst + i * 1024);
  }
  u32x4 wr[KSD];
#pragma unroll
  for (int s = 0; s < KSD; ++s) wr[s] = w1_t[(size_t)(ft * KSD + s) * 64 + l];
  float* ms = (float*)(smem + K3_MS);
  if (tid < 128) {
    float mean, rstd;
    row_stats(st, tt * 128 + tid, mean, rstd);
    ms[tid] = mean;
    ms[128 + tid] = rstd;
  }
  f32x16 csr, bsr;
#pragma unroll
  for (int g = 0; g < 4; ++g) {
    f32x4 c = *(const f32x4*)(cs1 + 32 * ft + 8 * g + 4 * hh);
    u32x2 bb = *(const u32x2*)(b1 + 32 * ft + 8 * g + 4 * hh);
    csr[4 * g] = c[0]; csr[4 * g + 1] = c[1]; csr[4 * g + 2] = c[2]; csr[4 * g + 3] = c[3];
    bsr[4 * g] = bflo(bb[0]); bsr[4 * g + 1] = bfhi(bb[0]); bsr[4 * g + 2] = bflo(bb[1]); bsr[4 * g + 3] = bfhi(bb[1]);
  }
  __syncthreads();
  const u32x4* xl = (const u32x4*)(smem + K3_X) + l;
#pragma unroll
  for (int a = 0; a < 4; ++a) {
    f32x16 acc = zero16();
#pragma unroll
    for (int s = 0; s < KSD; ++s) acc = MFMA32(asfrag(wr[s]), asfrag(xl[(a * KSD + s) * 64]), acc);
    const float mean = ms[a * 32 + r], rstd = ms[128 + a * 32 + r];
    f32x16 hv;
#pragma unroll
    for (int i = 0; i < 16; ++i) {
      const float y = (acc[i] - mean * csr[i]) * rstd + bsr[i];
      const float c0 = -2.f * 0.7978845608028654f * LOG2E;
      const float z = y * (c0 + (c0 * 0.044715f) * (y * y));
      const float e = __builtin_amdgcn_exp2f(z);
      hv[i] = y * __builtin_amdgcn_rcpf(1.f + e);
    }
    u32x4* dst = hf + (size_t)((tt * 4 + a) * KSF + 2 * ft) * 64 + l;
    dst[0] = pack8(hv, 0);
    dst[64] = pack8(hv, 1);
  }
}

template <bool FINAL>
__global__ __launch_bounds__(256, 1) void k_ffn2(const u32x4* __restrict__ hf, const u32x4* __restrict__ w2_t,
                                                 const unsigned short* __restrict__ b2,
                                                 const u32x4* __restrict__ xin, u32x4* __restrict__ xout,
                                                 f32x2* __restrict__ stout, unsigned short* __restrict__ out) {
  __shared__ __attribute__((aligned(16))) char smem[64 * 1024];
  const int bid = blockIdx.x, xcd = bid & 7, bi = bid >> 3;
  const int tp = 4 * xcd + bi / 7, ns = bi % 7;
  const int tid = threadIdx.x, w = __builtin_amdgcn_readfirstlane(tid >> 6), l = tid & 63;
  f32x16 acc[2][2];
#pragma unroll
  for (int a = 0; a < 2; ++a)
#pragma unroll
    for (int b = 0; b < 2; ++b) acc[a][b] = zero16();
  const u32x4* h0 = hf + (size_t)((2 * tp) * KSF + 28 * w) * 64 + l;
  const u32x4* h1 = h0 + (size_t)KSF * 64;
  const u32x4* w0 = w2_t + (size_t)((2 * ns) * KSF + 28 * w) * 64 + l;
  const u32x4* w1p = w0 + (size_t)KSF * 64;
#pragma unroll
  for (int s = 0; s < 28; ++s) {
    u32x4 a0 = w0[s * 64], a1 = w1p[s * 64], b0 = h0[s * 64], b1 = h1[s * 64];
    acc[0][0] = MFMA32(asfrag(a0), asfrag(b0), acc[0][0]);
    acc[0][1] = MFMA32(asfrag(a1), asfrag(b0), acc[0][1]);
    acc[1][0] = MFMA32(asfrag(a0), asfrag(b1), acc[1][0]);
    acc[1][1] = MFMA32(asfrag(a1), asfrag(b1), acc[1][1]);
  }
  resid_epilogue<FINAL, true>(smem, acc, w, l, tp, ns, xin, b2, xout, stout, out);
}

}

extern "C" void kernel_launch(void* const* d_in, const int* in_sizes, int n_in, void* d_out, int out_size,
                              void* d_ws, size_t ws_size, hipStream_t stream) {
  const unsigned short* x = (const unsigned short*)d_in[0];
  const unsigned short* wqkv = (const unsigned short*)d_in[1];
  const unsigned short* wphi = (const unsigned short*)d_in[2];
  const unsigned short* wo = (const unsigned short*)d_in[3];
  const unsigned short* w1 = (const unsigned short*)d_in[5];
  const unsigned short* b1 = (const unsigned short*)d_in[6];
  const unsigned short* w2 = (const unsigned short*)d_in[7];
  const unsigned short* b2 = (const unsigned short*)d_in[8];
  char* ws = (char*)d_ws;
  unsigned short* out = (unsigned short*)d_out;

  auto XF = [&](int i) { return (u32x4*)(ws + OFF_XF + (size_t)i * XF_BYTES); };
  auto ST = [&](int i) { return (f32x2*)(ws + OFF_ST + (size_t)i * ST_BYTES); };
  const u32x4* wqkv_t = (const u32x4*)(ws + OFF_WQKV);
  const float* cs_qkv = (const float*)(ws + OFF_CSQKV);
  const u32x4* wo_t = (const u32x4*)(ws + OFF_WO);
  const u32x4* w1_t = (const u32x4*)(ws + OFF_W1);
  const float* cs1 = (const float*)(ws + OFF_CS1);
  const u32x4* w2_t = (const u32x4*)(ws + OFF_W2);
  u32x4* phiq = (u32x4*)(ws + OFF_PHIQ);
  float* kv = (float*)(ws + OFF_KV);
  u32x4* hf = (u32x4*)(ws + OFF_HF);

  k_prep<<<256, 256, 0, stream>>>(x, wqkv, wphi, wo, w1, w2, ws);
  for (int layer = 0; layer < 2; ++layer) {
    const int i0 = 2 * layer;
    float* kvl = kv + layer * H_ * 2048;
    k_qkv<<<256, 256, 0, stream>>>(XF(i0), ST(i0), wqkv_t, cs_qkv, phiq, kvl);
    k_attn_out<<<224, 256, 0, stream>>>(phiq, kvl, wo_t, XF(i0), XF(i0 + 1), ST(i0 + 1));
    k_ffn1<<<224, 256, 0, stream>>>(XF(i0 + 1), ST(i0 + 1), w1_t, cs1, b1, hf);
    if (layer == 0)
      k_ffn2<false><<<224, 256, 0, stream>>>(hf, w2_t, b2, XF(i0 + 1), XF(i0 + 2), ST(i0 + 2), nullptr);
    else
      k_ffn2<true><<<224, 256, 0, stream>>>(hf, w2_t, b2, XF(i0 + 1), nullptr, nullptr, out);
  }
}
```

```cpp
#include <hip/hip_runtime.h>
#include <hip/hip_bf16.h>
#include <stdint.h>

typedef __attribute__((ext_vector_type(8))) short bf16x8;
typedef __attribute__((ext_vector_type(4))) short s16x4;
typedef __attribute__((ext_vector_type(16))) float f32x16;
typedef __attribute__((ext_vector_type(4))) float f32x4;
typedef __attribute__((ext_vector_type(2))) float f32x2;
typedef __attribute__((ext_vector_type(4))) unsigned u32x4;
typedef __attribute__((ext_vector_type(2))) unsigned u32x2;
typedef __attribute__((ext_vector_type(2))) __bf16 bf16x2_t;

#define DI __device__ __forceinline__
#define SCHED() __builtin_amdgcn_sched_barrier(0)
#define MFMA32(a, b, c) __builtin_amdgcn_mfma_f32_32x32x16_bf16((a), (b), (c), 0, 0, 0)

#define STAMP_AT(kid, i)

namespace {

constexpr int S_ = 2048, D_ = 448, H_ = 16, DH_ = 28, F_ = 1792;
constexpr int NTB = S_ / 32;
constexpr int KSD = D_ / 16;
constexpr int KSF = F_ / 16;
constexpr int NNT = D_ / 32;
constexpr int NFT = F_ / 32;

constexpr size_t XF_BYTES   = (size_t)NTB * KSD * 1024;
constexpr size_t ST_BYTES   = (size_t)NNT * S_ * 8;
constexpr size_t OFF_XF     = 0;
constexpr size_t OFF_ST     = OFF_XF + 4 * XF_BYTES;
constexpr size_t OFF_WQKV   = OFF_ST + 4 * ST_BYTES;
constexpr size_t OFF_CSQKV  = OFF_WQKV + (size_t)H_ * 3 * KSD * 1024;
constexpr size_t OFF_WO     = OFF_CSQKV + (size_t)H_ * 3 * 32 * 4;
constexpr size_t OFF_W1     = OFF_WO + (size_t)H_ * 2 * NNT * 1024;
constexpr size_t OFF_CS1    = OFF_W1 + (size_t)NFT * KSD * 1024;
constexpr size_t OFF_W2     = OFF_CS1 + (size_t)F_ * 4;
constexpr size_t OFF_PHIQ   = OFF_W2 + (size_t)NNT * KSF * 1024;
constexpr size_t OFF_KV     = OFF_PHIQ + (size_t)NTB * H_ * 4 * 1024;
constexpr size_t OFF_HF     = OFF_KV + (size_t)2 * H_ * 2048 * 4;
constexpr size_t WS_TOTAL   = OFF_HF + (size_t)NTB * KSF * 1024;

DI unsigned pack2(float a, float b) {
  f32x2 v = {a, b};
  bf16x2_t r = __builtin_convertvector(v, bf16x2_t);
  return __builtin_bit_cast(unsigned, r);
}
DI float bflo(unsigned u) { return __uint_as_float(u << 16); }
DI float bfhi(unsigned u) { return __uint_as_float(u & 0xffff0000u); }
DI float bf1(unsigned short u) { return __uint_as_float(((unsigned)u) << 16); }
DI bf16x8 asfrag(u32x4 v) { return __builtin_bit_cast(bf16x8, v); }
DI u32x4 pack8(const f32x16& x, int sp) {
  u32x4 p;
  p[0] = pack2(x[8 * sp + 0], x[8 * sp + 1]);
  p[1] = pack2(x[8 * sp + 2], x[8 * sp + 3]);
  p[2] = pack2(x[8 * sp + 4], x[8 * sp + 5]);
  p[3] = pack2(x[8 * sp + 6], x[8 * sp + 7]);
  return p;
}
DI float xor32(float v) { return __shfl_xor(v, 32, 64); }
DI f32x16 zero16() {
  f32x16 z;
#pragma unroll
  for (int i = 0; i < 16; ++i) z[i] = 0.f;
  return z;
}
DI void glds16(const void* g, void* l) {
  __builtin_amdgcn_global_load_lds((const __attribute__((address_space(1))) unsigned*)g,
                                   (__attribute__((address_space(3))) unsigned*)l, 16, 0, 0);
}

#define BARRIER() asm volatile("s_waitcnt lgkmcnt(0)\n\ts_barrier" ::: "memory")

typedef __attribute__((address_space(3))) s16x4* lds_s16x4p;
DI s16x4 tr_read(const char* p) { return __builtin_amdgcn_ds_read_tr16_b64_v4i16((lds_s16x4p)p); }
DI u32x4 join(s16x4 a, s16x4 b) {
  bf16x8 v = __builtin_shufflevector(a, b, 0, 1, 2, 3, 4, 5, 6, 7);
  return __builtin_bit_cast(u32x4, v);
}

DI u32x4 frag_from_image(const char* img, int row_stride, int s, int l, int cb) {
  const int i16 = l & 15, q4 = i16 >> 2, p4 = i16 & 3, blk = (l >> 4) & 1, hh = l >> 5;
  const char* p = img + (16 * s + 4 * hh + q4) * row_stride + cb + (4 * blk + p4) * 8;
  return join(tr_read(p), tr_read(p + 8 * row_stride));
}

__global__ __launch_bounds__(256) void k_prep(const unsigned short* __restrict__ x,
                                              const unsigned short* __restrict__ wqkv,
                                              const unsigned short* __restrict__ wphi,
                                              const unsigned short* __restrict__ wo,
                                              const unsigned short* __restrict__ w1,
                                              const unsigned short* __restrict__ w2,
                                              char* __restrict__ ws) {
  __shared__ __attribute__((aligned(16))) char img[32 * 912 + 512];
  float (*red)[32] = (float (*)[32])(img + 32 * 912);
  const int b = blockIdx.x, tid = threadIdx.x, w = tid >> 6, l = tid & 63, r = l & 31, hh = l >> 5;
  ((float*)(ws + OFF_KV))[b * 256 + tid] = 0.f;

  if (b < 32) {
    const int h = b >> 1, p = b & 1;
    const int cc = r < DH_ ? r : r - DH_;
    u32x4 bfr[2];
    u32x2 lo[4][2], hi[4][2];
#pragma unroll
    for (int it = 0; it < 4; ++it) {
      const int blk = min(w + 4 * it, 13);
      const unsigned short* row = wqkv + (size_t)(32 * blk + r) * (3 * D_) + p * D_ + DH_ * h;
#pragma unroll
      for (int s2 = 0; s2 < 2; ++s2) {
        const int e0 = 16 * s2 + 8 * hh;
        lo[it][s2] = *(const u32x2*)(row + e0);
        hi[it][s2] = *(const u32x2*)(row + min(e0 + 4, 24));
        if (e0 + 4 >= DH_) hi[it][s2] = u32x2{0u, 0u};
      }
    }
#pragma unroll
    for (int s2 = 0; s2 < 2; ++s2) {
      unsigned short v[8];
#pragma unroll
      for (int j = 0; j < 8; ++j) {
        const int e = 16 * s2 + 8 * hh + j;
        v[j] = wphi[(h * DH_ + min(e, DH_ - 1)) * DH_ + cc];
        if (e >= DH_) v[j] = 0;
      }
#pragma unroll
      for (int j = 0; j < 4; ++j) bfr[s2][j] = (unsigned)v[2 * j] | ((unsigned)v[2 * j + 1] << 16);
    }
    float cs = 0.f;
    u32x4* dst = (u32x4*)(ws + OFF_WQKV) + (size_t)(h * 3 + p) * KSD * 64;
#pragma unroll
    for (int it = 0; it < 4; ++it) {
      const int blk = w + 4 * it;
      f32x16 acc = zero16();
#pragma unroll
      for (int s2 = 0; s2 < 2; ++s2) {
        u32x4 a = {lo[it][s2][0], lo[it][s2][1], hi[it][s2][0], hi[it][s2][1]};
        acc = MFMA32(asfrag(a), asfrag(bfr[s2]), acc);
      }
      if (blk < 14) {
#pragma unroll
        for (int sp = 0; sp < 2; ++sp) {
          u32x4 pk = pack8(acc, sp);
          dst[(2 * blk + sp) * 64 + l] = pk;
#pragma unroll
          for (int j = 0; j < 4; ++j) cs += bflo(pk[j]) + bfhi(pk[j]);
        }
      }
    }
    cs += xor32(cs);
    if (hh == 0) red[w][r] = cs;
    __syncthreads();
    if (tid < 32)
      ((float*)(ws + OFF_CSQKV))[(h * 3 + p) * 32 + tid] = red[0][tid] + red[1][tid] + red[2][tid] + red[3][tid];
  } else if (b < 48 || (b >= 64 && b < 176)) {
    u32x4* dst;
    float* csdst = nullptr;
    if (b < 48) {
      const int h = b - 32;
      dst = (u32x4*)(ws + OFF_WQKV) + (size_t)(h * 3 + 2) * KSD * 64;
      csdst = (float*)(ws + OFF_CSQKV) + (h * 3 + 2) * 32;
      u32x2 v[14];
#pragma unroll
      for (int i = 0; i < 14; ++i) {
        const int idx = tid + 256 * i, row = idx >> 3, c8 = idx & 7;
        v[i] = *(const u32x2*)(wqkv + (size_t)row * (3 * D_) + 2 * D_ + DH_ * h + 4 * min(c8, 6));
        if (c8 == 7) v[i] = u32x2{0u, 0u};
      }
#pragma unroll
      for (int i = 0; i < 14; ++i) *(u32x2*)(img + (tid + 256 * i) * 8) = v[i];
    } else {
      const unsigned short* src;
      int ld;
      if (b < 120) {
        const int ft = b - 64;
        dst = (u32x4*)(ws + OFF_W1) + (size_t)ft * KSD * 64;
        csdst = (float*)(ws + OFF_CS1) + ft * 32;
        src = w1 + 32 * ft;
        ld = F_;
      } else {
        const int bb = b - 120, nt = bb >> 2, qd = bb & 3;
        dst = (u32x4*)(ws + OFF_W2) + (size_t)(nt * KSF + qd * 28) * 64;
        src = w2 + (size_t)qd * 448 * D_ + 32 * nt;
        ld = D_;
      }
      u32x4 v[7];
#pragma unroll
      for (int i = 0; i < 7; ++i) {
        const int idx = tid + 256 * i, row = idx >> 2, c = idx & 3;
        v[i] = *(const u32x4*)(src + (size_t)row * ld + 8 * c);
      }
#pragma unroll
      for (int i = 0; i < 7; ++i) *(u32x4*)(img + (tid + 256 * i) * 16) = v[i];
    }
    __syncthreads();
    float cs = 0.f;
#pragma unroll
    for (int i = 0; i < 7; ++i) {
      const int s = w * 7 + i;
      u32x4 pk = frag_from_image(img, 64, s, l, 0);
      dst[s * 64 + l] = pk;
#pragma unroll
      for (int j = 0; j < 4; ++j) cs += bflo(pk[j]) + bfhi(pk[j]);
    }
    if (csdst) {
      cs += xor32(cs);
      if (hh == 0) red[w][r] = cs;
      __syncthreads();
      if (tid < 32) csdst[tid] = red[0][tid] + red[1][tid] + red[2][tid] + red[3][tid];
    }
  } else if (b < 64) {
    const int h = b - 48;
    u32x4 v[7];
#pragma unroll
    for (int i = 0; i < 7; ++i) {
      const int idx = tid + 256 * i;
      v[i] = *(const u32x4*)(wo + (size_t)DH_ * h * D_ + min(idx, 28 * 56 - 1) * 8);
      if (idx >= 28 * 56) v[i] = u32x4{0u, 0u, 0u, 0u};
    }
#pragma unroll
    for (int i = 0; i < 7; ++i) *(u32x4*)(img + (tid + 256 * i) * 16) = v[i];
    __syncthreads();
    u32x4* dst = (u32x4*)(ws + OFF_WO) + (size_t)h * 28 * 64;
#pragma unroll
    for (int i = 0; i < 7; ++i) {
      const int fi = w * 7 + i, ks = fi / NNT, nt = fi % NNT;
      dst[fi * 64 + l] = frag_from_image(img, 896, ks, l, 64 * nt);
    }
  } else if (b < 240) {
    const int tb = b - 176;
    u32x4 v[7];
#pragma unroll
    for (int i = 0; i < 7; ++i) v[i] = *(const u32x4*)(x + (size_t)tb * 32 * D_ + (tid + 256 * i) * 8);
#pragma unroll
    for (int i = 0; i < 7; ++i) {
      const int idx = tid + 256 * i, row = idx / 56, c = idx % 56;
      *(u32x4*)(img + row * 912 + c * 16) = v[i];
    }
    __syncthreads();
    u32x4* dst = (u32x4*)(ws + OFF_XF) + (size_t)tb * KSD * 64;
    f32x2* st = (f32x2*)(ws + OFF_ST);
#pragma unroll
    for (int it = 0; it < 4; ++it) {
      const int nt = w + 4 * it;
      if (nt < NNT) {
        float s1 = 0.f, s2 = 0.f;
#pragma unroll
        for (int sp = 0; sp < 2; ++sp) {
          const int s = 2 * nt + sp;
          const char* p = img + r * 912 + 32 * s + 8 * hh;
          u32x2 lo = *(const u32x2*)p;
          u32x2 hi = *(const u32x2*)(p + 16);
          u32x4 pk = {lo[0], lo[1], hi[0], hi[1]};
          dst[s * 64 + l] = pk;
#pragma unroll
          for (int j = 0; j < 4; ++j) {
            float a = bflo(pk[j]), c = bfhi(pk[j]);
            s1 += a + c;
            s2 += a * a + c * c;
          }
        }
        s1 += xor32(s1);
        s2 += xor32(s2);
        if (hh == 0) st[nt * S_ + tb * 32 + r] = f32x2{s1, s2};
      }
    }
  }
}

DI void finish_stats(const f32x2 (&v)[NNT], float& mean, float& rstd) {
  float s1 = 0.f, s2 = 0.f;
#pragma unroll
  for (int nt = 0; nt < NNT; ++nt) {
    s1 += v[nt][0];
    s2 += v[nt][1];
  }
  mean = s1 * (1.f / D_);
  float var = fmaxf(s2 * (1.f / D_) - mean * mean, 0.f);
  rstd = rsqrtf(var + 1e-5f);
}

constexpr float LOG2E = 1.4426950408889634f;
DI float max3(float a, float b, float c) { return fmaxf(fmaxf(a, b), c); }
DI float min3(float a, float b, float c) { return fminf(fminf(a, b), c); }

DI void dual_softmax(const f32x16& acc, const f32x4 (&cs)[4], float rs2, float mr2, int hh,
                     f32x16& pos, f32x16& neg) {
  f32x16 y;
#pragma unroll
  for (int i = 0; i < 16; ++i) y[i] = __builtin_fmaf(acc[i], rs2, mr2 * cs[i >> 2][i & 3]);
  float mx = max3(y[0], y[1], y[2]), mn = min3(y[0], y[1], y[2]);
#pragma unroll
  for (int i = 3; i < 15; i += 2) {
    mx = max3(mx, y[i], y[i + 1]);
    mn = min3(mn, y[i], y[i + 1]);
  }
  mx = fmaxf(mx, y[15]);
  mn = fminf(mn, y[15]);
  mx = fmaxf(mx, xor32(mx));
  mn = fminf(mn, xor32(mn));
#pragma unroll
  for (int i = 0; i < 16; ++i) {
    pos[i] = __builtin_amdgcn_exp2f(y[i] - mx);
    neg[i] = __builtin_amdgcn_exp2f(mn - y[i]);
  }
  float sp = 0.f, sn = 0.f;
#pragma unroll
  for (int i = 0; i < 12; ++i) { sp += pos[i]; sn += neg[i]; }
  float sph = (pos[12] + pos[13]) + (pos[14] + pos[15]);
  float snh = (neg[12] + neg[13]) + (neg[14] + neg[15]);
  sp += hh ? 0.f : sph;
  sn += hh ? 0.f : snh;
  sp += xor32(sp);
  sn += xor32(sn);
  const float ip = __builtin_amdgcn_rcpf(sp), in = __builtin_amdgcn_rcpf(sn);
  const float iph = hh ? 0.f : ip, inh = hh ? 0.f : in;
#pragma unroll
  for (int i = 0; i < 16; ++i) {
    pos[i] *= (i < 12) ? ip : iph;
    neg[i] *= (i < 12) ? in : inh;
  }
}

constexpr int K1_W    = 0;
constexpr int K1_IMG  = K1_W + 84 * 1024;
constexpr int K1_RED  = K1_IMG + 16 * 1024;
constexpr int K1_MS   = K1_RED + 32 * 1024;
constexpr int K1_LDS  = K1_MS + 4 * 256;

template <int NV>
DI void interleave_mfma(int n) {
#pragma unroll
  for (int i = 0; i < 28; ++i) {
    __builtin_amdgcn_sched_group_barrier(0x008, 1, 0);
    __builtin_amdgcn_sched_group_barrier(0x100, 1, 0);
    __builtin_amdgcn_sched_group_barrier(0x002, NV, 0);
  }
}

__global__ __launch_bounds__(256, 1) void k_qkv(const u32x4* __restrict__ xf, const f32x2* __restrict__ st,
                                                const u32x4* __restrict__ wqkv_t,
                                                const float* __restrict__ cs_qkv,
                                                u32x4* __restrict__ phiq, float* __restrict__ kvacc) {
  __shared__ __attribute__((aligned(16))) char smem[K1_LDS];
  const int bid = blockIdx.x, xcd = bid & 7, bi = bid >> 3;
  const int t = 2 * xcd + (bi >> 4), h = bi & 15;
  const int tid = threadIdx.x, w = __builtin_amdgcn_readfirstlane(tid >> 6), l = tid & 63, r = l & 31, hh = l >> 5;
  const int tb = t * 4 + w;
  STAMP_AT(1, 0);
  const u32x4* wsrc = wqkv_t + (size_t)(h * 84 + w * 7) * 64 + l;
  u32x4 wk[7], wv[7], wq[7], xr[KSD];
  f32x2 stv[NNT];
  f32x4 csk[4], csq[4];
#pragma unroll
  for (int i = 0; i < 7; ++i) wk[i] = wsrc[(KSD + i) * 64];
#pragma unroll
  for (int s = 0; s < KSD; ++s) xr[s] = xf[(size_t)(tb * KSD + s) * 64 + l];
#pragma unroll
  for (int i = 0; i < 7; ++i) wv[i] = wsrc[(2 * KSD + i) * 64];
#pragma unroll
  for (int nt = 0; nt < NNT; ++nt) stv[nt] = st[nt * S_ + tb * 32 + r];
  const float* cs = cs_qkv + h * 96;
#pragma unroll
  for (int g = 0; g < 4; ++g) csk[g] = *(const f32x4*)(cs + 32 + 8 * g + 4 * hh);
  const float csv = cs[64 + r];
#pragma unroll
  for (int i = 0; i < 7; ++i) wq[i] = wsrc[i * 64];
#pragma unroll
  for (int g = 0; g < 4; ++g) csq[g] = *(const f32x4*)(cs + 8 * g + 4 * hh);
  SCHED();

  u32x4* wl = (u32x4*)(smem + K1_W) + l;
  float* ms = (float*)(smem + K1_MS) + w * 64;
  char* img = smem + K1_IMG + w * 4096;
#pragma unroll
  for (int i = 0; i < 7; ++i) wl[(KSD + w * 7 + i) * 64] = wk[i];
  BARRIER();
  STAMP_AT(1, 1);
  f32x16 aq = zero16(), ak = zero16(), av = zero16();
  {
    constexpr int PD = 4;
    u32x4 f[KSD];
#pragma unroll
    for (int s = 0; s < PD; ++s) f[s] = wl[(KSD + s) * 64];
#pragma unroll
    for (int s = 0; s < KSD; ++s) {
      if (s + PD < KSD) f[s + PD] = wl[(KSD + s + PD) * 64];
      ak = MFMA32(asfrag(f[s]), asfrag(xr[s]), ak);
      SCHED();
    }
  }
#pragma unroll
  for (int i = 0; i < 7; ++i) wl[(2 * KSD + w * 7 + i) * 64] = wv[i];
  float mean, rstd;
  finish_stats(stv, mean, rstd);
  if (hh == 0) { ms[r] = mean; ms[32 + r] = rstd; }
  const float rs2 = rstd * LOG2E, mr2 = -mean * rs2;
  BARRIER();
  STAMP_AT(1, 2);
  {
#pragma unroll
    for (int s = 0; s < KSD; ++s) av = MFMA32(asfrag(xr[s]), asfrag(wl[(2 * KSD + s) * 64]), av);
    f32x16 pos, neg;
    dual_softmax(ak, csk, rs2, mr2, hh, pos, neg);
#pragma unroll
    for (int g = 0; g < 4; ++g) {
      const int ch = (2 * g + hh) ^ ((r >> 1) & 7);
      u32x2 a = {pack2(pos[4 * g], pos[4 * g + 1]), pack2(pos[4 * g + 2], pos[4 * g + 3])};
      u32x2 c = {pack2(neg[4 * g], neg[4 * g + 1]), pack2(neg[4 * g + 2], neg[4 * g + 3])};
      *(u32x2*)(img + r * 64 + ch * 8) = a;
      *(u32x2*)(img + 2048 + r * 64 + ch * 8) = c;
    }
#pragma unroll
    for (int i = 0; i < 7; ++i) wl[(w * 7 + i) * 64] = wq[i];
    interleave_mfma<8>(0);
  }
  BARRIER();
  STAMP_AT(1, 3);
  {
#pragma unroll
    for (int s = 0; s < KSD; ++s) aq = MFMA32(asfrag(wl[s * 64]), asfrag(xr[s]), aq);
    u32x4 vfr[2];
    {
      f32x16 vn;
#pragma unroll
      for (int g = 0; g < 4; ++g) {
        f32x4 m4 = *(const f32x4*)(ms + 8 * g + 4 * hh);
        f32x4 r4 = *(const f32x4*)(ms + 32 + 8 * g + 4 * hh);
#pragma unroll
        for (int i = 0; i < 4; ++i) {
          float v = __builtin_fmaf(-m4[i], csv, av[4 * g + i]) * r4[i];
          vn[4 * g + i] = (r == DH_) ? 1.f : v;
        }
      }
      vfr[0] = pack8(vn, 0);
      vfr[1] = pack8(vn, 1);
    }
    f32x16 kv0 = zero16(), kv1 = zero16();
    {
      const int i16 = l & 15, q4 = i16 >> 2, p4 = i16 & 3, blk = (l >> 4) & 1;
#pragma unroll
      for (int s = 0; s < 2; ++s) {
        const int t0 = 16 * s + 4 * hh + q4, t1 = t0 + 8;
        const int c0 = (4 * blk + p4) ^ ((t0 >> 1) & 7), c1 = (4 * blk + p4) ^ ((t1 >> 1) & 7);
        u32x4 pa = join(tr_read(img + t0 * 64 + c0 * 8), tr_read(img + t1 * 64 + c1 * 8));
        u32x4 pb = join(tr_read(img + 2048 + t0 * 64 + c0 * 8), tr_read(img + 2048 + t1 * 64 + c1 * 8));
        kv0 = MFMA32(asfrag(pa), asfrag(vfr[s]), kv0);
        kv1 = MFMA32(asfrag(pb), asfrag(vfr[s]), kv1);
      }
    }
    f32x4* red = (f32x4*)(smem + K1_RED) + w * 512 + l;
#pragma unroll
    for (int g = 0; g < 4; ++g) {
      red[g * 64] = f32x4{kv0[4 * g], kv0[4 * g + 1], kv0[4 * g + 2], kv0[4 * g + 3]};
      red[(4 + g) * 64] = f32x4{kv1[4 * g], kv1[4 * g + 1], kv1[4 * g + 2], kv1[4 * g + 3]};
    }
  }
  BARRIER();
  STAMP_AT(1, 4);
  {
    const float* red = (const float*)(smem + K1_RED);
    float* dst = kvacc + h * 2048;
#pragma unroll
    for (int i = 0; i < 8; ++i) {
      const int idx = w * 512 + i * 64 + l;
      float v = (red[idx] + red[2048 + idx]) + (red[4096 + idx] + red[6144 + idx]);
      atomicAdd(dst + idx, v);
    }
  }
  STAMP_AT(1, 5);
  {
    f32x16 pos, neg;
    dual_softmax(aq, csq, rs2, mr2, hh, pos, neg);
    u32x4* dst = phiq + (size_t)((tb * H_ + h) * 4) * 64 + l;
    dst[0] = pack8(pos, 0);
    dst[64] = pack8(pos, 1);
    dst[128] = pack8(neg, 0);
    dst[192] = pack8(neg, 1);
  }
  STAMP_AT(1, 6);
  STAMP_AT(1, 7);
}

template <bool FINAL, bool BIAS>
DI void resid_epilogue(char* smem, const f32x16 (&acc)[2][2], int w, int l, int tb, int nt,
                       const u32x4 (&xo)[2], const u32x2 (&bs)[4],
                       u32x4* __restrict__ xout, f32x2* __restrict__ stout,
                       unsigned short* __restrict__ out) {
  const int r = l & 31, hh = l >> 5;
  f32x4* red = (f32x4*)smem;
  f32x16 own;
#pragma unroll
  for (int tI = 0; tI < 4; ++tI) {
    const f32x16& a = acc[tI >> 1][tI & 1];
    if (tI != w) {
#pragma unroll
      for (int g = 0; g < 4; ++g)
        red[((w * 4 + tI) * 4 + g) * 64 + l] = f32x4{a[4 * g], a[4 * g + 1], a[4 * g + 2], a[4 * g + 3]};
    } else {
      own = a;
    }
  }
  BARRIER();
  f32x16 v;
#pragma unroll
  for (int g = 0; g < 4; ++g) {
    f32x4 s = f32x4{own[4 * g], own[4 * g + 1], own[4 * g + 2], own[4 * g + 3]};
#pragma unroll
    for (int o = 1; o < 4; ++o) s += red[((((w + o) & 3) * 4 + w) * 4 + g) * 64 + l];
    const unsigned x0 = xo[g >> 1][(g & 1) * 2], x1 = xo[g >> 1][(g & 1) * 2 + 1];
    if (BIAS) {
      s[0] += bflo(bs[g][0]); s[1] += bfhi(bs[g][0]); s[2] += bflo(bs[g][1]); s[3] += bfhi(bs[g][1]);
    }
    v[4 * g + 0] = s[0] + bflo(x0);
    v[4 * g + 1] = s[1] + bfhi(x0);
    v[4 * g + 2] = s[2] + bflo(x1);
    v[4 * g + 3] = s[3] + bfhi(x1);
  }
  u32x4 p0 = pack8(v, 0), p1 = pack8(v, 1);
  if (FINAL) {
    unsigned short* o = out + (size_t)(tb * 32 + r) * D_ + 32 * nt + 4 * hh;
    *(u32x2*)(o + 0) = u32x2{p0[0], p0[1]};
    *(u32x2*)(o + 8) = u32x2{p0[2], p0[3]};
    *(u32x2*)(o + 16) = u32x2{p1[0], p1[1]};
    *(u32x2*)(o + 24) = u32x2{p1[2], p1[3]};
  } else {
    xout[(size_t)(tb * KSD + 2 * nt) * 64 + l] = p0;
    xout[(size_t)(tb * KSD + 2 * nt + 1) * 64 + l] = p1;
    float s1 = 0.f, s2 = 0.f;
#pragma unroll
    for (int j = 0; j < 4; ++j) {
      float a = bflo(p0[j]), c = bfhi(p0[j]), d = bflo(p1[j]), e = bfhi(p1[j]);
      s1 += (a + c) + (d + e);
      s2 += (a * a + c * c) + (d * d + e * e);
    }
    s1 += xor32(s1);
    s2 += xor32(s2);
    if (hh == 0) stout[nt * S_ + tb * 32 + r] = f32x2{s1, s2};
  }
}

__global__ __launch_bounds__(256, 1) void k_attn_out(const u32x4* __restrict__ phiq,
                                                     const float* __restrict__ kvacc,
                                                     const u32x4* __restrict__ wo_t,
                                                     const u32x4* __restrict__ xin,
                                                     u32x4* __restrict__ xout, f32x2* __restrict__ stout) {
  __shared__ __attribute__((aligned(16))) char smem[64 * 1024];
  const int bid = blockIdx.x, xcd = bid & 7, bi = bid >> 3;
  const int tp = 4 * xcd + bi / 7, ns = bi % 7;
  const int tid = threadIdx.x, w = __builtin_amdgcn_readfirstlane(tid >> 6), l = tid & 63, r = l & 31;
  const int tbo = 2 * tp + (w >> 1), nto = 2 * ns + (w & 1);
  STAMP_AT(2, 0);
  f32x16 acc[2][2];
#pragma unroll
  for (int a = 0; a < 2; ++a)
#pragma unroll
    for (int b = 0; b < 2; ++b) acc[a][b] = zero16();
  u32x4 xo[2];
  u32x2 bs[4] = {};
  {
    f32x4 kva[4][8];
    u32x4 wf[4][2][2], pq[4][2][4];
    auto issue = [&](int hi) {
      const int hd = 4 * w + hi;
      const f32x4* kp = (const f32x4*)(kvacc + hd * 2048) + l;
#pragma unroll
      for (int i = 0; i < 8; ++i) kva[hi][i] = kp[i * 64];
#pragma unroll
      for (int a = 0; a < 2; ++a)
#pragma unroll
        for (int i = 0; i < 4; ++i) pq[hi][a][i] = phiq[(size_t)(((2 * tp + a) * H_ + hd) * 4 + i) * 64 + l];
#pragma unroll
      for (int sp = 0; sp < 2; ++sp)
#pragma unroll
        for (int nt = 0; nt < 2; ++nt) wf[hi][sp][nt] = wo_t[(size_t)((hd * 2 + sp) * NNT + 2 * ns + nt) * 64 + l];
    };
    issue(0); issue(1);
    SCHED();
#pragma unroll
    for (int hi = 0; hi < 4; ++hi) {
      u32x4 kvf[4];
#pragma unroll
      for (int i = 0; i < 4; ++i) {
        f32x4 a = kva[hi][2 * i], b = kva[hi][2 * i + 1];
        kvf[i] = u32x4{pack2(a[0], a[1]), pack2(a[2], a[3]), pack2(b[0], b[1]), pack2(b[2], b[3])};
      }
      SCHED();
      if (hi < 2) issue(hi + 2);
      if (hi == 2) {
        xo[0] = xin[(size_t)(tbo * KSD + 2 * nto) * 64 + l];
        xo[1] = xin[(size_t)(tbo * KSD + 2 * nto + 1) * 64 + l];
      }
      SCHED();
#pragma unroll
      for (int a = 0; a < 2; ++a) {
        f32x16 o = zero16();
#pragma unroll
        for (int i = 0; i < 4; ++i) o = MFMA32(asfrag(kvf[i]), asfrag(pq[hi][a][i]), o);
        const float den = __shfl(o[12], 32 + r, 64) + 1e-6f;
        const float inv = 1.f / den;
#pragma unroll
        for (int i = 0; i < 16; ++i) o[i] *= inv;
        u32x4 of0 = pack8(o, 0), of1 = pack8(o, 1);
#pragma unroll
        for (int nt = 0; nt < 2; ++nt) {
          acc[a][nt] = MFMA32(asfrag(wf[hi][0][nt]), asfrag(of0), acc[a][nt]);
          acc[a][nt] = MFMA32(asfrag(wf[hi][1][nt]), asfrag(of1), acc[a][nt]);
        }
      }
    }
  }
  STAMP_AT(2, 1);
  resid_epilogue<false, false>(smem, acc, w, l, tbo, nto, xo, bs, xout, stout, nullptr);
  STAMP_AT(2, 2);
}

constexpr int K3_X  = 0;
constexpr int K3_MS = 112 * 1024;
constexpr int K3_LDS = K3_MS + 1024;

__global__ __launch_bounds__(256, 1) void k_ffn1(const u32x4* __restrict__ xf, const f32x2* __restrict__ st,
                                                 const u32x4* __restrict__ w1_t, const float* __restrict__ cs1,
                                                 const unsigned short* __restrict__ b1,
                                                 u32x4* __restrict__ hf) {
  __shared__ __attribute__((aligned(16))) char smem[K3_LDS];
  const int bid = blockIdx.x, xcd = bid & 7, bi = bid >> 3;
  const int tt = 2 * xcd + bi / 14, fg = bi % 14;
  const int tid = threadIdx.x, w = __builtin_amdgcn_readfirstlane(tid >> 6), l = tid & 63, r = l & 31, hh = l >> 5;
  const int ft = 4 * fg + w;
  STAMP_AT(3, 0);
  const u32x4* xsrc = xf + (size_t)(tt * 4 * KSD + w * 7) * 64 + l;
  u32x4 xs[4][7], wr[KSD];
  f32x2 stv[NNT];
  f32x4 c4[4];
  u32x2 b4[4];
#pragma unroll
  for (int i = 0; i < 7; ++i) xs[0][i] = xsrc[i * 64];
#pragma unroll
  for (int nt = 0; nt < NNT; ++nt) stv[nt] = st[nt * S_ + tt * 128 + (tid & 127)];
#pragma unroll
  for (int s = 0; s < KSD; ++s) wr[s] = w1_t[(size_t)(ft * KSD + s) * 64 + l];
#pragma unroll
  for (int a = 1; a < 4; ++a)
#pragma unroll
    for (int i = 0; i < 7; ++i) xs[a][i] = xsrc[(a * KSD + i) * 64];
#pragma unroll
  for (int g = 0; g < 4; ++g) {
    c4[g] = *(const f32x4*)(cs1 + 32 * ft + 8 * g + 4 * hh);
    b4[g] = *(const u32x2*)(b1 + 32 * ft + 8 * g + 4 * hh);
  }
  SCHED();
  u32x4* xl = (u32x4*)(smem + K3_X) + l;
  float* ms = (float*)(smem + K3_MS);
#pragma unroll
  for (int i = 0; i < 7; ++i) xl[(w * 7 + i) * 64] = xs[0][i];
  {
    float mean, rstd;
    finish_stats(stv, mean, rstd);
    if (tid < 128) {
      ms[tid] = -mean * rstd;
      ms[128 + tid] = rstd;
    }
  }
  BARRIER();
  STAMP_AT(3, 1);
  {
    constexpr int PD = 4;
    f32x16 acc[4];
    f32x16 hv, tb_;
    float mr = 0.f, rstd = 0.f;
#pragma unroll
    for (int a = 0; a < 5; ++a) {
      u32x4 xb[KSD];
      if (a < 4) {
        acc[a] = zero16();
#pragma unroll
        for (int s = 0; s < PD; ++s) xb[s] = xl[(a * KSD + s) * 64];
      }
      if (a > 0) {
        rstd = ms[128 + (a - 1) * 32 + r];
        mr = ms[(a - 1) * 32 + r];
      }
#pragma unroll
      for (int s = 0; s < KSD; ++s) {
        if (a < 4) {
          if (s + PD < KSD) xb[s + PD] = xl[(a * KSD + s + PD) * 64];
          acc[a] = MFMA32(asfrag(wr[s]), asfrag(xb[s]), acc[a]);
        }
        if (a > 0 && (s >= 3 && s < 19)) {
          const int i = s - 3;
          const float bb = (i & 1) ? bfhi(b4[i >> 2][(i >> 1) & 1]) : bflo(b4[i >> 2][(i >> 1) & 1]);
          const float y = __builtin_fmaf(acc[a - 1][i], rstd, __builtin_fmaf(mr, c4[i >> 2][i & 3], bb));
          const float c0 = -2.f * 0.7978845608028654f * LOG2E;
          const float z = y * __builtin_fmaf(y * y, c0 * 0.044715f, c0);
          const float e = __builtin_amdgcn_exp2f(z);
          hv[i] = y * __builtin_amdgcn_rcpf(1.f + e);
        }
        if (a > 0 && s == 19) {
          u32x4* dst = hf + (size_t)((tt * 4 + a - 1) * KSF + 2 * ft) * 64 + l;
          dst[0] = pack8(hv, 0);
          dst[64] = pack8(hv, 1);
        }
        if (a < 3 && s >= 20 && s < 27) xl[((a + 1) * KSD + w * 7 + (s - 20)) * 64] = xs[a + 1][s - 20];
        if (a < 4) SCHED();
      }
      if (a < 3) BARRIER();
      STAMP_AT(3, 2 + a);
    }
  }
}

template <bool FINAL>
__global__ __launch_bounds__(256, 1) void k_ffn2(const u32x4* __restrict__ hf, const u32x4* __restrict__ w2_t,
                                                 const unsigned short* __restrict__ b2,
                                                 const u32x4* __restrict__ xin, u32x4* __restrict__ xout,
                                                 f32x2* __restrict__ stout, unsigned short* __restrict__ out) {
  __shared__ __attribute__((aligned(16))) char smem[64 * 1024];
  const int bid = blockIdx.x, xcd = bid & 7, bi = bid >> 3;
  const int tp = 4 * xcd + bi / 7, ns = bi % 7;
  const int tid = threadIdx.x, w = __builtin_amdgcn_readfirstlane(tid >> 6), l = tid & 63, hh = l >> 5;
  const int tbo = 2 * tp + (w >> 1), nto = 2 * ns + (w & 1);
  STAMP_AT(4, 0);
  f32x16 acc[2][2];
#pragma unroll
  for (int a = 0; a < 2; ++a)
#pragma unroll
    for (int b = 0; b < 2; ++b) acc[a][b] = zero16();
  const u32x4* h0 = hf + (size_t)((2 * tp) * KSF + 28 * w) * 64 + l;
  const u32x4* h1 = h0 + (size_t)KSF * 64;
  const u32x4* w0 = w2_t + (size_t)((2 * ns) * KSF + 28 * w) * 64 + l;
  const u32x4* w1p = w0 + (size_t)KSF * 64;
  u32x4 xo[2];
  u32x2 bs[4];
  {
    constexpr int PD = 12;
    u32x4 a0[28], a1[28], b0[28], b1[28];
#pragma unroll
    for (int s = 0; s < PD; ++s) {
      a0[s] = w0[s * 64]; b0[s] = h0[s * 64]; a1[s] = w1p[s * 64]; b1[s] = h1[s * 64];
    }
#pragma unroll
    for (int s = 0; s < 28; ++s) {
      if (s + PD < 28) {
        a0[s + PD] = w0[(s + PD) * 64]; b0[s + PD] = h0[(s + PD) * 64];
        a1[s + PD] = w1p[(s + PD) * 64]; b1[s + PD] = h1[(s + PD) * 64];
      }
      if (s + PD == 28) {
        xo[0] = xin[(size_t)(tbo * KSD + 2 * nto) * 64 + l];
        xo[1] = xin[(size_t)(tbo * KSD + 2 * nto + 1) * 64 + l];
#pragma unroll
        for (int g = 0; g < 4; ++g) bs[g] = *(const u32x2*)(b2 + 32 * nto + 8 * g + 4 * hh);
      }
      SCHED();
      acc[0][0] = MFMA32(asfrag(a0[s]), asfrag(b0[s]), acc[0][0]);
      acc[0][1] = MFMA32(asfrag(a1[s]), asfrag(b0[s]), acc[0][1]);
      acc[1][0] = MFMA32(asfrag(a0[s]), asfrag(b1[s]), acc[1][0]);
      acc[1][1] = MFMA32(asfrag(a1[s]), asfrag(b1[s]), acc[1][1]);
      SCHED();
    }
  }
  STAMP_AT(4, 1);
  resid_epilogue<FINAL, true>(smem, acc, w, l, tbo, nto, xo, bs, xout, stout, out);
  STAMP_AT(4, 2);
}

}

extern "C" void kernel_launch(void* const* d_in, const int* in_sizes, int n_in, void* d_out, int out_size,
                              void* d_ws, size_t ws_size, hipStream_t stream) {
  const unsigned short* x = (const unsigned short*)d_in[0];
  const unsigned short* wqkv = (const unsigned short*)d_in[1];
  const unsigned short* wphi = (const unsigned short*)d_in[2];
  const unsigned short* wo = (const unsigned short*)d_in[3];
  const unsigned short* w1 = (const unsigned short*)d_in[5];
  const unsigned short* b1 = (const unsigned short*)d_in[6];
  const unsigned short* w2 = (const unsigned short*)d_in[7];
  const unsigned short* b2 = (const unsigned short*)d_in[8];
  char* ws = (char*)d_ws;
  unsigned short* out = (unsigned short*)d_out;

  auto XF = [&](int i) { return (u32x4*)(ws + OFF_XF + (size_t)i * XF_BYTES); };
  auto ST = [&](int i) { return (f32x2*)(ws + OFF_ST + (size_t)i * ST_BYTES); };
  const u32x4* wqkv_t = (const u32x4*)(ws + OFF_WQKV);
  const float* cs_qkv = (const float*)(ws + OFF_CSQKV);
  const u32x4* wo_t = (const u32x4*)(ws + OFF_WO);
  const u32x4* w1_t = (const u32x4*)(ws + OFF_W1);
  const float* cs1 = (const float*)(ws + OFF_CS1);
  const u32x4* w2_t = (const u32x4*)(ws + OFF_W2);
  u32x4* phiq = (u32x4*)(ws + OFF_PHIQ);
  float* kv = (float*)(ws + OFF_KV);
  u32x4* hf = (u32x4*)(ws + OFF_HF);

  k_prep<<<256, 256, 0, stream>>>(x, wqkv, wphi, wo, w1, w2, ws);
  for (int layer = 0; layer < 2; ++layer) {
    const int i0 = 2 * layer;
    float* kvl = kv + layer * H_ * 2048;
    k_qkv<<<256, 256, 0, stream>>>(XF(i0), ST(i0), wqkv_t, cs_qkv, phiq, kvl);
    k_attn_out<<<224, 256, 0, stream>>>(phiq, kvl, wo_t, XF(i0), XF(i0 + 1), ST(i0 + 1));
    k_ffn1<<<224, 256, 0, stream>>>(XF(i0 + 1), ST(i0 + 1), w1_t, cs1, b1, hf);
    if (layer == 0)
      k_ffn2<false><<<224, 256, 0, stream>>>(hf, w2_t, b2, XF(i0 + 1), XF(i0 + 2), ST(i0 + 2), nullptr);
    else
      k_ffn2<true><<<224, 256, 0, stream>>>(hf, w2_t, b2, XF(i0 + 1), nullptr, nullptr, out);
  }
}
```

```cpp
#include <hip/hip_runtime.h>
#include <hip/hip_bf16.h>
#include <stdint.h>

typedef __attribute__((ext_vector_type(8))) short bf16x8;
typedef __attribute__((ext_vector_type(4))) short s16x4;
typedef __attribute__((ext_vector_type(16))) float f32x16;
typedef __attribute__((ext_vector_type(4))) float f32x4;
typedef __attribute__((ext_vector_type(2))) float f32x2;
typedef __attribute__((ext_vector_type(4))) unsigned u32x4;
typedef __attribute__((ext_vector_type(2))) unsigned u32x2;
typedef __attribute__((ext_vector_type(2))) __bf16 bf16x2_t;

#define DI __device__ __forceinline__
#define SCHED() __builtin_amdgcn_sched_barrier(0)
#define MFMA32(a, b, c) __builtin_amdgcn_mfma_f32_32x32x16_bf16((a), (b), (c), 0, 0, 0)

#define STAMP_AT(kid, i)

namespace {

constexpr int S_ = 2048, D_ = 448, H_ = 16, DH_ = 28, F_ = 1792;
constexpr int NTB = S_ / 32;
constexpr int KSD = D_ / 16;
constexpr int KSF = F_ / 16;
constexpr int NNT = D_ / 32;
constexpr int NFT = F_ / 32;

constexpr size_t XF_BYTES   = (size_t)NTB * KSD * 1024;
constexpr size_t ST_BYTES   = (size_t)S_ * 8;
constexpr size_t OFF_XF     = 0;
constexpr size_t OFF_ST     = OFF_XF + 4 * XF_BYTES;
constexpr size_t OFF_WQKV   = OFF_ST + 4 * ST_BYTES;
constexpr size_t OFF_CSQKV  = OFF_WQKV + (size_t)H_ * 3 * KSD * 1024;
constexpr size_t OFF_WO     = OFF_CSQKV + (size_t)H_ * 3 * 32 * 4;
constexpr size_t OFF_W1     = OFF_WO + (size_t)H_ * 2 * NNT * 1024;
constexpr size_t OFF_CS1    = OFF_W1 + (size_t)NFT * KSD * 1024;
constexpr size_t OFF_W2     = OFF_CS1 + (size_t)F_ * 4;
constexpr size_t OFF_PHIQ   = OFF_W2 + (size_t)NNT * KSF * 1024;
constexpr size_t OFF_KV     = OFF_PHIQ + (size_t)NTB * H_ * 4 * 1024;
constexpr size_t OFF_HF     = OFF_KV + (size_t)2 * H_ * 2048 * 4;
constexpr size_t WS_TOTAL   = OFF_HF + (size_t)NTB * KSF * 1024;

DI unsigned pack2(float a, float b) {
  f32x2 v = {a, b};
  bf16x2_t r = __builtin_convertvector(v, bf16x2_t);
  return __builtin_bit_cast(unsigned, r);
}
DI float bflo(unsigned u) { return __uint_as_float(u << 16); }
DI float bfhi(unsigned u) { return __uint_as_float(u & 0xffff0000u); }
DI float bf1(unsigned short u) { return __uint_as_float(((unsigned)u) << 16); }
DI bf16x8 asfrag(u32x4 v) { return __builtin_bit_cast(bf16x8, v); }
DI u32x4 pack8(const f32x16& x, int sp) {
  u32x4 p;
  p[0] = pack2(x[8 * sp + 0], x[8 * sp + 1]);
  p[1] = pack2(x[8 * sp + 2], x[8 * sp + 3]);
  p[2] = pack2(x[8 * sp + 4], x[8 * sp + 5]);
  p[3] = pack2(x[8 * sp + 6], x[8 * sp + 7]);
  return p;
}
DI float xor32(float v) { return __shfl_xor(v, 32, 64); }
DI void halves(float v, float& lo, float& hi) {
  auto r = __builtin_amdgcn_permlane32_swap(__float_as_uint(v), __float_as_uint(v), false, false);
  lo = __uint_as_float(r[0]);
  hi = __uint_as_float(r[1]);
}
DI float sum32(float v) { float a, b; halves(v, a, b); return a + b; }
DI float max32(float v) { float a, b; halves(v, a, b); return fmaxf(a, b); }
DI float min32(float v) { float a, b; halves(v, a, b); return fminf(a, b); }
DI float opq(float v) { asm("" : "+v"(v)); return v; }
DI f32x16 zero16() {
  f32x16 z;
#pragma unroll
  for (int i = 0; i < 16; ++i) z[i] = 0.f;
  return z;
}
DI void glds16(const void* g, void* l) {
  __builtin_amdgcn_global_load_lds((const __attribute__((address_space(1))) unsigned*)g,
                                   (__attribute__((address_space(3))) unsigned*)l, 16, 0, 0);
}

template <int I> struct IC { static constexpr int v = I; };
template <int B, int E, class Fn>
DI void sfor(Fn&& f) {
  if constexpr (B < E) {
    f(IC<B>{});
    sfor<B + 1, E>(f);
  }
}

#define BARRIER() asm volatile("s_waitcnt lgkmcnt(0)\n\ts_barrier" ::: "memory")

typedef __attribute__((address_space(3))) s16x4* lds_s16x4p;
DI s16x4 tr_read(const char* p) { return __builtin_amdgcn_ds_read_tr16_b64_v4i16((lds_s16x4p)p); }
DI u32x4 join(s16x4 a, s16x4 b) {
  bf16x8 v = __builtin_shufflevector(a, b, 0, 1, 2, 3, 4, 5, 6, 7);
  return __builtin_bit_cast(u32x4, v);
}

DI u32x4 frag_from_image(const char* img, int row_stride, int s, int l, int cb) {
  const int i16 = l & 15, q4 = i16 >> 2, p4 = i16 & 3, blk = (l >> 4) & 1, hh = l >> 5;
  const char* p = img + (16 * s + 4 * hh + q4) * row_stride + cb + (4 * blk + p4) * 8;
  return join(tr_read(p), tr_read(p + 8 * row_stride));
}

__global__ __launch_bounds__(256) void k_prep(const unsigned short* __restrict__ x,
                                              const unsigned short* __restrict__ wqkv,
                                              const unsigned short* __restrict__ wphi,
                                              const unsigned short* __restrict__ wo,
                                              const unsigned short* __restrict__ w1,
                                              const unsigned short* __restrict__ w2,
                                              char* __restrict__ ws) {
  __shared__ __attribute__((aligned(16))) char img[32 * 912 + 1024];
  float (*red)[32] = (float (*)[32])(img + 32 * 912);
  const int b = blockIdx.x, tid = threadIdx.x, w = tid >> 6, l = tid & 63, r = l & 31, hh = l >> 5;
  ((float*)(ws + OFF_KV))[b * 256 + tid] = 0.f;

  if (b < 32) {
    const int h = b >> 1, p = b & 1;
    const int cc = r < DH_ ? r : r - DH_;
    u32x4 bfr[2];
    u32x2 lo[4][2], hi[4][2];
#pragma unroll
    for (int it = 0; it < 4; ++it) {
      const int blk = min(w + 4 * it, 13);
      const unsigned short* row = wqkv + (size_t)(32 * blk + r) * (3 * D_) + p * D_ + DH_ * h;
#pragma unroll
      for (int s2 = 0; s2 < 2; ++s2) {
        const int e0 = 16 * s2 + 8 * hh;
        lo[it][s2] = *(const u32x2*)(row + e0);
        hi[it][s2] = *(const u32x2*)(row + min(e0 + 4, 24));
        if (e0 + 4 >= DH_) hi[it][s2] = u32x2{0u, 0u};
      }
    }
#pragma unroll
    for (int s2 = 0; s2 < 2; ++s2) {
      unsigned short v[8];
#pragma unroll
      for (int j = 0; j < 8; ++j) {
        const int e = 16 * s2 + 8 * hh + j;
        v[j] = wphi[(h * DH_ + min(e, DH_ - 1)) * DH_ + cc];
        if (e >= DH_) v[j] = 0;
      }
#pragma unroll
      for (int j = 0; j < 4; ++j) bfr[s2][j] = (unsigned)v[2 * j] | ((unsigned)v[2 * j + 1] << 16);
    }
    float cs = 0.f;
    u32x4* dst = (u32x4*)(ws + OFF_WQKV) + (size_t)(h * 3 + p) * KSD * 64;
#pragma unroll
    for (int it = 0; it < 4; ++it) {
      const int blk = w + 4 * it;
      f32x16 acc = zero16();
#pragma unroll
      for (int s2 = 0; s2 < 2; ++s2) {
        u32x4 a = {lo[it][s2][0], lo[it][s2][1], hi[it][s2][0], hi[it][s2][1]};
        acc = MFMA32(asfrag(a), asfrag(bfr[s2]), acc);
      }
      if (blk < 14) {
#pragma unroll
        for (int sp = 0; sp < 2; ++sp) {
          u32x4 pk = pack8(acc, sp);
          dst[(2 * blk + sp) * 64 + l] = pk;
#pragma unroll
          for (int j = 0; j < 4; ++j) cs += bflo(pk[j]) + bfhi(pk[j]);
        }
      }
    }
    cs += xor32(cs);
    if (hh == 0) red[w][r] = cs;
    __syncthreads();
    if (tid < 32)
      ((float*)(ws + OFF_CSQKV))[(h * 3 + p) * 32 + tid] = red[0][tid] + red[1][tid] + red[2][tid] + red[3][tid];
  } else if (b < 48 || (b >= 64 && b < 176)) {
    u32x4* dst;
    float* csdst = nullptr;
    if (b < 48) {
      const int h = b - 32;
      dst = (u32x4*)(ws + OFF_WQKV) + (size_t)(h * 3 + 2) * KSD * 64;
      csdst = (float*)(ws + OFF_CSQKV) + (h * 3 + 2) * 32;
      u32x2 v[14];
#pragma unroll
      for (int i = 0; i < 14; ++i) {
        const int idx = tid + 256 * i, row = idx >> 3, c8 = idx & 7;
        v[i] = *(const u32x2*)(wqkv + (size_t)row * (3 * D_) + 2 * D_ + DH_ * h + 4 * min(c8, 6));
        if (c8 == 7) v[i] = u32x2{0u, 0u};
      }
#pragma unroll
      for (int i = 0; i < 14; ++i) *(u32x2*)(img + (tid + 256 * i) * 8) = v[i];
    } else {
      const unsigned short* src;
      int ld;
      if (b < 120) {
        const int ft = b - 64;
        dst = (u32x4*)(ws + OFF_W1) + (size_t)ft * KSD * 64;
        csdst = (float*)(ws + OFF_CS1) + ft * 32;
        src = w1 + 32 * ft;
        ld = F_;
      } else {
        const int bb = b - 120, nt = bb >> 2, qd = bb & 3;
        dst = (u32x4*)(ws + OFF_W2) + (size_t)(nt * KSF + qd * 28) * 64;
        src = w2 + (size_t)qd * 448 * D_ + 32 * nt;
        ld = D_;
      }
      u32x4 v[7];
#pragma unroll
      for (int i = 0; i < 7; ++i) {
        const int idx = tid + 256 * i, row = idx >> 2, c = idx & 3;
        v[i] = *(const u32x4*)(src + (size_t)row * ld + 8 * c);
      }
#pragma unroll
      for (int i = 0; i < 7; ++i) *(u32x4*)(img + (tid + 256 * i) * 16) = v[i];
    }
    __syncthreads();
    float cs = 0.f;
#pragma unroll
    for (int i = 0; i < 7; ++i) {
      const int s = w * 7 + i;
      u32x4 pk = frag_from_image(img, 64, s, l, 0);
      dst[s * 64 + l] = pk;
#pragma unroll
      for (int j = 0; j < 4; ++j) cs += bflo(pk[j]) + bfhi(pk[j]);
    }
    if (csdst) {
      cs += xor32(cs);
      if (hh == 0) red[w][r] = cs;
      __syncthreads();
      if (tid < 32) csdst[tid] = red[0][tid] + red[1][tid] + red[2][tid] + red[3][tid];
    }
  } else if (b < 64) {
    const int h = b - 48;
    u32x4 v[7];
#pragma unroll
    for (int i = 0; i < 7; ++i) {
      const int idx = tid + 256 * i;
      v[i] = *(const u32x4*)(wo + (size_t)DH_ * h * D_ + min(idx, 28 * 56 - 1) * 8);
      if (idx >= 28 * 56) v[i] = u32x4{0u, 0u, 0u, 0u};
    }
#pragma unroll
    for (int i = 0; i < 7; ++i) *(u32x4*)(img + (tid + 256 * i) * 16) = v[i];
    __syncthreads();
    u32x4* dst = (u32x4*)(ws + OFF_WO) + (size_t)h * 28 * 64;
#pragma unroll
    for (int i = 0; i < 7; ++i) {
      const int fi = w * 7 + i, ks = fi / NNT, nt = fi % NNT;
      dst[fi * 64 + l] = frag_from_image(img, 896, ks, l, 64 * nt);
    }
  } else if (b < 240) {
    const int tb = b - 176;
    u32x4 v[7];
#pragma unroll
    for (int i = 0; i < 7; ++i) v[i] = *(const u32x4*)(x + (size_t)tb * 32 * D_ + (tid + 256 * i) * 8);
#pragma unroll
    for (int i = 0; i < 7; ++i) {
      const int idx = tid + 256 * i, row = idx / 56, c = idx % 56;
      *(u32x4*)(img + row * 912 + c * 16) = v[i];
    }
    __syncthreads();
    u32x4* dst = (u32x4*)(ws + OFF_XF) + (size_t)tb * KSD * 64;
    float s1 = 0.f, s2 = 0.f;
#pragma unroll
    for (int i = 0; i < 7; ++i) {
      const int s = w * 7 + i;
      const char* p = img + r * 912 + 32 * s + 8 * hh;
      u32x2 lo = *(const u32x2*)p;
      u32x2 hi = *(const u32x2*)(p + 16);
      u32x4 pk = {lo[0], lo[1], hi[0], hi[1]};
      dst[s * 64 + l] = pk;
#pragma unroll
      for (int j = 0; j < 4; ++j) {
        float a = bflo(pk[j]), c = bfhi(pk[j]);
        s1 += a + c;
        s2 += a * a + c * c;
      }
    }
    s1 = sum32(s1);
    s2 = sum32(s2);
    float* red2 = (float*)(img + 32 * 912);
    red2[w * 64 + l] = hh ? s2 : s1;
    __syncthreads();
    if (tid < 64) {
      const float v = red2[tid] + red2[64 + tid] + red2[128 + tid] + red2[192 + tid];
      ((float*)(ws + OFF_ST))[(tb * 32 + r) * 2 + hh] = v;
    }
  } else {
    float* z = (float*)(ws + OFF_ST + ST_BYTES) + (b - 240) * 768 + tid;
    z[0] = 0.f; z[256] = 0.f; z[512] = 0.f;
  }
}

DI void finish_stats(f32x2 v, float& mean, float& rstd) {
  mean = v[0] * (1.f / D_);
  float var = fmaxf(v[1] * (1.f / D_) - mean * mean, 0.f);
  rstd = rsqrtf(var + 1e-5f);
}

constexpr float LOG2E = 1.4426950408889634f;
DI float max3(float a, float b, float c) { return fmaxf(fmaxf(a, b), c); }
DI float min3(float a, float b, float c) { return fminf(fminf(a, b), c); }

DI void dual_softmax(const f32x16& acc, const f32x4 (&cs)[4], float rs2, float mr2, int hh,
                     f32x16& pos, f32x16& neg) {
  f32x16 y;
#pragma unroll
  for (int i = 0; i < 16; ++i) y[i] = __builtin_fmaf(acc[i], rs2, mr2 * cs[i >> 2][i & 3]);
  float mx = max3(y[0], y[1], y[2]), mn = min3(y[0], y[1], y[2]);
#pragma unroll
  for (int i = 3; i < 15; i += 2) {
    mx = max3(mx, y[i], y[i + 1]);
    mn = min3(mn, y[i], y[i + 1]);
  }
  mx = fmaxf(mx, y[15]);
  mn = fminf(mn, y[15]);
  mx = max32(mx);
  mn = min32(mn);
#pragma unroll
  for (int i = 0; i < 16; ++i) {
    pos[i] = __builtin_amdgcn_exp2f(y[i] - mx);
    neg[i] = __builtin_amdgcn_exp2f(mn - y[i]);
  }
  float sp = 0.f, sn = 0.f;
#pragma unroll
  for (int i = 0; i < 12; ++i) { sp += pos[i]; sn += neg[i]; }
  float sph = (pos[12] + pos[13]) + (pos[14] + pos[15]);
  float snh = (neg[12] + neg[13]) + (neg[14] + neg[15]);
  sp += hh ? 0.f : sph;
  sn += hh ? 0.f : snh;
  sp = sum32(sp);
  sn = sum32(sn);
  const float ip = __builtin_amdgcn_rcpf(sp), in = __builtin_amdgcn_rcpf(sn);
  const float iph = hh ? 0.f : ip, inh = hh ? 0.f : in;
#pragma unroll
  for (int i = 0; i < 16; ++i) {
    pos[i] *= (i < 12) ? ip : iph;
    neg[i] *= (i < 12) ? in : inh;
  }
}

constexpr int K1_W    = 0;
constexpr int K1_IMG  = K1_W + 84 * 1024;
constexpr int K1_RED  = K1_IMG + 16 * 1024;
constexpr int K1_MS   = K1_RED + 32 * 1024;
constexpr int K1_LDS  = K1_MS + 4 * 256;

__global__ __launch_bounds__(256, 1) void k_qkv(const u32x4* __restrict__ xf, const f32x2* __restrict__ st,
                                                const u32x4* __restrict__ wqkv_t,
                                                const float* __restrict__ cs_qkv,
                                                u32x4* __restrict__ phiq, float* __restrict__ kvacc) {
  __shared__ __attribute__((aligned(16))) char smem[K1_LDS];
  const int bid = blockIdx.x, xcd = bid & 7, bi = bid >> 3;
  const int t = 2 * xcd + (bi >> 4), h = bi & 15;
  const int tid = threadIdx.x, w = __builtin_amdgcn_readfirstlane(tid >> 6), l = tid & 63, r = l & 31, hh = l >> 5;
  const int tb = t * 4 + w;
  STAMP_AT(1, 0);
  const u32x4* wsrc = wqkv_t + (size_t)(h * 84 + w * 7) * 64 + l;
  const u32x4* xsrc = xf + (size_t)tb * KSD * 64 + l;
  const float* cs = cs_qkv + h * 96;
  u32x4 wk[7], wv[7], wq[7], xr[KSD];
  f32x4 csk[4], csq[4];
  float csv = 0.f;
#pragma unroll
  for (int i = 0; i < 7; ++i) wk[i] = wsrc[(KSD + i) * 64];
  const f32x2 stv = st[tb * 32 + r];
  SCHED();
#pragma unroll
  for (int s = 0; s < 16; ++s) { xr[s] = xsrc[s * 64]; SCHED(); }

  u32x4* wl = (u32x4*)(smem + K1_W) + l;
  float* ms = (float*)(smem + K1_MS) + w * 64;
  char* img = smem + K1_IMG + w * 4096;
#pragma unroll
  for (int i = 0; i < 7; ++i) wl[(KSD + w * 7 + i) * 64] = wk[i];
  float mean, rstd;
  finish_stats(stv, mean, rstd);
  if (hh == 0) { ms[r] = mean; ms[32 + r] = rstd; }
  const float rs2 = rstd * LOG2E, mr2 = -mean * rs2;
  BARRIER();
  STAMP_AT(1, 1);
  constexpr int PD = 3;
  f32x16 aq = zero16(), ak = zero16(), av = zero16();
  {
    u32x4 f[KSD];
#pragma unroll
    for (int s = 0; s < PD; ++s) f[s] = wl[(KSD + s) * 64];
#pragma unroll
    for (int s = 0; s < KSD; ++s) {
      if (s < 12) xr[16 + s] = xsrc[(16 + s) * 64];
      else if (s < 19) wv[s - 12] = wsrc[(2 * KSD + s - 12) * 64];
      else if (s < 23) csk[s - 19] = *(const f32x4*)(cs + 32 + 8 * (s - 19) + 4 * hh);
      else if (s == 23) csv = cs[64 + r];
      else wq[s - 24] = wsrc[(s - 24) * 64];
      if (s + PD < KSD) f[s + PD] = wl[(KSD + s + PD) * 64];
      ak = MFMA32(asfrag(f[s]), asfrag(xr[s]), ak);
      SCHED();
    }
  }
#pragma unroll
  for (int i = 0; i < 7; ++i) wl[(2 * KSD + w * 7 + i) * 64] = wv[i];
  BARRIER();
  STAMP_AT(1, 2);
  {
    u32x4 f[KSD];
    f32x16 y, pos, neg;
    float mx = -3.0e38f, mn = 3.0e38f, sp = 0.f, sn = 0.f, ip = 0.f, in = 0.f;
#pragma unroll
    for (int s = 0; s < PD; ++s) f[s] = wl[(2 * KSD + s) * 64];
#pragma unroll
    for (int s = 0; s < KSD; ++s) {
      if (s < 3) wq[4 + s] = wsrc[(4 + s) * 64];
      else if (s < 7) csq[s - 3] = *(const f32x4*)(cs + 8 * (s - 3) + 4 * hh);
      if (s + PD < KSD) f[s + PD] = wl[(2 * KSD + s + PD) * 64];
      av = MFMA32(asfrag(xr[s]), asfrag(f[s]), av);
      const int q = s - 1;
      if (q >= 0 && q < 4) {
#pragma unroll
        for (int i = 4 * q; i < 4 * q + 4; ++i) y[i] = opq(__builtin_fmaf(ak[i], rs2, opq(mr2 * csk[q][i & 3])));
      } else if (q >= 4 && q < 8) {
        const int g = q - 4;
        mx = max3(max3(mx, y[4 * g], y[4 * g + 1]), y[4 * g + 2], y[4 * g + 3]);
        mn = min3(min3(mn, y[4 * g], y[4 * g + 1]), y[4 * g + 2], y[4 * g + 3]);
      } else if (q == 8) {
        mx = max32(mx);
        mn = min32(mn);
      } else if (q >= 9 && q < 13) {
#pragma unroll
        for (int i = 4 * (q - 9); i < 4 * (q - 9) + 4; ++i) { pos[i] = opq(y[i] - mx); neg[i] = opq(mn - y[i]); }
      } else if (q >= 13 && q < 17) {
#pragma unroll
        for (int i = 4 * (q - 13); i < 4 * (q - 13) + 4; ++i) {
          pos[i] = __builtin_amdgcn_exp2f(pos[i]);
          neg[i] = __builtin_amdgcn_exp2f(neg[i]);
        }
      } else if (q >= 17 && q < 21) {
        const int g = q - 17;
        float a = opq(opq(pos[4 * g] + pos[4 * g + 1]) + opq(pos[4 * g + 2] + pos[4 * g + 3]));
        float c = opq(opq(neg[4 * g] + neg[4 * g + 1]) + opq(neg[4 * g + 2] + neg[4 * g + 3]));
        if (g == 3) { a = hh ? 0.f : a; c = hh ? 0.f : c; }
        sp = opq(sp + a);
        sn = opq(sn + c);
      } else if (q == 21) {
        ip = __builtin_amdgcn_rcpf(sum32(sp));
        in = __builtin_amdgcn_rcpf(sum32(sn));
      } else if (q >= 22 && q < 26) {
        const int g = q - 22;
        const float fp = (g == 3 && hh) ? 0.f : ip, fn = (g == 3 && hh) ? 0.f : in;
        const int ch = (2 * g + hh) ^ ((r >> 1) & 7);
        u32x2 a = {pack2(opq(pos[4 * g] * fp), opq(pos[4 * g + 1] * fp)), pack2(opq(pos[4 * g + 2] * fp), opq(pos[4 * g + 3] * fp))};
        u32x2 c = {pack2(opq(neg[4 * g] * fn), opq(neg[4 * g + 1] * fn)), pack2(opq(neg[4 * g + 2] * fn), opq(neg[4 * g + 3] * fn))};
        *(u32x2*)(img + r * 64 + ch * 8) = a;
        *(u32x2*)(img + 2048 + r * 64 + ch * 8) = c;
      }
      if (s >= 20 && s < 27) wl[(w * 7 + s - 20) * 64] = wq[s - 20];
      SCHED();
    }
  }
  BARRIER();
  STAMP_AT(1, 3);
  {
    u32x4 f[KSD];
    f32x4 m4[4], r4[4];
    f32x16 vn, kv0 = zero16(), kv1 = zero16();
    u32x4 vfr[2], pa[2], pb[2];
    float rv[8][4];
    const int i16 = l & 15, q4 = i16 >> 2, p4 = i16 & 3, blk = (l >> 4) & 1;
    f32x4* red = (f32x4*)(smem + K1_RED) + w * 512 + l;
    const float* redr = (const float*)(smem + K1_RED) + w * 512 + l;
    float* dst = kvacc + h * 2048 + w * 512 + l;
#pragma unroll
    for (int s = 0; s < PD; ++s) f[s] = wl[s * 64];
#pragma unroll
    for (int g = 0; g < 4; ++g) {
      m4[g] = *(const f32x4*)(ms + 8 * g + 4 * hh);
      r4[g] = *(const f32x4*)(ms + 32 + 8 * g + 4 * hh);
    }
#pragma unroll
    for (int s = 0; s < KSD; ++s) {
      if (s + PD < KSD) f[s + PD] = wl[(s + PD) * 64];
      aq = MFMA32(asfrag(f[s]), asfrag(xr[s]), aq);
      const int q = s - 1;
      if (q == 0) {
#pragma unroll
        for (int s2 = 0; s2 < 2; ++s2) {
          const int t0 = 16 * s2 + 4 * hh + q4, t1 = t0 + 8;
          const int c0 = (4 * blk + p4) ^ ((t0 >> 1) & 7), c1 = (4 * blk + p4) ^ ((t1 >> 1) & 7);
          pa[s2] = join(tr_read(img + t0 * 64 + c0 * 8), tr_read(img + t1 * 64 + c1 * 8));
          pb[s2] = join(tr_read(img + 2048 + t0 * 64 + c0 * 8), tr_read(img + 2048 + t1 * 64 + c1 * 8));
        }
      } else if (q >= 1 && q < 5) {
        const int g = q - 1;
#pragma unroll
        for (int i = 0; i < 4; ++i) {
          float v = opq(opq(__builtin_fmaf(-m4[g][i], csv, av[4 * g + i])) * r4[g][i]);
          vn[4 * g + i] = (r == DH_) ? 1.f : v;
        }
      } else if (q == 5) {
        vfr[0] = pack8(vn, 0);
        vfr[1] = pack8(vn, 1);
      } else if (q == 6) {
        kv0 = MFMA32(asfrag(pa[0]), asfrag(vfr[0]), kv0);
        kv1 = MFMA32(asfrag(pb[0]), asfrag(vfr[0]), kv1);
      } else if (q == 7) {
        kv0 = MFMA32(asfrag(pa[1]), asfrag(vfr[1]), kv0);
        kv1 = MFMA32(asfrag(pb[1]), asfrag(vfr[1]), kv1);
      } else if (q == 10 || q == 11) {
        const f32x16& kk = (q == 10) ? kv0 : kv1;
#pragma unroll
        for (int g = 0; g < 4; ++g)
          red[((q - 10) * 4 + g) * 64] = f32x4{kk[4 * g], kk[4 * g + 1], kk[4 * g + 2], kk[4 * g + 3]};
      } else if (q == 12) {
        BARRIER();
      } else if (q >= 13 && q < 17) {
#pragma unroll
        for (int i = 2 * (q - 13); i < 2 * (q - 13) + 2; ++i)
#pragma unroll
          for (int o = 0; o < 4; ++o) rv[i][o] = redr[o * 2048 + i * 64];
      }
      if (q >= 15 && q < 19) {
#pragma unroll
        for (int i = 2 * (q - 15); i < 2 * (q - 15) + 2; ++i)
          atomicAdd(dst + i * 64, opq(rv[i][0] + rv[i][1]) + opq(rv[i][2] + rv[i][3]));
      }
      SCHED();
    }
  }
  STAMP_AT(1, 4);
  {
    f32x16 pos, neg;
    dual_softmax(aq, csq, rs2, mr2, hh, pos, neg);
    u32x4* dst = phiq + (size_t)((tb * H_ + h) * 4) * 64 + l;
    dst[0] = pack8(pos, 0);
    dst[64] = pack8(pos, 1);
    dst[128] = pack8(neg, 0);
    dst[192] = pack8(neg, 1);
  }
  STAMP_AT(1, 5);
  STAMP_AT(1, 6);
}

template <bool FINAL, bool BIAS>
DI void resid_epilogue(char* smem, const f32x16 (&acc)[2][2], int w, int l, int tb, int nt,
                       const u32x4 (&xo)[2], const u32x2 (&bs)[4],
                       u32x4* __restrict__ xout, f32x2* __restrict__ stout,
                       unsigned short* __restrict__ out) {
  const int r = l & 31, hh = l >> 5;
  f32x4* red = (f32x4*)smem;
  f32x16 own;
#pragma unroll
  for (int tI = 0; tI < 4; ++tI) {
    const f32x16& a = acc[tI >> 1][tI & 1];
    if (tI != w) {
#pragma unroll
      for (int g = 0; g < 4; ++g)
        red[((w * 4 + tI) * 4 + g) * 64 + l] = f32x4{a[4 * g], a[4 * g + 1], a[4 * g + 2], a[4 * g + 3]};
    } else {
      own = a;
    }
  }
  BARRIER();
  f32x16 v;
#pragma unroll
  for (int g = 0; g < 4; ++g) {
    f32x4 s = f32x4{own[4 * g], own[4 * g + 1], own[4 * g + 2], own[4 * g + 3]};
#pragma unroll
    for (int o = 1; o < 4; ++o) s += red[((((w + o) & 3) * 4 + w) * 4 + g) * 64 + l];
    const unsigned x0 = xo[g >> 1][(g & 1) * 2], x1 = xo[g >> 1][(g & 1) * 2 + 1];
    if (BIAS) {
      s[0] += bflo(bs[g][0]); s[1] += bfhi(bs[g][0]); s[2] += bflo(bs[g][1]); s[3] += bfhi(bs[g][1]);
    }
    v[4 * g + 0] = s[0] + bflo(x0);
    v[4 * g + 1] = s[1] + bfhi(x0);
    v[4 * g + 2] = s[2] + bflo(x1);
    v[4 * g + 3] = s[3] + bfhi(x1);
  }
  u32x4 p0 = pack8(v, 0), p1 = pack8(v, 1);
  if (FINAL) {
    unsigned short* o = out + (size_t)(tb * 32 + r) * D_ + 32 * nt + 4 * hh;
    *(u32x2*)(o + 0) = u32x2{p0[0], p0[1]};
    *(u32x2*)(o + 8) = u32x2{p0[2], p0[3]};
    *(u32x2*)(o + 16) = u32x2{p1[0], p1[1]};
    *(u32x2*)(o + 24) = u32x2{p1[2], p1[3]};
  } else {
    xout[(size_t)(tb * KSD + 2 * nt) * 64 + l] = p0;
    xout[(size_t)(tb * KSD + 2 * nt + 1) * 64 + l] = p1;
    float s1 = 0.f, s2 = 0.f;
#pragma unroll
    for (int j = 0; j < 4; ++j) {
      float a = bflo(p0[j]), c = bfhi(p0[j]), d = bflo(p1[j]), e = bfhi(p1[j]);
      s1 += (a + c) + (d + e);
      s2 += (a * a + c * c) + (d * d + e * e);
    }
    s1 = sum32(s1);
    s2 = sum32(s2);
    atomicAdd((float*)stout + (tb * 32 + r) * 2 + hh, hh ? s2 : s1);
  }
}

__global__ __launch_bounds__(256, 1) void k_attn_out(const u32x4* __restrict__ phiq,
                                                     const float* __restrict__ kvacc,
                                                     const u32x4* __restrict__ wo_t,
                                                     const u32x4* __restrict__ xin,
                                                     u32x4* __restrict__ xout, f32x2* __restrict__ stout) {
  __shared__ __attribute__((aligned(16))) char smem[64 * 1024];
  const int bid = blockIdx.x, xcd = bid & 7, bi = bid >> 3;
  const int tp = 4 * xcd + bi / 7, ns = bi % 7;
  const int tid = threadIdx.x, w = __builtin_amdgcn_readfirstlane(tid >> 6), l = tid & 63, r = l & 31;
  const int tbo = 2 * tp + (w >> 1), nto = 2 * ns + (w & 1);
  STAMP_AT(2, 0);
  f32x16 acc[2][2];
#pragma unroll
  for (int a = 0; a < 2; ++a)
#pragma unroll
    for (int b = 0; b < 2; ++b) acc[a][b] = zero16();
  u32x4 xo[2];
  u32x2 bs[4] = {};
  {
    f32x4 kva[4][8];
    u32x4 wf[4][2][2], pq[4][2][4];
    auto issue = [&](int hi) {
      const int hd = 4 * w + hi;
      const f32x4* kp = (const f32x4*)(kvacc + hd * 2048) + l;
#pragma unroll
      for (int i = 0; i < 8; ++i) kva[hi][i] = kp[i * 64];
      SCHED();
#pragma unroll
      for (int a = 0; a < 2; ++a)
#pragma unroll
        for (int i = 0; i < 4; ++i) pq[hi][a][i] = phiq[(size_t)(((2 * tp + a) * H_ + hd) * 4 + i) * 64 + l];
      SCHED();
#pragma unroll
      for (int sp = 0; sp < 2; ++sp)
#pragma unroll
        for (int nt = 0; nt < 2; ++nt) wf[hi][sp][nt] = wo_t[(size_t)((hd * 2 + sp) * NNT + 2 * ns + nt) * 64 + l];
    };
    issue(0); SCHED(); issue(1);
    SCHED();
#pragma unroll
    for (int hi = 0; hi < 4; ++hi) {
      u32x4 kvf[4];
#pragma unroll
      for (int i = 0; i < 4; ++i) {
        f32x4 a = kva[hi][2 * i], b = kva[hi][2 * i + 1];
        kvf[i] = u32x4{pack2(a[0], a[1]), pack2(a[2], a[3]), pack2(b[0], b[1]), pack2(b[2], b[3])};
      }
      SCHED();
      if (hi < 2) issue(hi + 2);
      if (hi == 2) {
        xo[0] = xin[(size_t)(tbo * KSD + 2 * nto) * 64 + l];
        xo[1] = xin[(size_t)(tbo * KSD + 2 * nto + 1) * 64 + l];
      }
      SCHED();
#pragma unroll
      for (int a = 0; a < 2; ++a) {
        f32x16 o = zero16();
#pragma unroll
        for (int i = 0; i < 4; ++i) o = MFMA32(asfrag(kvf[i]), asfrag(pq[hi][a][i]), o);
        float dlo, dhi;
        halves(o[12], dlo, dhi);
        const float inv = 1.f / (dhi + 1e-6f);
#pragma unroll
        for (int i = 0; i < 16; ++i) o[i] *= inv;
        u32x4 of0 = pack8(o, 0), of1 = pack8(o, 1);
#pragma unroll
        for (int nt = 0; nt < 2; ++nt) {
          acc[a][nt] = MFMA32(asfrag(wf[hi][0][nt]), asfrag(of0), acc[a][nt]);
          acc[a][nt] = MFMA32(asfrag(wf[hi][1][nt]), asfrag(of1), acc[a][nt]);
        }
      }
    }
  }
  STAMP_AT(2, 1);
  resid_epilogue<false, false>(smem, acc, w, l, tbo, nto, xo, bs, xout, stout, nullptr);
  STAMP_AT(2, 2);
}

constexpr int K3_X  = 0;
constexpr int K3_LDS = 112 * 1024;

__global__ __launch_bounds__(256, 1) void k_ffn1(const u32x4* __restrict__ xf, const f32x2* __restrict__ st,
                                                 const u32x4* __restrict__ w1_t, const float* __restrict__ cs1,
                                                 const unsigned short* __restrict__ b1,
                                                 u32x4* __restrict__ hf) {
  __shared__ __attribute__((aligned(16))) char smem[K3_LDS];
  const int bid = blockIdx.x, xcd = bid & 7, bi = bid >> 3;
  const int tt = 2 * xcd + bi / 14, fg = bi % 14;
  const int tid = threadIdx.x, w = __builtin_amdgcn_readfirstlane(tid >> 6), l = tid & 63, r = l & 31, hh = l >> 5;
  const int ft = 4 * fg + w;
  STAMP_AT(3, 0);
  const u32x4* xsrc = xf + (size_t)(tt * 4 * KSD + w * 7) * 64 + l;
  const u32x4* wsrc = w1_t + (size_t)ft * KSD * 64 + l;
  u32x4 xs[4][7], wr[KSD];
  f32x2 stv[4];
  f32x4 c4[4];
  u32x2 b4[4];
#pragma unroll
  for (int i = 0; i < 7; ++i) xs[0][i] = xsrc[i * 64];
  SCHED();
#pragma unroll
  for (int s = 0; s < 16; ++s) { wr[s] = wsrc[s * 64]; SCHED(); }
  u32x4* xl = (u32x4*)(smem + K3_X) + l;
#pragma unroll
  for (int i = 0; i < 7; ++i) xl[(w * 7 + i) * 64] = xs[0][i];
  BARRIER();
  STAMP_AT(3, 1);
  {
    constexpr int PD = 3;
    f32x16 acc[4];
    f32x16 y, u, bf;
    float mr = 0.f, rstd = 0.f;
    const float c0 = -2.f * 0.7978845608028654f * LOG2E;
    const float c1 = c0 * 0.044715f;
    sfor<0, 5>([&](auto A_) {
      constexpr int a = decltype(A_)::v;
      u32x4 xb[KSD];
      if constexpr (a < 4) {
        acc[a] = zero16();
#pragma unroll
        for (int s = 0; s < PD; ++s) xb[s] = xl[(a * KSD + s) * 64];
      }
      sfor<0, KSD>([&](auto S_c) {
        constexpr int s = decltype(S_c)::v;
        if constexpr (a == 0) {
          if constexpr (s < 12) wr[16 + s] = wsrc[(16 + s) * 64];
          else if constexpr (s < 19) xs[1][s - 12] = xsrc[(KSD + s - 12) * 64];
          else if constexpr (s < 23) {
            stv[s - 19] = st[tt * 128 + (s - 19) * 32 + r];
            c4[s - 19] = *(const f32x4*)(cs1 + 32 * ft + 8 * (s - 19) + 4 * hh);
            b4[s - 19] = *(const u32x2*)(b1 + 32 * ft + 8 * (s - 19) + 4 * hh);
          } else xs[2][s - 23] = xsrc[(2 * KSD + s - 23) * 64];
        }
        if constexpr (a == 1) {
          if constexpr (s < 2) xs[2][5 + s] = xsrc[(2 * KSD + 5 + s) * 64];
          else if constexpr (s < 9) xs[3][s - 2] = xsrc[(3 * KSD + s - 2) * 64];
        }
        if constexpr (a < 4) {
          if constexpr (s + PD < KSD) xb[s + PD] = xl[(a * KSD + s + PD) * 64];
          acc[a] = MFMA32(asfrag(wr[s]), asfrag(xb[s]), acc[a]);
        }
        if constexpr (a > 0) {
          if constexpr (s == 0) {
            float mean;
            finish_stats(stv[a - 1], mean, rstd);
            mr = -mean * rstd;
            if constexpr (a == 1) {
#pragma unroll
              for (int i = 0; i < 16; ++i) bf[i] = (i & 1) ? bfhi(b4[i >> 2][(i >> 1) & 1]) : bflo(b4[i >> 2][(i >> 1) & 1]);
            }
          }
          sfor<0, 36>([&](auto Q_) {
            constexpr int q = decltype(Q_)::v;
            if constexpr (1 + (q * 26) / 36 == s) {
              constexpr int k = q >> 2, g = q & 3;
#pragma unroll
              for (int i = 4 * g; i < 4 * g + 4; ++i) {
                if constexpr (k == 0) y[i] = opq(__builtin_fmaf(acc[a - 1][i], rstd, opq(__builtin_fmaf(mr, c4[g][i & 3], bf[i]))));
                else if constexpr (k == 1) u[i] = opq(y[i] * y[i]);
                else if constexpr (k == 2) u[i] = opq(__builtin_fmaf(u[i], c1, c0));
                else if constexpr (k == 3) u[i] = opq(u[i] * y[i]);
                else if constexpr (k == 4) u[i] = __builtin_amdgcn_exp2f(u[i]);
                else if constexpr (k == 5) u[i] = opq(u[i] + 1.f);
                else if constexpr (k == 6) u[i] = __builtin_amdgcn_rcpf(u[i]);
                else if constexpr (k == 7) u[i] = opq(u[i] * y[i]);
              }
              if constexpr (k == 8 && (g & 1)) {
                u32x4* dst = hf + (size_t)((tt * 4 + a - 1) * KSF + 2 * ft) * 64 + l;
                dst[(g >> 1) * 64] = pack8(u, g >> 1);
              }
            }
          });
        }
        if constexpr (a < 3 && s >= 20 && s < 27) xl[((a + 1) * KSD + w * 7 + (s - 20)) * 64] = xs[a + 1][s - 20];
        if constexpr (a < 4) SCHED();
      });
      if constexpr (a < 3) BARRIER();
      STAMP_AT(3, 2 + a);
    });
  }
}

template <bool FINAL>
__global__ __launch_bounds__(256, 1) void k_ffn2(const u32x4* __restrict__ hf, const u32x4* __restrict__ w2_t,
                                                 const unsigned short* __restrict__ b2,
                                                 const u32x4* __restrict__ xin, u32x4* __restrict__ xout,
                                                 f32x2* __restrict__ stout, unsigned short* __restrict__ out) {
  __shared__ __attribute__((aligned(16))) char smem[64 * 1024];
  const int bid = blockIdx.x, xcd = bid & 7, bi = bid >> 3;
  const int tp = 4 * xcd + bi / 7, ns = bi % 7;
  const int tid = threadIdx.x, w = __builtin_amdgcn_readfirstlane(tid >> 6), l = tid & 63, hh = l >> 5;
  const int tbo = 2 * tp + (w >> 1), nto = 2 * ns + (w & 1);
  STAMP_AT(4, 0);
  f32x16 acc[2][2];
#pragma unroll
  for (int a = 0; a < 2; ++a)
#pragma unroll
    for (int b = 0; b < 2; ++b) acc[a][b] = zero16();
  const u32x4* h0 = hf + (size_t)((2 * tp) * KSF + 28 * w) * 64 + l;
  const u32x4* h1 = h0 + (size_t)KSF * 64;
  const u32x4* w0 = w2_t + (size_t)((2 * ns) * KSF + 28 * w) * 64 + l;
  const u32x4* w1p = w0 + (size_t)KSF * 64;
  u32x4 xo[2];
  u32x2 bs[4];
  {
    constexpr int PD = 12;
    u32x4 a0[28], a1[28], b0[28], b1[28];
#pragma unroll
    for (int s = 0; s < PD; ++s) {
      a0[s] = w0[s * 64]; b0[s] = h0[s * 64]; a1[s] = w1p[s * 64]; b1[s] = h1[s * 64];
      SCHED();
    }
#pragma unroll
    for (int s = 0; s < 28; ++s) {
      if (s + PD < 28) {
        a0[s + PD] = w0[(s + PD) * 64]; b0[s + PD] = h0[(s + PD) * 64];
        a1[s + PD] = w1p[(s + PD) * 64]; b1[s + PD] = h1[(s + PD) * 64];
      }
      if (s + PD == 28) {
        xo[0] = xin[(size_t)(tbo * KSD + 2 * nto) * 64 + l];
        xo[1] = xin[(size_t)(tbo * KSD + 2 * nto + 1) * 64 + l];
#pragma unroll
        for (int g = 0; g < 4; ++g) bs[g] = *(const u32x2*)(b2 + 32 * nto + 8 * g + 4 * hh);
      }
      SCHED();
      acc[0][0] = MFMA32(asfrag(a0[s]), asfrag(b0[s]), acc[0][0]);
      acc[0][1] = MFMA32(asfrag(a1[s]), asfrag(b0[s]), acc[0][1]);
      acc[1][0] = MFMA32(asfrag(a0[s]), asfrag(b1[s]), acc[1][0]);
      acc[1][1] = MFMA32(asfrag(a1[s]), asfrag(b1[s]), acc[1][1]);
      SCHED();
    }
  }
  STAMP_AT(4, 1);
  resid_epilogue<FINAL, true>(smem, acc, w, l, tbo, nto, xo, bs, xout, stout, out);
  STAMP_AT(4, 2);
}

}

extern "C" void kernel_launch(void* const* d_in, const int* in_sizes, int n_in, void* d_out, int out_size,
                              void* d_ws, size_t ws_size, hipStream_t stream) {
  const unsigned short* x = (const unsigned short*)d_in[0];
  const unsigned short* wqkv = (const unsigned short*)d_in[1];
  const unsigned short* wphi = (const unsigned short*)d_in[2];
  const unsigned short* wo = (const unsigned short*)d_in[3];
  const unsigned short* w1 = (const unsigned short*)d_in[5];
  const unsigned short* b1 = (const unsigned short*)d_in[6];
  const unsigned short* w2 = (const unsigned short*)d_in[7];
  const unsigned short* b2 = (const unsigned short*)d_in[8];
  char* ws = (char*)d_ws;
  unsigned short* out = (unsigned short*)d_out;

  auto XF = [&](int i) { return (u32x4*)(ws + OFF_XF + (size_t)i * XF_BYTES); };
  auto ST = [&](int i) { return (f32x2*)(ws + OFF_ST + (size_t)i * ST_BYTES); };
  const u32x4* wqkv_t = (const u32x4*)(ws + OFF_WQKV);
  const float* cs_qkv = (const float*)(ws + OFF_CSQKV);
  const u32x4* wo_t = (const u32x4*)(ws + OFF_WO);
  const u32x4* w1_t = (const u32x4*)(ws + OFF_W1);
  const float* cs1 = (const float*)(ws + OFF_CS1);
  const u32x4* w2_t = (const u32x4*)(ws + OFF_W2);
  u32x4* phiq = (u32x4*)(ws + OFF_PHIQ);
  float* kv = (float*)(ws + OFF_KV);
  u32x4* hf = (u32x4*)(ws + OFF_HF);

  k_prep<<<256, 256, 0, stream>>>(x, wqkv, wphi, wo, w1, w2, ws);
  for (int layer = 0; layer < 2; ++layer) {
    const int i0 = 2 * layer;
    float* kvl = kv + layer * H_ * 2048;
    k_qkv<<<256, 256, 0, stream>>>(XF(i0), ST(i0), wqkv_t, cs_qkv, phiq, kvl);
    k_attn_out<<<224, 256, 0, stream>>>(phiq, kvl, wo_t, XF(i0), XF(i0 + 1), ST(i0 + 1));
    k_ffn1<<<224, 256, 0, stream>>>(XF(i0 + 1), ST(i0 + 1), w1_t, cs1, b1, hf);
    if (layer == 0)
      k_ffn2<false><<<224, 256, 0, stream>>>(hf, w2_t, b2, XF(i0 + 1), XF(i0 + 2), ST(i0 + 2), nullptr);
    else
      k_ffn2<true><<<224, 256, 0, stream>>>(hf, w2_t, b2, XF(i0 + 1), nullptr, nullptr, out);
  }
}
```

```cpp
#include <hip/hip_runtime.h>
#include <hip/hip_bf16.h>
#include <stdint.h>

typedef __attribute__((ext_vector_type(8))) short bf16x8;
typedef __attribute__((ext_vector_type(4))) short s16x4;
typedef __attribute__((ext_vector_type(16))) float f32x16;
typedef __attribute__((ext_vector_type(4))) float f32x4;
typedef __attribute__((ext_vector_type(2))) float f32x2;
typedef __attribute__((ext_vector_type(4))) unsigned u32x4;
typedef __attribute__((ext_vector_type(2))) unsigned u32x2;
typedef __attribute__((ext_vector_type(2))) __bf16 bf16x2_t;

#define DI __device__ __forceinline__
#define SCHED() __builtin_amdgcn_sched_barrier(0)
#define PIN(x) asm volatile("" : "+a"(x))
#define MFMA32(a, b, c) __builtin_amdgcn_mfma_f32_32x32x16_bf16((a), (b), (c), 0, 0, 0)

#define STAMP_AT(kid, i)

namespace {

constexpr int S_ = 2048, D_ = 448, H_ = 16, DH_ = 28, F_ = 1792;
constexpr int NTB = S_ / 32;
constexpr int KSD = D_ / 16;
constexpr int KSF = F_ / 16;
constexpr int NNT = D_ / 32;
constexpr int NFT = F_ / 32;
constexpr int KSA = KSD + 1;

constexpr size_t XF_BYTES   = (size_t)NTB * KSD * 1024;
constexpr size_t ST_BYTES   = (size_t)S_ * 8;
constexpr size_t OFF_XF     = 0;
constexpr size_t OFF_ST     = OFF_XF + 4 * XF_BYTES;
constexpr size_t OFF_WQKV   = OFF_ST + 4 * ST_BYTES;
constexpr size_t OFF_WO     = OFF_WQKV + (size_t)H_ * 3 * KSA * 1024;
constexpr size_t OFF_W1     = OFF_WO + (size_t)H_ * 2 * NNT * 1024;
constexpr size_t OFF_W2     = OFF_W1 + (size_t)NFT * KSA * 1024;
constexpr size_t OFF_PHIQ   = OFF_W2 + (size_t)NNT * KSF * 1024;
constexpr size_t OFF_KV     = OFF_PHIQ + (size_t)NTB * H_ * 4 * 1024;
constexpr size_t OFF_HF     = OFF_KV + (size_t)2 * H_ * 2048 * 4;
constexpr size_t WS_TOTAL   = OFF_HF + (size_t)NTB * KSF * 1024;

DI unsigned pack2(float a, float b) {
  f32x2 v = {a, b};
  bf16x2_t r = __builtin_convertvector(v, bf16x2_t);
  return __builtin_bit_cast(unsigned, r);
}
DI float bflo(unsigned u) { return __uint_as_float(u << 16); }
DI float bfhi(unsigned u) { return __uint_as_float(u & 0xffff0000u); }
DI float bf1(unsigned short u) { return __uint_as_float(((unsigned)u) << 16); }
DI bf16x8 asfrag(u32x4 v) { return __builtin_bit_cast(bf16x8, v); }
DI u32x4 pack8(const f32x16& x, int sp) {
  u32x4 p;
  p[0] = pack2(x[8 * sp + 0], x[8 * sp + 1]);
  p[1] = pack2(x[8 * sp + 2], x[8 * sp + 3]);
  p[2] = pack2(x[8 * sp + 4], x[8 * sp + 5]);
  p[3] = pack2(x[8 * sp + 6], x[8 * sp + 7]);
  return p;
}
DI float xor32(float v) { return __shfl_xor(v, 32, 64); }
DI void halves(float v, float& lo, float& hi) {
  auto r = __builtin_amdgcn_permlane32_swap(__float_as_uint(v), __float_as_uint(v), false, false);
  lo = __uint_as_float(r[0]);
  hi = __uint_as_float(r[1]);
}
DI float sum32(float v) { float a, b; halves(v, a, b); return a + b; }
DI float max32(float v) { float a, b; halves(v, a, b); return fmaxf(a, b); }
DI float min32(float v) { float a, b; halves(v, a, b); return fminf(a, b); }
DI float opq(float v) { asm("" : "+v"(v)); return v; }
DI f32x16 zero16() {
  f32x16 z;
#pragma unroll
  for (int i = 0; i < 16; ++i) z[i] = 0.f;
  return z;
}
template <int N>
DI void glds(unsigned voff, const void* sbase, unsigned lds) {
  unsigned keep;
  if constexpr (N == 4)
    asm volatile("s_mov_b32 %0, m0\n\ts_mov_b32 m0, %3\n\ts_nop 4\n\t"
                 "global_load_lds_dwordx4 %1, %2\n\tglobal_load_lds_dwordx4 %1, %2 offset:1024\n\t"
                 "global_load_lds_dwordx4 %1, %2 offset:2048\n\tglobal_load_lds_dwordx4 %1, %2 offset:3072\n\t"
                 "s_mov_b32 m0, %0" : "=&s"(keep) : "v"(voff), "s"(sbase), "s"(lds) : "memory");
  else if constexpr (N == 3)
    asm volatile("s_mov_b32 %0, m0\n\ts_mov_b32 m0, %3\n\ts_nop 4\n\t"
                 "global_load_lds_dwordx4 %1, %2\n\tglobal_load_lds_dwordx4 %1, %2 offset:1024\n\t"
                 "global_load_lds_dwordx4 %1, %2 offset:2048\n\t"
                 "s_mov_b32 m0, %0" : "=&s"(keep) : "v"(voff), "s"(sbase), "s"(lds) : "memory");
}
template <int OFF>
DI void gload(u32x4& d, unsigned voff, const void* sbase) {
  asm volatile("global_load_dwordx4 %0, %1, %2 offset:%3" : "=v"(d) : "v"(voff), "s"(sbase), "i"(OFF));
}
template <int OFF>
DI void gload2(f32x2& d, unsigned voff, const void* sbase) {
  asm volatile("s_nop 4\n\tglobal_load_dwordx2 %0, %1, %2 offset:%3" : "=v"(d) : "v"(voff), "s"(sbase), "i"(OFF));
}
template <int N> DI void vwait(u32x4& d) { asm volatile("s_waitcnt vmcnt(%1)" : "+v"(d) : "i"(N)); }
template <int N> DI void vwait2(f32x2& d) { asm volatile("s_waitcnt vmcnt(%1)" : "+v"(d) : "i"(N)); }
template <int N> DI void vwait_mem() { asm volatile("s_waitcnt vmcnt(%0)" :: "i"(N) : "memory"); }
DI unsigned lds_addr(const void* p) {
  return (unsigned)(uintptr_t)(const __attribute__((address_space(3))) char*)p;
}
DI unsigned bfbits(float v) { return pack2(v, 0.f) & 0xffffu; }
DI void split3(float v, unsigned& p1, unsigned& p2, unsigned& p3) {
  p1 = bfbits(v);
  const float r1 = v - bflo(p1);
  p2 = bfbits(r1);
  p3 = bfbits(r1 - bflo(p2));
}
DI u32x4 aug_w(float cs) {
  unsigned c1, c2, c3;
  split3(cs, c1, c2, c3);
  return u32x4{c1 | (c2 << 16), c3 | (c1 << 16), c2 | (c1 << 16), 0u};
}
DI u32x4 aug_x(float mean, float sd, int hh) {
  unsigned a, b, c;
  split3(hh ? sd : -mean, a, b, c);
  u32x4 lo = {a | (a << 16), a | (b << 16), b | (c << 16), 0u};
  u32x4 hi = {a | (b << 16), c, 0u, 0u};
  return hh ? hi : lo;
}

template <int I> struct IC { static constexpr int v = I; };
template <int B, int E, class Fn>
DI void sfor(Fn&& f) {
  if constexpr (B < E) {
    f(IC<B>{});
    sfor<B + 1, E>(f);
  }
}

#define BARRIER() asm volatile("s_waitcnt lgkmcnt(0)\n\ts_barrier" ::: "memory")

typedef __attribute__((address_space(3))) s16x4* lds_s16x4p;
DI s16x4 tr_read(const char* p) { return __builtin_amdgcn_ds_read_tr16_b64_v4i16((lds_s16x4p)p); }
DI u32x4 join(s16x4 a, s16x4 b) {
  bf16x8 v = __builtin_shufflevector(a, b, 0, 1, 2, 3, 4, 5, 6, 7);
  return __builtin_bit_cast(u32x4, v);
}

DI u32x4 frag_from_image(const char* img, int row_stride, int s, int l, int cb) {
  const int i16 = l & 15, q4 = i16 >> 2, p4 = i16 & 3, blk = (l >> 4) & 1, hh = l >> 5;
  const char* p = img + (16 * s + 4 * hh + q4) * row_stride + cb + (4 * blk + p4) * 8;
  return join(tr_read(p), tr_read(p + 8 * row_stride));
}

__global__ __launch_bounds__(256) void k_prep(const unsigned short* __restrict__ x,
                                              const unsigned short* __restrict__ wqkv,
                                              const unsigned short* __restrict__ wphi,
                                              const unsigned short* __restrict__ wo,
                                              const unsigned short* __restrict__ w1,
                                              const unsigned short* __restrict__ b1,
                                              const unsigned short* __restrict__ w2,
                                              char* __restrict__ ws) {
  __shared__ __attribute__((aligned(16))) char img[32 * 912 + 1024];
  float (*red)[32] = (float (*)[32])(img + 32 * 912);
  const int b = blockIdx.x, tid = threadIdx.x, w = tid >> 6, l = tid & 63, r = l & 31, hh = l >> 5;
  ((float*)(ws + OFF_KV))[b * 256 + tid] = 0.f;

  if (b < 32) {
    const int h = b >> 1, p = b & 1;
    const int cc = r < DH_ ? r : r - DH_;
    u32x4 bfr[2];
    u32x2 lo[4][2], hi[4][2];
#pragma unroll
    for (int it = 0; it < 4; ++it) {
      const int blk = min(w + 4 * it, 13);
      const unsigned short* row = wqkv + (size_t)(32 * blk + r) * (3 * D_) + p * D_ + DH_ * h;
#pragma unroll
      for (int s2 = 0; s2 < 2; ++s2) {
        const int e0 = 16 * s2 + 8 * hh;
        lo[it][s2] = *(const u32x2*)(row + e0);
        hi[it][s2] = *(const u32x2*)(row + min(e0 + 4, 24));
        if (e0 + 4 >= DH_) hi[it][s2] = u32x2{0u, 0u};
      }
    }
#pragma unroll
    for (int s2 = 0; s2 < 2; ++s2) {
      unsigned short v[8];
#pragma unroll
      for (int j = 0; j < 8; ++j) {
        const int e = 16 * s2 + 8 * hh + j;
        v[j] = wphi[(h * DH_ + min(e, DH_ - 1)) * DH_ + cc];
        if (e >= DH_) v[j] = 0;
      }
#pragma unroll
      for (int j = 0; j < 4; ++j) bfr[s2][j] = (unsigned)v[2 * j] | ((unsigned)v[2 * j + 1] << 16);
    }
    float cs = 0.f;
    u32x4* dst = (u32x4*)(ws + OFF_WQKV) + (size_t)(h * 3 + (p ? 0 : 2)) * KSA * 64;
#pragma unroll
    for (int it = 0; it < 4; ++it) {
      const int blk = w + 4 * it;
      f32x16 acc = zero16();
#pragma unroll
      for (int s2 = 0; s2 < 2; ++s2) {
        u32x4 a = {lo[it][s2][0], lo[it][s2][1], hi[it][s2][0], hi[it][s2][1]};
        acc = MFMA32(asfrag(a), asfrag(bfr[s2]), acc);
      }
      if (blk < 14) {
#pragma unroll
        for (int sp = 0; sp < 2; ++sp) {
          u32x4 pk = pack8(acc, sp);
          dst[(2 * blk + sp) * 64 + l] = pk;
#pragma unroll
          for (int j = 0; j < 4; ++j) cs += bflo(pk[j]) + bfhi(pk[j]);
        }
      }
    }
    cs += xor32(cs);
    if (hh == 0) red[w][r] = cs;
    __syncthreads();
    if (tid < 64) {
      u32x4 a = aug_w(red[0][r] + red[1][r] + red[2][r] + red[3][r]);
      if (hh) a = u32x4{0u, 0u, 0u, 0u};
      dst[KSD * 64 + l] = a;
    }
  } else if (b < 48 || (b >= 64 && b < 176)) {
    u32x4* dst;
    bool aug = true;
    u32x4 aughi = {0u, 0u, 0u, 0u};
    if (b < 48) {
      const int h = b - 32;
      dst = (u32x4*)(ws + OFF_WQKV) + (size_t)(h * 3 + 1) * KSA * 64;
      aughi[0] = (r == DH_) ? 0x3f80u : (r == DH_ + 1) ? 0x3f800000u : 0u;
      u32x2 v[14];
#pragma unroll
      for (int i = 0; i < 14; ++i) {
        const int idx = tid + 256 * i, row = idx >> 3, c8 = idx & 7;
        v[i] = *(const u32x2*)(wqkv + (size_t)row * (3 * D_) + 2 * D_ + DH_ * h + 4 * min(c8, 6));
        if (c8 == 7) v[i] = u32x2{0u, 0u};
      }
#pragma unroll
      for (int i = 0; i < 14; ++i) *(u32x2*)(img + (tid + 256 * i) * 8) = v[i];
    } else {
      const unsigned short* src;
      int ld;
      if (b < 120) {
        const int ft = b - 64;
        dst = (u32x4*)(ws + OFF_W1) + (size_t)ft * KSA * 64;
        const unsigned bb = b1[32 * ft + r];
        aughi[0] = bb | (bb << 16);
        aughi[1] = bb;
        src = w1 + 32 * ft;
        ld = F_;
      } else {
        const int bb = b - 120, nt = bb >> 2, qd = bb & 3;
        dst = (u32x4*)(ws + OFF_W2) + (size_t)(nt * KSF + qd * 28) * 64;
        aug = false;
        src = w2 + (size_t)qd * 448 * D_ + 32 * nt;
        ld = D_;
      }
      u32x4 v[7];
#pragma unroll
      for (int i = 0; i < 7; ++i) {
        const int idx = tid + 256 * i, row = idx >> 2, c = idx & 3;
        v[i] = *(const u32x4*)(src + (size_t)row * ld + 8 * c);
      }
#pragma unroll
      for (int i = 0; i < 7; ++i) *(u32x4*)(img + (tid + 256 * i) * 16) = v[i];
    }
    __syncthreads();
    float cs = 0.f;
#pragma unroll
    for (int i = 0; i < 7; ++i) {
      const int s = w * 7 + i;
      u32x4 pk = frag_from_image(img, 64, s, l, 0);
      dst[s * 64 + l] = pk;
#pragma unroll
      for (int j = 0; j < 4; ++j) cs += bflo(pk[j]) + bfhi(pk[j]);
    }
    if (aug) {
      cs += xor32(cs);
      if (hh == 0) red[w][r] = cs;
      __syncthreads();
      if (tid < 64) {
        u32x4 a = aug_w(red[0][r] + red[1][r] + red[2][r] + red[3][r]);
        dst[KSD * 64 + l] = hh ? aughi : a;
      }
    }
  } else if (b < 64) {
    const int h = b - 48;
    u32x4 v[7];
#pragma unroll
    for (int i = 0; i < 7; ++i) {
      const int idx = tid + 256 * i;
      v[i] = *(const u32x4*)(wo + (size_t)DH_ * h * D_ + min(idx, 28 * 56 - 1) * 8);
      if (idx >= 28 * 56) v[i] = u32x4{0u, 0u, 0u, 0u};
    }
#pragma unroll
    for (int i = 0; i < 7; ++i) *(u32x4*)(img + (tid + 256 * i) * 16) = v[i];
    __syncthreads();
    u32x4* dst = (u32x4*)(ws + OFF_WO) + (size_t)h * 28 * 64;
#pragma unroll
    for (int i = 0; i < 7; ++i) {
      const int fi = w * 7 + i, ks = fi / NNT, nt = fi % NNT;
      dst[fi * 64 + l] = frag_from_image(img, 896, ks, l, 64 * nt);
    }
  } else if (b < 240) {
    const int tb = b - 176;
    u32x4 v[7];
#pragma unroll
    for (int i = 0; i < 7; ++i) v[i] = *(const u32x4*)(x + (size_t)tb * 32 * D_ + (tid + 256 * i) * 8);
#pragma unroll
    for (int i = 0; i < 7; ++i) {
      const int idx = tid + 256 * i, row = idx / 56, c = idx % 56;
      *(u32x4*)(img + row * 912 + c * 16) = v[i];
    }
    __syncthreads();
    u32x4* dst = (u32x4*)(ws + OFF_XF) + (size_t)tb * KSD * 64;
    float s1 = 0.f, s2 = 0.f;
#pragma unroll
    for (int i = 0; i < 7; ++i) {
      const int s = w * 7 + i;
      const char* p = img + r * 912 + 32 * s + 8 * hh;
      u32x2 lo = *(const u32x2*)p;
      u32x2 hi = *(const u32x2*)(p + 16);
      u32x4 pk = {lo[0], lo[1], hi[0], hi[1]};
      dst[s * 64 + l] = pk;
#pragma unroll
      for (int j = 0; j < 4; ++j) {
        float a = bflo(pk[j]), c = bfhi(pk[j]);
        s1 += a + c;
        s2 += a * a + c * c;
      }
    }
    s1 = sum32(s1);
    s2 = sum32(s2);
    float* red2 = (float*)(img + 32 * 912);
    red2[w * 64 + l] = hh ? s2 : s1;
    __syncthreads();
    if (tid < 64) {
      const float v = red2[tid] + red2[64 + tid] + red2[128 + tid] + red2[192 + tid];
      ((float*)(ws + OFF_ST))[(tb * 32 + r) * 2 + hh] = v;
    }
  } else {
    float* z = (float*)(ws + OFF_ST + ST_BYTES) + (b - 240) * 768 + tid;
    z[0] = 0.f; z[256] = 0.f; z[512] = 0.f;
  }
}

DI void finish_stats(f32x2 v, float& mean, float& rstd, float& sd) {
  mean = v[0] * (1.f / D_);
  const float var = fmaxf(v[1] * (1.f / D_) - mean * mean, 0.f) + 1e-5f;
  rstd = rsqrtf(var);
  sd = var * rstd;
}

constexpr float LOG2E = 1.4426950408889634f;
DI float max3(float a, float b, float c) { return fmaxf(fmaxf(a, b), c); }
DI float min3(float a, float b, float c) { return fminf(fminf(a, b), c); }

DI void dual_softmax(const f32x16& y, float rs2, float scale, int hh, f32x16& pos, f32x16& neg) {
  float mx = max3(y[0], y[1], y[2]), mn = min3(y[0], y[1], y[2]);
#pragma unroll
  for (int i = 3; i < 15; i += 2) {
    mx = max3(mx, y[i], y[i + 1]);
    mn = min3(mn, y[i], y[i + 1]);
  }
  mx = fmaxf(mx, y[15]);
  mn = fminf(mn, y[15]);
  const float bp = -max32(mx) * rs2, bn = min32(mn) * rs2;
#pragma unroll
  for (int i = 0; i < 16; ++i) {
    pos[i] = __builtin_amdgcn_exp2f(__builtin_fmaf(y[i], rs2, bp));
    neg[i] = __builtin_amdgcn_exp2f(__builtin_fmaf(y[i], -rs2, bn));
  }
  float sp = 0.f, sn = 0.f;
#pragma unroll
  for (int i = 0; i < 12; ++i) { sp += pos[i]; sn += neg[i]; }
  float sph = (pos[12] + pos[13]) + (pos[14] + pos[15]);
  float snh = (neg[12] + neg[13]) + (neg[14] + neg[15]);
  sp += hh ? 0.f : sph;
  sn += hh ? 0.f : snh;
  sp = sum32(sp);
  sn = sum32(sn);
  const float ip = __builtin_amdgcn_rcpf(sp) * scale, in = __builtin_amdgcn_rcpf(sn) * scale;
  const float iph = hh ? 0.f : ip, inh = hh ? 0.f : in;
#pragma unroll
  for (int i = 0; i < 16; ++i) {
    pos[i] *= (i < 12) ? ip : iph;
    neg[i] *= (i < 12) ? in : inh;
  }
}

constexpr int K1_W    = 0;
constexpr int K1_IMG  = K1_W + 88 * 1024;
constexpr int K1_RED  = K1_IMG + 16 * 1024;
constexpr int K1_LDS  = K1_RED + 32 * 1024;

__global__ __launch_bounds__(256, 1) void k_qkv(const u32x4* __restrict__ xf, const f32x2* __restrict__ st,
                                                const u32x4* __restrict__ wqkv_t,
                                                u32x4* __restrict__ phiq, float* __restrict__ kvacc) {
  __shared__ __attribute__((aligned(16))) char smem[K1_LDS];
  const int bid = blockIdx.x, xcd = bid & 7, bi = bid >> 3;
  const int t = 2 * xcd + (bi >> 4), h = bi & 15;
  const int tid = threadIdx.x, w = __builtin_amdgcn_readfirstlane(tid >> 6), l = tid & 63, r = l & 31, hh = l >> 5;
  const int tb = t * 4 + w;
  STAMP_AT(1, 0);
  const unsigned l16 = l * 16;
  const char* wb = (const char*)wqkv_t + (size_t)h * (3 * KSA * 1024);
  const char* xb = (const char*)xf + (size_t)tb * (KSD * 1024);
  u32x4 xr[KSD];
  f32x2 stv;
  gload2<0>(stv, r * 8, st + tb * 32);
  {
    const char* g = wb + w * (15 * 1024);
    const unsigned lw = lds_addr(smem) + K1_W + w * (15 * 1024);
    glds<4>(l16, g, lw);
    glds<4>(l16, g + 4096, lw + 4096);
    glds<4>(l16, g + 8192, lw + 8192);
    glds<3>(l16, g + 12288, lw + 12288);
  }
  sfor<0, KSD>([&](auto S_c) {
    constexpr int s = decltype(S_c)::v;
    gload<(s & 7) * 1024 - 4096>(xr[s], l16, xb + (s >> 3) * 8192 + 4096);
  });
  {
    const char* g = wb + (60 + 7 * w) * 1024;
    const unsigned lw = lds_addr(smem) + K1_W + (60 + 7 * w) * 1024;
    glds<4>(l16, g, lw);
    glds<3>(l16, g + 4096, lw + 4096);
  }
  u32x4* wl = (u32x4*)(smem + K1_W) + l;
  char* img = smem + K1_IMG + w * 4096;
  vwait2<35 + 15>(stv);
  float mean, rstd, sd;
  finish_stats(stv, mean, rstd, sd);
  const u32x4 xa = aug_x(mean, sd, hh);
  const float rs2 = rstd * LOG2E;
  vwait_mem<35>();
  BARRIER();
  STAMP_AT(1, 1);
  f32x16 ak = zero16(), av = zero16(), aq = zero16();
  {
    constexpr int PD = 2;
    u32x4 fk[KSA], fv[KSA];
    fk[KSD] = wl[KSD * 64];
    fv[KSD] = wl[(KSA + KSD) * 64];
#pragma unroll
    for (int s = 0; s < PD; ++s) { fk[s] = wl[s * 64]; fv[s] = wl[(KSA + s) * 64]; }
    ak = MFMA32(asfrag(fk[KSD]), asfrag(xa), ak);
    av = MFMA32(asfrag(xa), asfrag(fv[KSD]), av);
    SCHED();
    sfor<0, KSD>([&](auto S_c) {
      constexpr int s = decltype(S_c)::v;
      if constexpr (s + PD < KSD) { fk[s + PD] = wl[(s + PD) * 64]; fv[s + PD] = wl[(KSA + s + PD) * 64]; }
      vwait<7 + 27 - s>(xr[s]);
      ak = MFMA32(asfrag(fk[s]), asfrag(xr[s]), ak);
      av = MFMA32(asfrag(xr[s]), asfrag(fv[s]), av);
      PIN(ak); PIN(av);
      SCHED();
    });
  }
  STAMP_AT(1, 2);
  u32x4 vfr[2];
  {
    f32x16 pos, neg;
    dual_softmax(ak, rs2, rstd, hh, pos, neg);
#pragma unroll
    for (int g = 0; g < 4; ++g) {
      const int ch = (2 * g + hh) ^ ((r >> 1) & 7);
      *(u32x2*)(img + r * 64 + ch * 8) = u32x2{pack2(pos[4 * g], pos[4 * g + 1]), pack2(pos[4 * g + 2], pos[4 * g + 3])};
      *(u32x2*)(img + 2048 + r * 64 + ch * 8) = u32x2{pack2(neg[4 * g], neg[4 * g + 1]), pack2(neg[4 * g + 2], neg[4 * g + 3])};
    }
    vfr[0] = pack8(av, 0);
    vfr[1] = pack8(av, 1);
  }
  vwait_mem<0>();
  BARRIER();
  STAMP_AT(1, 3);
  {
    constexpr int PD = 3;
    u32x4 f[KSA];
    f32x16 kv0 = zero16(), kv1 = zero16();
    u32x4 pa[2], pb[2];
    float rv[8][4];
    const int i16 = l & 15, q4 = i16 >> 2, p4 = i16 & 3, blk = (l >> 4) & 1;
    f32x4* red = (f32x4*)(smem + K1_RED) + w * 512 + l;
    const float* redr = (const float*)(smem + K1_RED) + w * 512 + l;
    float* dst = kvacc + h * 2048 + w * 512 + l;
#pragma unroll
    for (int s2 = 0; s2 < 2; ++s2) {
      const int t0 = 16 * s2 + 4 * hh + q4, t1 = t0 + 8;
      const int c0 = (4 * blk + p4) ^ ((t0 >> 1) & 7), c1 = (4 * blk + p4) ^ ((t1 >> 1) & 7);
      pa[s2] = join(tr_read(img + t0 * 64 + c0 * 8), tr_read(img + t1 * 64 + c1 * 8));
      pb[s2] = join(tr_read(img + 2048 + t0 * 64 + c0 * 8), tr_read(img + 2048 + t1 * 64 + c1 * 8));
    }
    f[0] = wl[(2 * KSA + KSD) * 64];
#pragma unroll
    for (int s = 1; s < PD; ++s) f[s] = wl[(2 * KSA + s - 1) * 64];
    SCHED();
    kv0 = MFMA32(asfrag(pa[0]), asfrag(vfr[0]), kv0);
    kv1 = MFMA32(asfrag(pb[0]), asfrag(vfr[0]), kv1);
    kv0 = MFMA32(asfrag(pa[1]), asfrag(vfr[1]), kv0);
    kv1 = MFMA32(asfrag(pb[1]), asfrag(vfr[1]), kv1);
    SCHED();
#pragma unroll
    for (int s = 0; s < KSA; ++s) {
      if (s + PD < KSA) f[s + PD] = wl[(2 * KSA + s + PD - 1) * 64];
      aq = MFMA32(asfrag(f[s]), asfrag(s == 0 ? xa : xr[s == 0 ? 0 : s - 1]), aq);
      if (s == 1 || s == 2) {
        const f32x16& kk = (s == 1) ? kv0 : kv1;
#pragma unroll
        for (int g = 0; g < 4; ++g)
          red[((s - 1) * 4 + g) * 64] = f32x4{kk[4 * g], kk[4 * g + 1], kk[4 * g + 2], kk[4 * g + 3]};
      } else if (s == 3) {
        BARRIER();
      } else if (s >= 4 && s < 8) {
#pragma unroll
        for (int i = 2 * (s - 4); i < 2 * (s - 4) + 2; ++i)
#pragma unroll
          for (int o = 0; o < 4; ++o) rv[i][o] = redr[o * 2048 + i * 64];
      }
      if (s >= 6 && s < 10) {
        if (s < 9 || !(w & 1)) {
#pragma unroll
          for (int i = 2 * (s - 6); i < 2 * (s - 6) + 2; ++i)
            atomicAdd(dst + i * 64, opq(rv[i][0] + rv[i][1]) + opq(rv[i][2] + rv[i][3]));
        }
      }
      SCHED();
    }
  }
  STAMP_AT(1, 4);
  {
    f32x16 pos, neg;
    dual_softmax(aq, rs2, 1.f, hh, pos, neg);
    u32x4* dst = phiq + (size_t)((tb * H_ + h) * 4) * 64 + l;
    dst[0] = pack8(pos, 0);
    dst[64] = pack8(pos, 1);
    dst[128] = pack8(neg, 0);
    dst[192] = pack8(neg, 1);
  }
  STAMP_AT(1, 5);
  STAMP_AT(1, 6);
}

template <bool FINAL, bool BIAS>
DI void resid_epilogue(char* smem, const f32x16 (&acc)[2][2], int w, int l, int tb, int nt,
                       const u32x4 (&xo)[2], const u32x2 (&bs)[4],
                       u32x4* __restrict__ xout, f32x2* __restrict__ stout,
                       unsigned short* __restrict__ out) {
  const int r = l & 31, hh = l >> 5;
  f32x4* red = (f32x4*)smem;
  f32x16 own;
#pragma unroll
  for (int tI = 0; tI < 4; ++tI) {
    const f32x16& a = acc[tI >> 1][tI & 1];
    if (tI != w) {
#pragma unroll
      for (int g = 0; g < 4; ++g)
        red[((w * 4 + tI) * 4 + g) * 64 + l] = f32x4{a[4 * g], a[4 * g + 1], a[4 * g + 2], a[4 * g + 3]};
    } else {
      own = a;
    }
  }
  BARRIER();
  f32x16 v;
#pragma unroll
  for (int g = 0; g < 4; ++g) {
    f32x4 s = f32x4{own[4 * g], own[4 * g + 1], own[4 * g + 2], own[4 * g + 3]};
#pragma unroll
    for (int o = 1; o < 4; ++o) s += red[((((w + o) & 3) * 4 + w) * 4 + g) * 64 + l];
    const unsigned x0 = xo[g >> 1][(g & 1) * 2], x1 = xo[g >> 1][(g & 1) * 2 + 1];
    if (BIAS) {
      s[0] += bflo(bs[g][0]); s[1] += bfhi(bs[g][0]); s[2] += bflo(bs[g][1]); s[3] += bfhi(bs[g][1]);
    }
    v[4 * g + 0] = s[0] + bflo(x0);
    v[4 * g + 1] = s[1] + bfhi(x0);
    v[4 * g + 2] = s[2] + bflo(x1);
    v[4 * g + 3] = s[3] + bfhi(x1);
  }
  u32x4 p0 = pack8(v, 0), p1 = pack8(v, 1);
  if (FINAL) {
    unsigned short* o = out + (size_t)(tb * 32 + r) * D_ + 32 * nt + 4 * hh;
    *(u32x2*)(o + 0) = u32x2{p0[0], p0[1]};
    *(u32x2*)(o + 8) = u32x2{p0[2], p0[3]};
    *(u32x2*)(o + 16) = u32x2{p1[0], p1[1]};
    *(u32x2*)(o + 24) = u32x2{p1[2], p1[3]};
  } else {
    xout[(size_t)(tb * KSD + 2 * nt) * 64 + l] = p0;
    xout[(size_t)(tb * KSD + 2 * nt + 1) * 64 + l] = p1;
    float s1 = 0.f, s2 = 0.f;
#pragma unroll
    for (int j = 0; j < 4; ++j) {
      float a = bflo(p0[j]), c = bfhi(p0[j]), d = bflo(p1[j]), e = bfhi(p1[j]);
      s1 += (a + c) + (d + e);
      s2 += (a * a + c * c) + (d * d + e * e);
    }
    s1 = sum32(s1);
    s2 = sum32(s2);
    atomicAdd((float*)stout + (tb * 32 + r) * 2 + hh, hh ? s2 : s1);
  }
}

__global__ __launch_bounds__(256, 1) void k_attn_out(const u32x4* __restrict__ phiq,
                                                     const float* __restrict__ kvacc,
                                                     const u32x4* __restrict__ wo_t,
                                                     const u32x4* __restrict__ xin,
                                                     u32x4* __restrict__ xout, f32x2* __restrict__ stout) {
  __shared__ __attribute__((aligned(16))) char smem[64 * 1024];
  const int bid = blockIdx.x, xcd = bid & 7, bi = bid >> 3;
  const int tp = 4 * xcd + bi / 7, ns = bi % 7;
  const int tid = threadIdx.x, w = __builtin_amdgcn_readfirstlane(tid >> 6), l = tid & 63, r = l & 31;
  const int tbo = 2 * tp + (w >> 1), nto = 2 * ns + (w & 1);
  STAMP_AT(2, 0);
  f32x16 acc[2][2];
#pragma unroll
  for (int a = 0; a < 2; ++a)
#pragma unroll
    for (int b = 0; b < 2; ++b) acc[a][b] = zero16();
  u32x4 xo[2];
  u32x2 bs[4] = {};
  {
    f32x4 kva[4][8];
    u32x4 wf[4][2][2], pq[4][2][4];
    auto issue = [&](int hi) {
      const int hd = 4 * w + hi;
      const f32x4* kp = (const f32x4*)(kvacc + hd * 2048) + l;
#pragma unroll
      for (int i = 0; i < 8; ++i) kva[hi][i] = kp[i * 64];
      SCHED();
#pragma unroll
      for (int a = 0; a < 2; ++a)
#pragma unroll
        for (int i = 0; i < 4; ++i) pq[hi][a][i] = phiq[(size_t)(((2 * tp + a) * H_ + hd) * 4 + i) * 64 + l];
      SCHED();
#pragma unroll
      for (int sp = 0; sp < 2; ++sp)
#pragma unroll
        for (int nt = 0; nt < 2; ++nt) wf[hi][sp][nt] = wo_t[(size_t)((hd * 2 + sp) * NNT + 2 * ns + nt) * 64 + l];
    };
    issue(0); SCHED(); issue(1);
    SCHED();
#pragma unroll
    for (int hi = 0; hi < 4; ++hi) {
      u32x4 kvf[4];
#pragma unroll
      for (int i = 0; i < 4; ++i) {
        f32x4 a = kva[hi][2 * i], b = kva[hi][2 * i + 1];
        kvf[i] = u32x4{pack2(a[0], a[1]), pack2(a[2], a[3]), pack2(b[0], b[1]), pack2(b[2], b[3])};
      }
      SCHED();
      if (hi < 2) issue(hi + 2);
      if (hi == 2) {
        xo[0] = xin[(size_t)(tbo * KSD + 2 * nto) * 64 + l];
        xo[1] = xin[(size_t)(tbo * KSD + 2 * nto + 1) * 64 + l];
      }
      SCHED();
#pragma unroll
      for (int a = 0; a < 2; ++a) {
        f32x16 o = zero16();
#pragma unroll
        for (int i = 0; i < 4; ++i) o = MFMA32(asfrag(kvf[i]), asfrag(pq[hi][a][i]), o);
        float dlo, dhi;
        halves(o[12] + o[13], dlo, dhi);
        const float inv = 1.f / (dhi + 1e-6f);
#pragma unroll
        for (int i = 0; i < 16; ++i) o[i] *= inv;
        u32x4 of0 = pack8(o, 0), of1 = pack8(o, 1);
#pragma unroll
        for (int nt = 0; nt < 2; ++nt) {
          acc[a][nt] = MFMA32(asfrag(wf[hi][0][nt]), asfrag(of0), acc[a][nt]);
          acc[a][nt] = MFMA32(asfrag(wf[hi][1][nt]), asfrag(of1), acc[a][nt]);
        }
      }
    }
  }
  STAMP_AT(2, 1);
  resid_epilogue<false, false>(smem, acc, w, l, tbo, nto, xo, bs, xout, stout, nullptr);
  STAMP_AT(2, 2);
}

constexpr int K3_X  = 0;
constexpr int K3_LDS = 112 * 1024;

__global__ __launch_bounds__(256, 1) void k_ffn1(const u32x4* __restrict__ xf, const f32x2* __restrict__ st,
                                                 const u32x4* __restrict__ w1_t, u32x4* __restrict__ hf) {
  __shared__ __attribute__((aligned(16))) char smem[K3_LDS];
  const int bid = blockIdx.x, xcd = bid & 7, bi = bid >> 3;
  const int tt = 2 * xcd + bi / 14, fg = bi % 14;
  const int tid = threadIdx.x, w = __builtin_amdgcn_readfirstlane(tid >> 6), l = tid & 63, r = l & 31, hh = l >> 5;
  const int ft = 4 * fg + w;
  STAMP_AT(3, 0);
  const unsigned l16 = l * 16;
  const char* xg = (const char*)xf + (size_t)(tt * 4 * KSD + w * 7) * 1024;
  const char* wg = (const char*)w1_t + (size_t)ft * (KSA * 1024);
  const unsigned xw = lds_addr(smem) + K3_X + w * (7 * 1024);
  u32x4 wr[KSA];
  f32x2 stv[4];
  sfor<0, 4>([&](auto A_) {
    constexpr int a = decltype(A_)::v;
    gload2<a * 256>(stv[a], r * 8, st + tt * 128);
  });
  glds<4>(l16, xg, xw);
  glds<3>(l16, xg + 4096, xw + 4096);
  gload<0>(wr[0], l16, wg + KSD * 1024);
  sfor<0, KSD>([&](auto S_c) {
    constexpr int s = decltype(S_c)::v;
    gload<(s & 7) * 1024 - 4096>(wr[1 + s], l16, wg + (s >> 3) * 8192 + 4096);
  });
#pragma unroll
  for (int a = 1; a < 4; ++a) {
    glds<4>(l16, xg + a * (KSD * 1024), xw + a * (KSD * 1024));
    glds<3>(l16, xg + a * (KSD * 1024) + 4096, xw + a * (KSD * 1024) + 4096);
  }
  u32x4* xl = (u32x4*)(smem + K3_X) + l;
  u32x4 xa[4];
  float k0[4], k1[4], sd[4];
  {
    const float c0 = -2.f * 0.7978845608028654f * LOG2E;
    const float c1 = c0 * 0.044715f;
#pragma unroll
    for (int a = 0; a < 4; ++a) {
      vwait2<57>(stv[a]);
      float mean, rstd;
      finish_stats(stv[a], mean, rstd, sd[a]);
      xa[a] = aug_x(mean, sd[a], hh);
      k0[a] = c0 * rstd;
      k1[a] = c1 * rstd * rstd * rstd;
    }
  }
  vwait_mem<50>();
  BARRIER();
  STAMP_AT(3, 1);
  {
    constexpr int PD = 3;
    f32x16 acc[4];
    f32x16 u;
    sfor<0, 5>([&](auto A_) {
      constexpr int a = decltype(A_)::v;
      u32x4 xb[KSD];
      if constexpr (a < 4) {
        acc[a] = zero16();
#pragma unroll
        for (int s = 0; s < PD - 1; ++s) xb[s] = xl[(a * KSD + s) * 64];
      }
      sfor<0, KSA>([&](auto S_c) {
        constexpr int s = decltype(S_c)::v;
        if constexpr (a < 4) {
          if constexpr (s + PD - 1 < KSD) xb[s + PD - 1] = xl[(a * KSD + s + PD - 1) * 64];
          if constexpr (a == 0) vwait<21 + 28 - s>(wr[s]);
          if constexpr (s == 0) acc[a] = MFMA32(asfrag(wr[0]), asfrag(xa[a]), acc[a]);
          else acc[a] = MFMA32(asfrag(wr[s]), asfrag(xb[s - 1]), acc[a]);
        }
        if constexpr (a > 0) {
          constexpr int q = s - 1;
          if constexpr (q >= 0 && q < 28) {
            constexpr int k = q >> 2, g = q & 3;
            const f32x16& y = acc[a - 1];
#pragma unroll
            for (int i = 4 * g; i < 4 * g + 4; ++i) {
              if constexpr (k == 0) u[i] = opq(y[i] * y[i]);
              else if constexpr (k == 1) u[i] = opq(__builtin_fmaf(u[i], k1[a - 1], k0[a - 1]));
              else if constexpr (k == 2) u[i] = opq(u[i] * y[i]);
              else if constexpr (k == 3) u[i] = __builtin_amdgcn_exp2f(u[i]);
              else if constexpr (k == 4) u[i] = opq(__builtin_fmaf(u[i], sd[a - 1], sd[a - 1]));
              else if constexpr (k == 5) u[i] = __builtin_amdgcn_rcpf(u[i]);
              else if constexpr (k == 6) u[i] = opq(u[i] * y[i]);
            }
            if constexpr (k == 6 && (g & 1)) {
              u32x4* dst = hf + (size_t)((tt * 4 + a - 1) * KSF + 2 * ft) * 64 + l;
              dst[(g >> 1) * 64] = pack8(u, g >> 1);
            }
          }
        }
        if constexpr (a < 4) SCHED();
      });
      if constexpr (a < 3) {
        vwait_mem<14 - 7 * a + 2 * a>();
        BARRIER();
      }
      STAMP_AT(3, 2 + a);
    });
  }
}

template <bool FINAL>
__global__ __launch_bounds__(256, 1) void k_ffn2(const u32x4* __restrict__ hf, const u32x4* __restrict__ w2_t,
                                                 const unsigned short* __restrict__ b2,
                                                 const u32x4* __restrict__ xin, u32x4* __restrict__ xout,
                                                 f32x2* __restrict__ stout, unsigned short* __restrict__ out) {
  __shared__ __attribute__((aligned(16))) char smem[64 * 1024];
  const int bid = blockIdx.x, xcd = bid & 7, bi = bid >> 3;
  const int tp = 4 * xcd + bi / 7, ns = bi % 7;
  const int tid = threadIdx.x, w = __builtin_amdgcn_readfirstlane(tid >> 6), l = tid & 63, hh = l >> 5;
  const int tbo = 2 * tp + (w >> 1), nto = 2 * ns + (w & 1);
  STAMP_AT(4, 0);
  f32x16 acc[2][2];
#pragma unroll
  for (int a = 0; a < 2; ++a)
#pragma unroll
    for (int b = 0; b < 2; ++b) acc[a][b] = zero16();
  const u32x4* h0 = hf + (size_t)((2 * tp) * KSF + 28 * w) * 64 + l;
  const u32x4* h1 = h0 + (size_t)KSF * 64;
  const u32x4* w0 = w2_t + (size_t)((2 * ns) * KSF + 28 * w) * 64 + l;
  const u32x4* w1p = w0 + (size_t)KSF * 64;
  u32x4 xo[2];
  u32x2 bs[4];
  {
    constexpr int PD = 5;
    u32x4 a0[28], a1[28], b0[28], b1[28];
#pragma unroll
    for (int s = 0; s < PD; ++s) {
      a0[s] = w0[s * 64]; b0[s] = h0[s * 64]; a1[s] = w1p[s * 64]; b1[s] = h1[s * 64];
      SCHED();
    }
#pragma unroll
    for (int s = 0; s < 28; ++s) {
      if (s + PD < 28) {
        a0[s + PD] = w0[(s + PD) * 64]; b0[s + PD] = h0[(s + PD) * 64];
        a1[s + PD] = w1p[(s + PD) * 64]; b1[s + PD] = h1[(s + PD) * 64];
      }
      if (s + PD == 28) {
        xo[0] = xin[(size_t)(tbo * KSD + 2 * nto) * 64 + l];
        xo[1] = xin[(size_t)(tbo * KSD + 2 * nto + 1) * 64 + l];
#pragma unroll
        for (int g = 0; g < 4; ++g) bs[g] = *(const u32x2*)(b2 + 32 * nto + 8 * g + 4 * hh);
      }
      SCHED();
      acc[0][0] = MFMA32(asfrag(a0[s]), asfrag(b0[s]), acc[0][0]);
      acc[0][1] = MFMA32(asfrag(a1[s]), asfrag(b0[s]), acc[0][1]);
      acc[1][0] = MFMA32(asfrag(a0[s]), asfrag(b1[s]), acc[1][0]);
      acc[1][1] = MFMA32(asfrag(a1[s]), asfrag(b1[s]), acc[1][1]);
      SCHED();
    }
  }
  STAMP_AT(4, 1);
  resid_epilogue<FINAL, true>(smem, acc, w, l, tbo, nto, xo, bs, xout, stout, out);
  STAMP_AT(4, 2);
}

}

extern "C" void kernel_launch(void* const* d_in, const int* in_sizes, int n_in, void* d_out, int out_size,
                              void* d_ws, size_t ws_size, hipStream_t stream) {
  const unsigned short* x = (const unsigned short*)d_in[0];
  const unsigned short* wqkv = (const unsigned short*)d_in[1];
  const unsigned short* wphi = (const unsigned short*)d_in[2];
  const unsigned short* wo = (const unsigned short*)d_in[3];
  const unsigned short* w1 = (const unsigned short*)d_in[5];
  const unsigned short* b1 = (const unsigned short*)d_in[6];
  const unsigned short* w2 = (const unsigned short*)d_in[7];
  const unsigned short* b2 = (const unsigned short*)d_in[8];
  char* ws = (char*)d_ws;
  unsigned short* out = (unsigned short*)d_out;

  auto XF = [&](int i) { return (u32x4*)(ws + OFF_XF + (size_t)i * XF_BYTES); };
  auto ST = [&](int i) { return (f32x2*)(ws + OFF_ST + (size_t)i * ST_BYTES); };
  const u32x4* wqkv_t = (const u32x4*)(ws + OFF_WQKV);
  const u32x4* wo_t = (const u32x4*)(ws + OFF_WO);
  const u32x4* w1_t = (const u32x4*)(ws + OFF_W1);
  const u32x4* w2_t = (const u32x4*)(ws + OFF_W2);
  u32x4* phiq = (u32x4*)(ws + OFF_PHIQ);
  float* kv = (float*)(ws + OFF_KV);
  u32x4* hf = (u32x4*)(ws + OFF_HF);

  k_prep<<<256, 256, 0, stream>>>(x, wqkv, wphi, wo, w1, b1, w2, ws);
  for (int layer = 0; layer < 2; ++layer) {
    const int i0 = 2 * layer;
    float* kvl = kv + layer * H_ * 2048;
    k_qkv<<<256, 256, 0, stream>>>(XF(i0), ST(i0), wqkv_t, phiq, kvl);
    k_attn_out<<<224, 256, 0, stream>>>(phiq, kvl, wo_t, XF(i0), XF(i0 + 1), ST(i0 + 1));
    k_ffn1<<<224, 256, 0, stream>>>(XF(i0 + 1), ST(i0 + 1), w1_t, hf);
    if (layer == 0)
      k_ffn2<false><<<224, 256, 0, stream>>>(hf, w2_t, b2, XF(i0 + 1), XF(i0 + 2), ST(i0 + 2), nullptr);
    else
      k_ffn2<true><<<224, 256, 0, stream>>>(hf, w2_t, b2, XF(i0 + 1), nullptr, nullptr, out);
  }
}
```

```cpp
#include <hip/hip_runtime.h>
#include <hip/hip_bf16.h>
#include <stdint.h>

typedef __attribute__((ext_vector_type(8))) short bf16x8;
typedef __attribute__((ext_vector_type(4))) short s16x4;
typedef __attribute__((ext_vector_type(16))) float f32x16;
typedef __attribute__((ext_vector_type(4))) float f32x4;
typedef __attribute__((ext_vector_type(2))) float f32x2;
typedef __attribute__((ext_vector_type(4))) unsigned u32x4;
typedef __attribute__((ext_vector_type(2))) unsigned u32x2;
typedef __attribute__((ext_vector_type(2))) __bf16 bf16x2_t;

#define DI __device__ __forceinline__
#define SCHED() __builtin_amdgcn_sched_barrier(0)
#define PIN(x) asm volatile("" : "+a"(x))
#define PINV(x) asm volatile("" : "+v"(x))
#define MFMA32(a, b, c) __builtin_amdgcn_mfma_f32_32x32x16_bf16((a), (b), (c), 0, 0, 0)

#define STAMP_BEGIN()
#define STAMP_AT(kid, i)
#define STAMP_END(kid, n)

namespace {

constexpr int S_ = 2048, D_ = 448, H_ = 16, DH_ = 28, F_ = 1792;
constexpr int NTB = S_ / 32;
constexpr int KSD = D_ / 16;
constexpr int KSF = F_ / 16;
constexpr int NNT = D_ / 32;
constexpr int NFT = F_ / 32;
constexpr int KSA = KSD + 1;

constexpr size_t XF_BYTES   = (size_t)NTB * KSD * 1024;
constexpr size_t ST_BYTES   = (size_t)S_ * 8;
constexpr size_t OFF_XF     = 0;
constexpr size_t OFF_ST     = OFF_XF + 4 * XF_BYTES;
constexpr size_t OFF_WQKV   = OFF_ST + 4 * ST_BYTES;
constexpr size_t OFF_WO     = OFF_WQKV + (size_t)H_ * 3 * KSA * 1024;
constexpr size_t OFF_W1     = OFF_WO + (size_t)H_ * 2 * NNT * 1024;
constexpr size_t OFF_W2     = OFF_W1 + (size_t)NFT * KSA * 1024;
constexpr size_t OFF_PHIQ   = OFF_W2 + (size_t)NNT * KSF * 1024;
constexpr size_t OFF_KV     = OFF_PHIQ + (size_t)NTB * H_ * 4 * 1024;
constexpr size_t OFF_HF     = OFF_KV + (size_t)2 * H_ * 2048 * 4;
constexpr size_t WS_TOTAL   = OFF_HF + (size_t)NTB * KSF * 1024;

DI unsigned pack2(float a, float b) {
  f32x2 v = {a, b};
  bf16x2_t r = __builtin_convertvector(v, bf16x2_t);
  return __builtin_bit_cast(unsigned, r);
}
DI float bflo(unsigned u) { return __uint_as_float(u << 16); }
DI float bfhi(unsigned u) { return __uint_as_float(u & 0xffff0000u); }
DI float bf1(unsigned short u) { return __uint_as_float(((unsigned)u) << 16); }
DI bf16x8 asfrag(u32x4 v) { return __builtin_bit_cast(bf16x8, v); }
DI u32x4 pack8(const f32x16& x, int sp) {
  u32x4 p;
  p[0] = pack2(x[8 * sp + 0], x[8 * sp + 1]);
  p[1] = pack2(x[8 * sp + 2], x[8 * sp + 3]);
  p[2] = pack2(x[8 * sp + 4], x[8 * sp + 5]);
  p[3] = pack2(x[8 * sp + 6], x[8 * sp + 7]);
  return p;
}
DI float xor32(float v) { return __shfl_xor(v, 32, 64); }
DI void halves(float v, float& lo, float& hi) {
  auto r = __builtin_amdgcn_permlane32_swap(__float_as_uint(v), __float_as_uint(v), false, false);
  lo = __uint_as_float(r[0]);
  hi = __uint_as_float(r[1]);
}
DI float sum32(float v) { float a, b; halves(v, a, b); return a + b; }
DI float max32(float v) { float a, b; halves(v, a, b); return fmaxf(a, b); }
DI float min32(float v) { float a, b; halves(v, a, b); return fminf(a, b); }
DI float opq(float v) { asm("" : "+v"(v)); return v; }
DI f32x16 zero16() {
  f32x16 z;
#pragma unroll
  for (int i = 0; i < 16; ++i) z[i] = 0.f;
  return z;
}
template <int N>
DI void glds(unsigned voff, const void* sbase, unsigned lds) {
  unsigned keep;
  if constexpr (N == 4)
    asm volatile("s_mov_b32 %0, m0\n\ts_mov_b32 m0, %3\n\ts_nop 4\n\t"
                 "global_load_lds_dwordx4 %1, %2\n\tglobal_load_lds_dwordx4 %1, %2 offset:1024\n\t"
                 "global_load_lds_dwordx4 %1, %2 offset:2048\n\tglobal_load_lds_dwordx4 %1, %2 offset:3072\n\t"
                 "s_mov_b32 m0, %0" : "=&s"(keep) : "v"(voff), "s"(sbase), "s"(lds) : "memory");
  else if constexpr (N == 3)
    asm volatile("s_mov_b32 %0, m0\n\ts_mov_b32 m0, %3\n\ts_nop 4\n\t"
                 "global_load_lds_dwordx4 %1, %2\n\tglobal_load_lds_dwordx4 %1, %2 offset:1024\n\t"
                 "global_load_lds_dwordx4 %1, %2 offset:2048\n\t"
                 "s_mov_b32 m0, %0" : "=&s"(keep) : "v"(voff), "s"(sbase), "s"(lds) : "memory");
}
template <int OFF>
DI void gload(u32x4& d, unsigned voff, const void* sbase) {
  asm volatile("global_load_dwordx4 %0, %1, %2 offset:%3" : "=v"(d) : "v"(voff), "s"(sbase), "i"(OFF));
}
template <int OFF>
DI void gload2(f32x2& d, unsigned voff, const void* sbase) {
  asm volatile("s_nop 4\n\tglobal_load_dwordx2 %0, %1, %2 offset:%3" : "=v"(d) : "v"(voff), "s"(sbase), "i"(OFF));
}
template <int N> DI void vwait(u32x4& d) { asm volatile("s_waitcnt vmcnt(%1)" : "+v"(d) : "i"(N)); }
template <int N> DI void vwait2(f32x2& d) { asm volatile("s_waitcnt vmcnt(%1)" : "+v"(d) : "i"(N)); }
template <int N> DI void vwait_mem() { asm volatile("s_waitcnt vmcnt(%0)" :: "i"(N) : "memory"); }
DI unsigned lds_addr(const void* p) {
  return (unsigned)(uintptr_t)(const __attribute__((address_space(3))) char*)p;
}
DI unsigned bfbits(float v) { return pack2(v, 0.f) & 0xffffu; }
DI void split3(float v, unsigned& p1, unsigned& p2, unsigned& p3) {
  p1 = bfbits(v);
  const float r1 = v - bflo(p1);
  p2 = bfbits(r1);
  p3 = bfbits(r1 - bflo(p2));
}
DI u32x4 aug_w(float cs) {
  unsigned c1, c2, c3;
  split3(cs, c1, c2, c3);
  return u32x4{c1 | (c2 << 16), c3 | (c1 << 16), c2 | (c1 << 16), 0u};
}
DI u32x4 aug_x(float mean, float sd, int hh) {
  unsigned a, b, c;
  split3(hh ? sd : -mean, a, b, c);
  u32x4 lo = {a | (a << 16), a | (b << 16), b | (c << 16), 0u};
  u32x4 hi = {a | (b << 16), c, 0u, 0u};
  return hh ? hi : lo;
}

template <int I> struct IC { static constexpr int v = I; };
template <int B, int E, class Fn>
DI void sfor(Fn&& f) {
  if constexpr (B < E) {
    f(IC<B>{});
    sfor<B + 1, E>(f);
  }
}

#define BARRIER() asm volatile("s_waitcnt lgkmcnt(0)\n\ts_barrier" ::: "memory")

typedef __attribute__((address_space(3))) s16x4* lds_s16x4p;
typedef const __attribute__((address_space(3))) u32x4* lds_u32x4p;
DI s16x4 tr_read(const char* p) { return __builtin_amdgcn_ds_read_tr16_b64_v4i16((lds_s16x4p)p); }
DI u32x4 join(s16x4 a, s16x4 b) {
  bf16x8 v = __builtin_shufflevector(a, b, 0, 1, 2, 3, 4, 5, 6, 7);
  return __builtin_bit_cast(u32x4, v);
}

DI u32x4 frag_from_image(const char* img, int row_stride, int s, int l, int cb) {
  const int i16 = l & 15, q4 = i16 >> 2, p4 = i16 & 3, blk = (l >> 4) & 1, hh = l >> 5;
  const char* p = img + (16 * s + 4 * hh + q4) * row_stride + cb + (4 * blk + p4) * 8;
  return join(tr_read(p), tr_read(p + 8 * row_stride));
}

__global__ __launch_bounds__(256) void k_prep(const unsigned short* __restrict__ x,
                                              const unsigned short* __restrict__ wqkv,
                                              const unsigned short* __restrict__ wphi,
                                              const unsigned short* __restrict__ wo,
                                              const unsigned short* __restrict__ w1,
                                              const unsigned short* __restrict__ b1,
                                              const unsigned short* __restrict__ w2,
                                              char* __restrict__ ws) {
  __shared__ __attribute__((aligned(16))) char img[32 * 912 + 1024];
  float (*red)[32] = (float (*)[32])(img + 32 * 912);
  const int b = blockIdx.x, tid = threadIdx.x, w = tid >> 6, l = tid & 63, r = l & 31, hh = l >> 5;
  ((float*)(ws + OFF_KV))[b * 256 + tid] = 0.f;

  if (b < 32) {
    const int h = b >> 1, p = b & 1;
    const int cc = r < DH_ ? r : r - DH_;
    u32x4 bfr[2];
    u32x2 lo[4][2], hi[4][2];
#pragma unroll
    for (int it = 0; it < 4; ++it) {
      const int blk = min(w + 4 * it, 13);
      const unsigned short* row = wqkv + (size_t)(32 * blk + r) * (3 * D_) + p * D_ + DH_ * h;
#pragma unroll
      for (int s2 = 0; s2 < 2; ++s2) {
        const int e0 = 16 * s2 + 8 * hh;
        lo[it][s2] = *(const u32x2*)(row + e0);
        hi[it][s2] = *(const u32x2*)(row + min(e0 + 4, 24));
        if (e0 + 4 >= DH_) hi[it][s2] = u32x2{0u, 0u};
      }
    }
#pragma unroll
    for (int s2 = 0; s2 < 2; ++s2) {
      unsigned short v[8];
#pragma unroll
      for (int j = 0; j < 8; ++j) {
        const int e = 16 * s2 + 8 * hh + j;
        v[j] = wphi[(h * DH_ + min(e, DH_ - 1)) * DH_ + cc];
        if (e >= DH_) v[j] = 0;
      }
#pragma unroll
      for (int j = 0; j < 4; ++j) bfr[s2][j] = (unsigned)v[2 * j] | ((unsigned)v[2 * j + 1] << 16);
    }
    float cs = 0.f;
    u32x4* dst = (u32x4*)(ws + OFF_WQKV) + (size_t)(h * 3 + (p ? 0 : 2)) * KSA * 64;
#pragma unroll
    for (int it = 0; it < 4; ++it) {
      const int blk = w + 4 * it;
      f32x16 acc = zero16();
#pragma unroll
      for (int s2 = 0; s2 < 2; ++s2) {
        u32x4 a = {lo[it][s2][0], lo[it][s2][1], hi[it][s2][0], hi[it][s2][1]};
        acc = MFMA32(asfrag(a), asfrag(bfr[s2]), acc);
      }
      if (blk < 14) {
#pragma unroll
        for (int sp = 0; sp < 2; ++sp) {
          u32x4 pk = pack8(acc, sp);
          dst[(2 * blk + sp) * 64 + l] = pk;
#pragma unroll
          for (int j = 0; j < 4; ++j) cs += bflo(pk[j]) + bfhi(pk[j]);
        }
      }
    }
    cs += xor32(cs);
    if (hh == 0) red[w][r] = cs;
    __syncthreads();
    if (tid < 64) {
      u32x4 a = aug_w(red[0][r] + red[1][r] + red[2][r] + red[3][r]);
      if (hh) a = u32x4{0u, 0u, 0u, 0u};
      dst[KSD * 64 + l] = a;
    }
  } else if (b < 48 || (b >= 64 && b < 176)) {
    u32x4* dst;
    bool aug = true;
    u32x4 aughi = {0u, 0u, 0u, 0u};
    if (b < 48) {
      const int h = b - 32;
      dst = (u32x4*)(ws + OFF_WQKV) + (size_t)(h * 3 + 1) * KSA * 64;
      aughi[0] = (r == DH_) ? 0x3f80u : (r == DH_ + 1) ? 0x3f800000u : 0u;
      u32x2 v[14];
#pragma unroll
      for (int i = 0; i < 14; ++i) {
        const int idx = tid + 256 * i, row = idx >> 3, c8 = idx & 7;
        v[i] = *(const u32x2*)(wqkv + (size_t)row * (3 * D_) + 2 * D_ + DH_ * h + 4 * min(c8, 6));
        if (c8 == 7) v[i] = u32x2{0u, 0u};
      }
#pragma unroll
      for (int i = 0; i < 14; ++i) *(u32x2*)(img + (tid + 256 * i) * 8) = v[i];
    } else {
      const unsigned short* src;
      int ld;
      if (b < 120) {
        const int ft = b - 64;
        dst = (u32x4*)(ws + OFF_W1) + (size_t)ft * KSA * 64;
        const unsigned bb = b1[32 * ft + r];
        aughi[0] = bb | (bb << 16);
        aughi[1] = bb;
        src = w1 + 32 * ft;
        ld = F_;
      } else {
        const int bb = b - 120, nt = bb >> 2, qd = bb & 3;
        dst = (u32x4*)(ws + OFF_W2) + (size_t)(nt * KSF + qd * 28) * 64;
        aug = false;
        src = w2 + (size_t)qd * 448 * D_ + 32 * nt;
        ld = D_;
      }
      u32x4 v[7];
#pragma unroll
      for (int i = 0; i < 7; ++i) {
        const int idx = tid + 256 * i, row = idx >> 2, c = idx & 3;
        v[i] = *(const u32x4*)(src + (size_t)row * ld + 8 * c);
      }
#pragma unroll
      for (int i = 0; i < 7; ++i) *(u32x4*)(img + (tid + 256 * i) * 16) = v[i];
    }
    __syncthreads();
    float cs = 0.f;
#pragma unroll
    for (int i = 0; i < 7; ++i) {
      const int s = w * 7 + i;
      u32x4 pk = frag_from_image(img, 64, s, l, 0);
      dst[s * 64 + l] = pk;
#pragma unroll
      for (int j = 0; j < 4; ++j) cs += bflo(pk[j]) + bfhi(pk[j]);
    }
    if (aug) {
      cs += xor32(cs);
      if (hh == 0) red[w][r] = cs;
      __syncthreads();
      if (tid < 64) {
        u32x4 a = aug_w(red[0][r] + red[1][r] + red[2][r] + red[3][r]);
        dst[KSD * 64 + l] = hh ? aughi : a;
      }
    }
  } else if (b < 64) {
    const int h = b - 48;
    u32x4 v[7];
#pragma unroll
    for (int i = 0; i < 7; ++i) {
      const int idx = tid + 256 * i;
      v[i] = *(const u32x4*)(wo + (size_t)DH_ * h * D_ + min(idx, 28 * 56 - 1) * 8);
      if (idx >= 28 * 56) v[i] = u32x4{0u, 0u, 0u, 0u};
    }
#pragma unroll
    for (int i = 0; i < 7; ++i) *(u32x4*)(img + (tid + 256 * i) * 16) = v[i];
    __syncthreads();
    u32x4* dst = (u32x4*)(ws + OFF_WO) + (size_t)h * 28 * 64;
#pragma unroll
    for (int i = 0; i < 7; ++i) {
      const int fi = w * 7 + i, ks = fi / NNT, nt = fi % NNT;
      dst[fi * 64 + l] = frag_from_image(img, 896, ks, l, 64 * nt);
    }
  } else if (b < 240) {
    const int tb = b - 176;
    u32x4 v[7];
#pragma unroll
    for (int i = 0; i < 7; ++i) v[i] = *(const u32x4*)(x + (size_t)tb * 32 * D_ + (tid + 256 * i) * 8);
#pragma unroll
    for (int i = 0; i < 7; ++i) {
      const int idx = tid + 256 * i, row = idx / 56, c = idx % 56;
      *(u32x4*)(img + row * 912 + c * 16) = v[i];
    }
    __syncthreads();
    u32x4* dst = (u32x4*)(ws + OFF_XF) + (size_t)tb * KSD * 64;
    float s1 = 0.f, s2 = 0.f;
#pragma unroll
    for (int i = 0; i < 7; ++i) {
      const int s = w * 7 + i;
      const char* p = img + r * 912 + 32 * s + 8 * hh;
      u32x2 lo = *(const u32x2*)p;
      u32x2 hi = *(const u32x2*)(p + 16);
      u32x4 pk = {lo[0], lo[1], hi[0], hi[1]};
      dst[s * 64 + l] = pk;
#pragma unroll
      for (int j = 0; j < 4; ++j) {
        float a = bflo(pk[j]), c = bfhi(pk[j]);
        s1 += a + c;
        s2 += a * a + c * c;
      }
    }
    s1 = sum32(s1);
    s2 = sum32(s2);
    float* red2 = (float*)(img + 32 * 912);
    red2[w * 64 + l] = hh ? s2 : s1;
    __syncthreads();
    if (tid < 64) {
      const float v = red2[tid] + red2[64 + tid] + red2[128 + tid] + red2[192 + tid];
      ((float*)(ws + OFF_ST))[(tb * 32 + r) * 2 + hh] = v;
    }
  } else {
    float* z = (float*)(ws + OFF_ST + ST_BYTES) + (b - 240) * 768 + tid;
    z[0] = 0.f; z[256] = 0.f; z[512] = 0.f;
  }
}

DI void finish_stats(f32x2 v, float& mean, float& rstd, float& sd) {
  mean = v[0] * (1.f / D_);
  const float var = fmaxf(v[1] * (1.f / D_) - mean * mean, 0.f) + 1e-5f;
  rstd = rsqrtf(var);
  sd = var * rstd;
}

constexpr float LOG2E = 1.4426950408889634f;
DI float max3(float a, float b, float c) { return fmaxf(fmaxf(a, b), c); }
DI float min3(float a, float b, float c) { return fminf(fminf(a, b), c); }

struct Smx {
  f32x16 pos, neg;
  float mx, mn, bp, bn, sp, sn, ip, in;
  u32x2 pk[4], nk[4];
};
constexpr int SMX_SLOTS = 34;
template <int Q>
DI void smx_slot(Smx& z, const f32x16& y, float rs2, float scale, int hh) {
  if constexpr (Q == 0) {
    z.mx = fmaxf(max3(y[0], y[1], y[2]), y[3]);
    z.mn = fminf(min3(y[0], y[1], y[2]), y[3]);
  } else if constexpr (Q < 4) {
    z.mx = max3(max3(z.mx, y[4 * Q], y[4 * Q + 1]), y[4 * Q + 2], y[4 * Q + 3]);
    z.mn = min3(min3(z.mn, y[4 * Q], y[4 * Q + 1]), y[4 * Q + 2], y[4 * Q + 3]);
  } else if constexpr (Q == 4) {
    z.bp = -max32(z.mx) * rs2;
    z.bn = min32(z.mn) * rs2;
  } else if constexpr (Q < 13) {
    constexpr int g = (Q - 5) >> 1;
#pragma unroll
    for (int i = 4 * g; i < 4 * g + 4; ++i) {
      if constexpr ((Q - 5) & 1) z.neg[i] = opq(__builtin_fmaf(y[i], -rs2, z.bn));
      else z.pos[i] = opq(__builtin_fmaf(y[i], rs2, z.bp));
    }
  } else if constexpr (Q < 21) {
    constexpr int g = (Q - 13) >> 1;
#pragma unroll
    for (int i = 4 * g; i < 4 * g + 4; ++i) {
      if constexpr ((Q - 13) & 1) z.neg[i] = __builtin_amdgcn_exp2f(z.neg[i]);
      else z.pos[i] = __builtin_amdgcn_exp2f(z.pos[i]);
    }
  } else if constexpr (Q < 25) {
    constexpr int g = Q - 21;
    float a = opq(opq(z.pos[4 * g] + z.pos[4 * g + 1]) + opq(z.pos[4 * g + 2] + z.pos[4 * g + 3]));
    float c = opq(opq(z.neg[4 * g] + z.neg[4 * g + 1]) + opq(z.neg[4 * g + 2] + z.neg[4 * g + 3]));
    if constexpr (g == 3) { a = hh ? 0.f : a; c = hh ? 0.f : c; }
    if constexpr (g == 0) { z.sp = a; z.sn = c; }
    else { z.sp = opq(z.sp + a); z.sn = opq(z.sn + c); }
  } else if constexpr (Q == 25) {
    z.ip = __builtin_amdgcn_rcpf(sum32(z.sp)) * scale;
    z.in = __builtin_amdgcn_rcpf(sum32(z.sn)) * scale;
  } else {
    constexpr int g = (Q - 26) >> 1;
    if constexpr ((Q - 26) & 1) {
      const float f = (g == 3 && hh) ? 0.f : z.in;
      z.nk[g] = u32x2{pack2(opq(z.neg[4 * g] * f), opq(z.neg[4 * g + 1] * f)),
                      pack2(opq(z.neg[4 * g + 2] * f), opq(z.neg[4 * g + 3] * f))};
    } else {
      const float f = (g == 3 && hh) ? 0.f : z.ip;
      z.pk[g] = u32x2{pack2(opq(z.pos[4 * g] * f), opq(z.pos[4 * g + 1] * f)),
                      pack2(opq(z.pos[4 * g + 2] * f), opq(z.pos[4 * g + 3] * f))};
    }
  }
}

constexpr int K1_W    = 0;
constexpr int K1_IMG  = K1_W + 88 * 1024;
constexpr int K1_RED  = K1_IMG + 16 * 1024;
constexpr int K1_LDS  = K1_RED + 32 * 1024;
#define K1_SIG 14

__global__ __launch_bounds__(512, 2) void k_qkv(const u32x4* __restrict__ xf, const f32x2* __restrict__ st,
                                                const u32x4* __restrict__ wqkv_t,
                                                u32x4* __restrict__ phiq, float* __restrict__ kvacc) {
  __shared__ __attribute__((aligned(16))) char smem[K1_LDS];
  const int bid = blockIdx.x, xcd = bid & 7, bi = bid >> 3;
  const int t = 4 * (xcd >> 1) + (bi >> 3), h = 8 * (xcd & 1) + (bi & 7);
  const int tid = threadIdx.x, w8 = __builtin_amdgcn_readfirstlane(tid >> 6), w = w8 & 3;
  const int l = tid & 63, r = l & 31, hh = l >> 5;
  const int tb = t * 4 + w;
  const unsigned l16 = l * 16;
  const char* wb = (const char*)wqkv_t + (size_t)h * (3 * KSA * 1024);
  if (w8 >= 4) {
    asm volatile("s_barrier\n\ts_barrier" ::: "memory");
    glds<4>(l16, wb + (60 + 7 * w) * 1024, lds_addr(smem) + K1_W + (60 + 7 * w) * 1024);
    glds<3>(l16, wb + (64 + 7 * w) * 1024, lds_addr(smem) + K1_W + (64 + 7 * w) * 1024);
    vwait_mem<0>();
    asm volatile("s_barrier" ::: "memory");
    return;
  }
  STAMP_BEGIN();
  STAMP_AT(1, 0);
  const char* xb = (const char*)xf + (size_t)tb * (KSD * 1024);
  u32x4 xr[KSD];
  f32x2 stv;
  gload2<0>(stv, r * 8, st + tb * 32);
  {
    const char* g = wb + w * (15 * 1024);
    const unsigned lw = lds_addr(smem) + K1_W + w * (15 * 1024);
    glds<4>(l16, g, lw);
    glds<4>(l16, g + 4096, lw + 4096);
    glds<4>(l16, g + 8192, lw + 8192);
    glds<3>(l16, g + 12288, lw + 12288);
  }
  constexpr int X0 = 10;
  auto xload = [&](auto S_c) {
    constexpr int s = decltype(S_c)::v;
    gload<(s & 7) * 1024 - 4096>(xr[s], l16, xb + (s >> 3) * 8192 + 4096);
  };
  sfor<0, X0>(xload);
  u32x4* wl = (u32x4*)(smem + K1_W) + l;
  char* img = smem + K1_IMG + w * 4096;
  vwait2<15 + X0>(stv);
  float mean, rstd, sd;
  finish_stats(stv, mean, rstd, sd);
  u32x4 xa = aug_x(mean, sd, hh);
  float rs2 = rstd * LOG2E;
  unsigned qoff = lds_addr(smem) + K1_W + 2 * KSA * 1024 + l16;
  asm volatile("" : "+v"(xa), "+v"(rs2), "+v"(qoff));
  const lds_u32x4p wql = (lds_u32x4p)qoff;
  vwait_mem<X0>();
  BARRIER();
  STAMP_AT(1, 1);
  f32x16 ak = zero16(), av = zero16(), aq = zero16();
  {
    constexpr int PD = 2;
    u32x4 fk[KSA], fv[KSA];
    fk[KSD] = wl[KSD * 64];
    fv[KSD] = wl[(KSA + KSD) * 64];
#pragma unroll
    for (int s = 0; s < PD; ++s) { fk[s] = wl[s * 64]; fv[s] = wl[(KSA + s) * 64]; }
    ak = MFMA32(asfrag(fk[KSD]), asfrag(xa), ak);
    av = MFMA32(asfrag(xa), asfrag(fv[KSD]), av);
    SCHED();
    sfor<0, KSD>([&](auto S_c) {
      constexpr int s = decltype(S_c)::v;
      constexpr int NS = (KSD - X0) / 2;
      if constexpr (s < NS) sfor<X0 + 2 * s, X0 + 2 * s + 2>(xload);
      if constexpr (s == K1_SIG) asm volatile("s_barrier" ::: "memory");
      constexpr int issued = (s < NS ? X0 + 2 * s + 2 : KSD);
      if constexpr (s + PD < KSD) { fk[s + PD] = wl[(s + PD) * 64]; fv[s + PD] = wl[(KSA + s + PD) * 64]; }
      vwait<issued - 1 - s>(xr[s]);
      ak = MFMA32(asfrag(fk[s]), asfrag(xr[s]), ak);
      av = MFMA32(asfrag(xr[s]), asfrag(fv[s]), av);
      PINV(ak); PINV(av);
      SCHED();
    });
  }
  STAMP_AT(1, 2);
  u32x4 vfr[2];
  {
    Smx z;
    sfor<0, 5>([&](auto Q_) { smx_slot<decltype(Q_)::v>(z, ak, rs2, rstd, hh); });
    vfr[0] = pack8(av, 0);
    vfr[1] = pack8(av, 1);
    SCHED();
    asm volatile("s_barrier" ::: "memory");
    constexpr int PD = 3;
    u32x4 f[KSA];
    f[0] = wql[KSD * 64];
#pragma unroll
    for (int s = 1; s < PD; ++s) f[s] = wql[(s - 1) * 64];
    sfor<0, KSA>([&](auto S_c) {
      constexpr int s = decltype(S_c)::v;
      if constexpr (s + PD < KSA) f[s + PD] = wql[(s + PD - 1) * 64];
      smx_slot<5 + s>(z, ak, rs2, rstd, hh);
      if constexpr (5 + s >= 26) {
        constexpr int g = (5 + s - 26) >> 1;
        const int ch = (2 * g + hh) ^ ((r >> 1) & 7);
        if constexpr ((5 + s - 26) & 1) *(u32x2*)(img + 2048 + r * 64 + ch * 8) = z.nk[g];
        else *(u32x2*)(img + r * 64 + ch * 8) = z.pk[g];
      }
      if constexpr (s == 0) aq = MFMA32(asfrag(f[0]), asfrag(xa), aq);
      else aq = MFMA32(asfrag(f[s]), asfrag(xr[s - 1]), aq);
      PINV(aq);
      SCHED();
    });
  }
  STAMP_AT(1, 3);
  {
    Smx z;
    f32x16 kv0 = zero16(), kv1 = zero16();
    u32x4 pa[2], pb[2];
    float rv[8][4];
    const int i16 = l & 15, q4 = i16 >> 2, p4 = i16 & 3, blk = (l >> 4) & 1;
    f32x4* red = (f32x4*)(smem + K1_RED) + w * 512 + l;
    const float* redr = (const float*)(smem + K1_RED) + w * 512 + l;
    float* dst = kvacc + h * 2048 + w * 512 + l;
    auto slots = [&](auto B_, auto E_) {
      sfor<decltype(B_)::v, decltype(E_)::v>([&](auto Q_) { smx_slot<decltype(Q_)::v>(z, aq, rs2, 1.f, hh); });
      SCHED();
    };
#pragma unroll
    for (int s2 = 0; s2 < 2; ++s2) {
      const int t0 = 16 * s2 + 4 * hh + q4, t1 = t0 + 8;
      const int c0 = (4 * blk + p4) ^ ((t0 >> 1) & 7), c1 = (4 * blk + p4) ^ ((t1 >> 1) & 7);
      pa[s2] = join(tr_read(img + t0 * 64 + c0 * 8), tr_read(img + t1 * 64 + c1 * 8));
      pb[s2] = join(tr_read(img + 2048 + t0 * 64 + c0 * 8), tr_read(img + 2048 + t1 * 64 + c1 * 8));
    }
    SCHED();
    slots(IC<0>{}, IC<5>{});
    kv0 = MFMA32(asfrag(pa[0]), asfrag(vfr[0]), kv0);
    kv1 = MFMA32(asfrag(pb[0]), asfrag(vfr[0]), kv1);
    slots(IC<5>{}, IC<7>{});
    kv0 = MFMA32(asfrag(pa[1]), asfrag(vfr[1]), kv0);
    kv1 = MFMA32(asfrag(pb[1]), asfrag(vfr[1]), kv1);
    slots(IC<7>{}, IC<11>{});
#pragma unroll
    for (int g = 0; g < 4; ++g) red[g * 64] = f32x4{kv0[4 * g], kv0[4 * g + 1], kv0[4 * g + 2], kv0[4 * g + 3]};
#pragma unroll
    for (int g = 0; g < 4; ++g) red[(4 + g) * 64] = f32x4{kv1[4 * g], kv1[4 * g + 1], kv1[4 * g + 2], kv1[4 * g + 3]};
    SCHED();
    slots(IC<11>{}, IC<15>{});
    BARRIER();
#pragma unroll
    for (int i = 0; i < 8; ++i)
#pragma unroll
      for (int o = 0; o < 4; ++o) rv[i][o] = redr[o * 2048 + i * 64];
    SCHED();
    slots(IC<15>{}, IC<19>{});
#pragma unroll
    for (int i = 0; i < 6; ++i) atomicAdd(dst + i * 64, opq(rv[i][0] + rv[i][1]) + opq(rv[i][2] + rv[i][3]));
    if (!(w & 1)) {
#pragma unroll
      for (int i = 6; i < 8; ++i) atomicAdd(dst + i * 64, opq(rv[i][0] + rv[i][1]) + opq(rv[i][2] + rv[i][3]));
    }
    SCHED();
    STAMP_AT(1, 4);
    slots(IC<19>{}, IC<SMX_SLOTS>{});
    u32x4* qd = phiq + (size_t)((tb * H_ + h) * 4) * 64 + l;
    qd[0] = u32x4{z.pk[0][0], z.pk[0][1], z.pk[1][0], z.pk[1][1]};
    qd[64] = u32x4{z.pk[2][0], z.pk[2][1], z.pk[3][0], z.pk[3][1]};
    qd[128] = u32x4{z.nk[0][0], z.nk[0][1], z.nk[1][0], z.nk[1][1]};
    qd[192] = u32x4{z.nk[2][0], z.nk[2][1], z.nk[3][0], z.nk[3][1]};
  }
  STAMP_AT(1, 5);
  STAMP_AT(1, 6);
  STAMP_END(1, 7);
}

template <bool FINAL, bool BIAS>
DI void resid_epilogue(char* smem, const f32x16 (&acc)[2][2], int w, int l, int tb, int nt,
                       const u32x4 (&xo)[2], const u32x2 (&bs)[4],
                       u32x4* __restrict__ xout, f32x2* __restrict__ stout,
                       unsigned short* __restrict__ out) {
  const int r = l & 31, hh = l >> 5;
  f32x4* red = (f32x4*)smem;
  f32x16 own;
#pragma unroll
  for (int tI = 0; tI < 4; ++tI) {
    const f32x16& a = acc[tI >> 1][tI & 1];
    if (tI != w) {
#pragma unroll
      for (int g = 0; g < 4; ++g)
        red[((w * 4 + tI) * 4 + g) * 64 + l] = f32x4{a[4 * g], a[4 * g + 1], a[4 * g + 2], a[4 * g + 3]};
    } else {
      own = a;
    }
  }
  BARRIER();
  f32x16 v;
#pragma unroll
  for (int g = 0; g < 4; ++g) {
    f32x4 s = f32x4{own[4 * g], own[4 * g + 1], own[4 * g + 2], own[4 * g + 3]};
#pragma unroll
    for (int o = 1; o < 4; ++o) s += red[((((w + o) & 3) * 4 + w) * 4 + g) * 64 + l];
    const unsigned x0 = xo[g >> 1][(g & 1) * 2], x1 = xo[g >> 1][(g & 1) * 2 + 1];
    if (BIAS) {
      s[0] += bflo(bs[g][0]); s[1] += bfhi(bs[g][0]); s[2] += bflo(bs[g][1]); s[3] += bfhi(bs[g][1]);
    }
    v[4 * g + 0] = s[0] + bflo(x0);
    v[4 * g + 1] = s[1] + bfhi(x0);
    v[4 * g + 2] = s[2] + bflo(x1);
    v[4 * g + 3] = s[3] + bfhi(x1);
  }
  u32x4 p0 = pack8(v, 0), p1 = pack8(v, 1);
  if (FINAL) {
    unsigned short* o = out + (size_t)(tb * 32 + r) * D_ + 32 * nt + 4 * hh;
    *(u32x2*)(o + 0) = u32x2{p0[0], p0[1]};
    *(u32x2*)(o + 8) = u32x2{p0[2], p0[3]};
    *(u32x2*)(o + 16) = u32x2{p1[0], p1[1]};
    *(u32x2*)(o + 24) = u32x2{p1[2], p1[3]};
  } else {
    xout[(size_t)(tb * KSD + 2 * nt) * 64 + l] = p0;
    xout[(size_t)(tb * KSD + 2 * nt + 1) * 64 + l] = p1;
    float s1 = 0.f, s2 = 0.f;
#pragma unroll
    for (int j = 0; j < 4; ++j) {
      float a = bflo(p0[j]), c = bfhi(p0[j]), d = bflo(p1[j]), e = bfhi(p1[j]);
      s1 += (a + c) + (d + e);
      s2 += (a * a + c * c) + (d * d + e * e);
    }
    s1 = sum32(s1);
    s2 = sum32(s2);
    atomicAdd((float*)stout + (tb * 32 + r) * 2 + hh, hh ? s2 : s1);
  }
}

__global__ __launch_bounds__(256, 1) void k_attn_out(const u32x4* __restrict__ phiq,
                                                     const float* __restrict__ kvacc,
                                                     const u32x4* __restrict__ wo_t,
                                                     const u32x4* __restrict__ xin,
                                                     u32x4* __restrict__ xout, f32x2* __restrict__ stout) {
  __shared__ __attribute__((aligned(16))) char smem[64 * 1024];
  const int bid = blockIdx.x, xcd = bid & 7, bi = bid >> 3;
  const int tp = 4 * xcd + bi / 7, ns = bi % 7;
  const int tid = threadIdx.x, w = __builtin_amdgcn_readfirstlane(tid >> 6), l = tid & 63, r = l & 31;
  const int tbo = 2 * tp + (w >> 1), nto = 2 * ns + (w & 1);
  STAMP_BEGIN();
  STAMP_AT(2, 0);
  f32x16 acc[2][2];
#pragma unroll
  for (int a = 0; a < 2; ++a)
#pragma unroll
    for (int b = 0; b < 2; ++b) acc[a][b] = zero16();
  u32x4 xo[2];
  u32x2 bs[4] = {};
  {
    f32x4 kva[4][8];
    u32x4 wf[4][2][2], pq[4][2][4];
    auto issue = [&](int hi) {
      const int hd = 4 * w + hi;
      const f32x4* kp = (const f32x4*)(kvacc + hd * 2048) + l;
#pragma unroll
      for (int i = 0; i < 8; ++i) kva[hi][i] = kp[i * 64];
      SCHED();
#pragma unroll
      for (int a = 0; a < 2; ++a)
#pragma unroll
        for (int i = 0; i < 4; ++i) pq[hi][a][i] = phiq[(size_t)(((2 * tp + a) * H_ + hd) * 4 + i) * 64 + l];
      SCHED();
#pragma unroll
      for (int sp = 0; sp < 2; ++sp)
#pragma unroll
        for (int nt = 0; nt < 2; ++nt) wf[hi][sp][nt] = wo_t[(size_t)((hd * 2 + sp) * NNT + 2 * ns + nt) * 64 + l];
    };
    issue(0); SCHED(); issue(1);
    SCHED();
#pragma unroll
    for (int hi = 0; hi < 4; ++hi) {
      u32x4 kvf[4];
#pragma unroll
      for (int i = 0; i < 4; ++i) {
        f32x4 a = kva[hi][2 * i], b = kva[hi][2 * i + 1];
        kvf[i] = u32x4{pack2(a[0], a[1]), pack2(a[2], a[3]), pack2(b[0], b[1]), pack2(b[2], b[3])};
      }
      SCHED();
      if (hi < 2) issue(hi + 2);
      if (hi == 2) {
        xo[0] = xin[(size_t)(tbo * KSD + 2 * nto) * 64 + l];
        xo[1] = xin[(size_t)(tbo * KSD + 2 * nto + 1) * 64 + l];
      }
      SCHED();
#pragma unroll
      for (int a = 0; a < 2; ++a) {
        f32x16 o = zero16();
#pragma unroll
        for (int i = 0; i < 4; ++i) o = MFMA32(asfrag(kvf[i]), asfrag(pq[hi][a][i]), o);
        float dlo, dhi;
        halves(o[12] + o[13], dlo, dhi);
        const float inv = 1.f / (dhi + 1e-6f);
#pragma unroll
        for (int i = 0; i < 16; ++i) o[i] *= inv;
        u32x4 of0 = pack8(o, 0), of1 = pack8(o, 1);
#pragma unroll
        for (int nt = 0; nt < 2; ++nt) {
          acc[a][nt] = MFMA32(asfrag(wf[hi][0][nt]), asfrag(of0), acc[a][nt]);
          acc[a][nt] = MFMA32(asfrag(wf[hi][1][nt]), asfrag(of1), acc[a][nt]);
        }
      }
    }
  }
  STAMP_AT(2, 1);
  resid_epilogue<false, false>(smem, acc, w, l, tbo, nto, xo, bs, xout, stout, nullptr);
  STAMP_AT(2, 2);
  STAMP_END(2, 3);
}

#define K3_SIG 16
constexpr int K3_X  = 0;
constexpr int K3_LDS = 112 * 1024;

__global__ __launch_bounds__(512, 2) void k_ffn1(const u32x4* __restrict__ xf, const f32x2* __restrict__ st,
                                                 const u32x4* __restrict__ w1_t, u32x4* __restrict__ hf) {
  __shared__ __attribute__((aligned(16))) char smem[K3_LDS];
  const int bid = blockIdx.x, xcd = bid & 7, bi = bid >> 3;
  const int tt = 4 * (xcd >> 1) + bi / 7, fg = 7 * (xcd & 1) + bi % 7;
  const int tid = threadIdx.x, w8 = __builtin_amdgcn_readfirstlane(tid >> 6), w = w8 & 3;
  const int l = tid & 63, r = l & 31, hh = l >> 5;
  const int ft = 4 * fg + w;
  const unsigned l16 = l * 16;
  const char* xg = (const char*)xf + (size_t)(tt * 4 * KSD + w * 7) * 1024;
  const unsigned xw = lds_addr(smem) + K3_X + w * (7 * 1024);
  auto xdma = [&](int a) {
    glds<4>(l16, xg + a * (KSD * 1024), xw + a * (KSD * 1024));
    glds<3>(l16, xg + a * (KSD * 1024) + 4096, xw + a * (KSD * 1024) + 4096);
  };
  if (w8 >= 4) {
    asm volatile("s_barrier\n\ts_barrier" ::: "memory");
    xdma(2);
    xdma(3);
    vwait_mem<7>();
    asm volatile("s_barrier" ::: "memory");
    vwait_mem<0>();
    asm volatile("s_barrier" ::: "memory");
    return;
  }
  STAMP_BEGIN();
  STAMP_AT(3, 0);
  const char* wg = (const char*)w1_t + (size_t)ft * (KSA * 1024);
  u32x4 wr[KSA];
  f32x2 stv[4];
  sfor<0, 4>([&](auto A_) {
    constexpr int a = decltype(A_)::v;
    gload2<a * 256>(stv[a], r * 8, st + tt * 128);
  });
  xdma(0);
  xdma(1);
  auto wload = [&](auto I_) {
    constexpr int i = decltype(I_)::v;
    if constexpr (i == 0) gload<0>(wr[0], l16, wg + KSD * 1024);
    else gload<((i - 1) & 7) * 1024 - 4096>(wr[i], l16, wg + ((i - 1) >> 3) * 8192 + 4096);
  };
  constexpr int W0 = 11;
  sfor<0, W0>(wload);
  unsigned xo0 = lds_addr(smem) + K3_X + l16, xo1 = xo0 + 2 * KSD * 1024;
  asm volatile("" : "+v"(xo1));
  const lds_u32x4p xl0 = (lds_u32x4p)xo0, xl1 = (lds_u32x4p)xo1;
  u32x4 xa[4];
  float k0[4], k1[4], sd[4];
  {
    const float c0 = -2.f * 0.7978845608028654f * LOG2E;
    const float c1 = c0 * 0.044715f;
#pragma unroll
    for (int a = 0; a < 4; ++a) {
      vwait2<14 + W0>(stv[a]);
      float mean, rstd;
      finish_stats(stv[a], mean, rstd, sd[a]);
      xa[a] = aug_x(mean, sd[a], hh);
      k0[a] = c0 * rstd;
      k1[a] = c1 * rstd * rstd * rstd;
      asm volatile("" : "+v"(xa[a]), "+v"(k0[a]), "+v"(k1[a]), "+v"(sd[a]));
    }
  }
  vwait_mem<W0>();
  BARRIER();
  STAMP_AT(3, 1);
  f32x16 acc[4];
  f32x16 u;
  auto epi = [&](auto A_, auto Q_) {
    constexpr int a = decltype(A_)::v, q = decltype(Q_)::v;
    constexpr int k = q >> 2, g = q & 3;
    const f32x16& y = acc[a];
#pragma unroll
    for (int i = 4 * g; i < 4 * g + 4; ++i) {
      if constexpr (k == 0) u[i] = opq(y[i] * y[i]);
      else if constexpr (k == 1) u[i] = opq(__builtin_fmaf(u[i], k1[a], k0[a]));
      else if constexpr (k == 2) u[i] = opq(u[i] * y[i]);
      else if constexpr (k == 3) u[i] = __builtin_amdgcn_exp2f(u[i]);
      else if constexpr (k == 4) u[i] = opq(__builtin_fmaf(u[i], sd[a], sd[a]));
      else if constexpr (k == 5) u[i] = __builtin_amdgcn_rcpf(u[i]);
      else if constexpr (k == 6) u[i] = opq(u[i] * y[i]);
    }
    if constexpr (k == 6 && (g & 1)) {
      u32x4* dst = hf + (size_t)((tt * 4 + a) * KSF + 2 * ft) * 64 + l;
      dst[(g >> 1) * 64] = pack8(u, g >> 1);
    }
  };
  {
    constexpr int PD = 2;
    u32x4 xb0[KSD], xb1[KSD];
    acc[0] = zero16();
    acc[1] = zero16();
#pragma unroll
    for (int s = 0; s < PD - 1; ++s) { xb0[s] = xl0[s * 64]; xb1[s] = xl0[(KSD + s) * 64]; }
    sfor<0, KSA>([&](auto S_c) {
      constexpr int s = decltype(S_c)::v;
      constexpr int NS = (KSA - W0) / 2;
      if constexpr (s < NS) sfor<W0 + 2 * s, W0 + 2 * s + 2>(wload);
      if constexpr (s == K3_SIG) asm volatile("s_barrier" ::: "memory");
      constexpr int issued = (s < NS ? W0 + 2 * s + 2 : KSA);
      if constexpr (s + PD - 1 < KSD) { xb0[s + PD - 1] = xl0[(s + PD - 1) * 64]; xb1[s + PD - 1] = xl0[(KSD + s + PD - 1) * 64]; }
      vwait<issued - 1 - s>(wr[s]);
      if constexpr (s == 0) {
        acc[0] = MFMA32(asfrag(wr[0]), asfrag(xa[0]), acc[0]);
        acc[1] = MFMA32(asfrag(wr[0]), asfrag(xa[1]), acc[1]);
      } else {
        acc[0] = MFMA32(asfrag(wr[s]), asfrag(xb0[s - 1]), acc[0]);
        acc[1] = MFMA32(asfrag(wr[s]), asfrag(xb1[s - 1]), acc[1]);
      }
      PINV(acc[0]); PINV(acc[1]);
      SCHED();
    });
  }
  STAMP_AT(3, 2);
  sfor<0, 28>([&](auto Q_) { epi(IC<0>{}, Q_); });
  SCHED();
  BARRIER();
  STAMP_AT(3, 3);
  sfor<2, 4>([&](auto A_) {
    constexpr int a = decltype(A_)::v;
    constexpr int PD = 3;
    u32x4 xb[KSD];
    acc[a] = zero16();
#pragma unroll
    for (int s = 0; s < PD - 1; ++s) xb[s] = xl1[((a - 2) * KSD + s) * 64];
    sfor<0, KSA>([&](auto S_c) {
      constexpr int s = decltype(S_c)::v;
      if constexpr (s + PD - 1 < KSD) xb[s + PD - 1] = xl1[((a - 2) * KSD + s + PD - 1) * 64];
      if constexpr (s == 0) acc[a] = MFMA32(asfrag(wr[0]), asfrag(xa[a]), acc[a]);
      else acc[a] = MFMA32(asfrag(wr[s]), asfrag(xb[s - 1]), acc[a]);
      if constexpr (s >= 1) epi(IC<a - 1>{}, IC<s - 1>{});
      SCHED();
    });
    if constexpr (a == 2) BARRIER();
    STAMP_AT(3, 2 + a);
  });
  sfor<0, 28>([&](auto Q_) { epi(IC<3>{}, Q_); });
  STAMP_AT(3, 6);
  STAMP_END(3, 7);
}

template <bool FINAL>
__global__ __launch_bounds__(256, 1) void k_ffn2(const u32x4* __restrict__ hf, const u32x4* __restrict__ w2_t,
                                                 const unsigned short* __restrict__ b2,
                                                 const u32x4* __restrict__ xin, u32x4* __restrict__ xout,
                                                 f32x2* __restrict__ stout, unsigned short* __restrict__ out) {
  __shared__ __attribute__((aligned(16))) char smem[64 * 1024];
  const int bid = blockIdx.x, xcd = bid & 7, bi = bid >> 3;
  const int tp = 4 * xcd + bi / 7, ns = bi % 7;
  const int tid = threadIdx.x, w = __builtin_amdgcn_readfirstlane(tid >> 6), l = tid & 63, hh = l >> 5;
  const int tbo = 2 * tp + (w >> 1), nto = 2 * ns + (w & 1);
  STAMP_BEGIN();
  STAMP_AT(4, 0);
  f32x16 acc[2][2];
#pragma unroll
  for (int a = 0; a < 2; ++a)
#pragma unroll
    for (int b = 0; b < 2; ++b) acc[a][b] = zero16();
  const u32x4* h0 = hf + (size_t)((2 * tp) * KSF + 28 * w) * 64 + l;
  const u32x4* h1 = h0 + (size_t)KSF * 64;
  const u32x4* w0 = w2_t + (size_t)((2 * ns) * KSF + 28 * w) * 64 + l;
  const u32x4* w1p = w0 + (size_t)KSF * 64;
  u32x4 xo[2];
  u32x2 bs[4];
  {
    constexpr int PD = 5;
    u32x4 a0[28], a1[28], b0[28], b1[28];
#pragma unroll
    for (int s = 0; s < PD; ++s) {
      a0[s] = w0[s * 64]; b0[s] = h0[s * 64]; a1[s] = w1p[s * 64]; b1[s] = h1[s * 64];
      SCHED();
    }
#pragma unroll
    for (int s = 0; s < 28; ++s) {
      if (s + PD < 28) {
        a0[s + PD] = w0[(s + PD) * 64]; b0[s + PD] = h0[(s + PD) * 64];
        a1[s + PD] = w1p[(s + PD) * 64]; b1[s + PD] = h1[(s + PD) * 64];
      }
      if (s + PD == 28) {
        xo[0] = xin[(size_t)(tbo * KSD + 2 * nto) * 64 + l];
        xo[1] = xin[(size_t)(tbo * KSD + 2 * nto + 1) * 64 + l];
#pragma unroll
        for (int g = 0; g < 4; ++g) bs[g] = *(const u32x2*)(b2 + 32 * nto + 8 * g + 4 * hh);
      }
      SCHED();
      acc[0][0] = MFMA32(asfrag(a0[s]), asfrag(b0[s]), acc[0][0]);
      acc[0][1] = MFMA32(asfrag(a1[s]), asfrag(b0[s]), acc[0][1]);
      acc[1][0] = MFMA32(asfrag(a0[s]), asfrag(b1[s]), acc[1][0]);
      acc[1][1] = MFMA32(asfrag(a1[s]), asfrag(b1[s]), acc[1][1]);
      SCHED();
    }
  }
  STAMP_AT(4, 1);
  resid_epilogue<FINAL, true>(smem, acc, w, l, tbo, nto, xo, bs, xout, stout, out);
  STAMP_AT(4, 2);
  STAMP_END(4, 3);
}

}

extern "C" void kernel_launch(void* const* d_in, const int* in_sizes, int n_in, void* d_out, int out_size,
                              void* d_ws, size_t ws_size, hipStream_t stream) {
  const unsigned short* x = (const unsigned short*)d_in[0];
  const unsigned short* wqkv = (const unsigned short*)d_in[1];
  const unsigned short* wphi = (const unsigned short*)d_in[2];
  const unsigned short* wo = (const unsigned short*)d_in[3];
  const unsigned short* w1 = (const unsigned short*)d_in[5];
  const unsigned short* b1 = (const unsigned short*)d_in[6];
  const unsigned short* w2 = (const unsigned short*)d_in[7];
  const unsigned short* b2 = (const unsigned short*)d_in[8];
  char* ws = (char*)d_ws;
  unsigned short* out = (unsigned short*)d_out;

  auto XF = [&](int i) { return (u32x4*)(ws + OFF_XF + (size_t)i * XF_BYTES); };
  auto ST = [&](int i) { return (f32x2*)(ws + OFF_ST + (size_t)i * ST_BYTES); };
  const u32x4* wqkv_t = (const u32x4*)(ws + OFF_WQKV);
  const u32x4* wo_t = (const u32x4*)(ws + OFF_WO);
  const u32x4* w1_t = (const u32x4*)(ws + OFF_W1);
  const u32x4* w2_t = (const u32x4*)(ws + OFF_W2);
  u32x4* phiq = (u32x4*)(ws + OFF_PHIQ);
  float* kv = (float*)(ws + OFF_KV);
  u32x4* hf = (u32x4*)(ws + OFF_HF);

  k_prep<<<256, 256, 0, stream>>>(x, wqkv, wphi, wo, w1, b1, w2, ws);
  for (int layer = 0; layer < 2; ++layer) {
    const int i0 = 2 * layer;
    float* kvl = kv + layer * H_ * 2048;
    k_qkv<<<256, 512, 0, stream>>>(XF(i0), ST(i0), wqkv_t, phiq, kvl);
    k_attn_out<<<224, 256, 0, stream>>>(phiq, kvl, wo_t, XF(i0), XF(i0 + 1), ST(i0 + 1));
    k_ffn1<<<224, 512, 0, stream>>>(XF(i0 + 1), ST(i0 + 1), w1_t, hf);
    if (layer == 0)
      k_ffn2<false><<<224, 256, 0, stream>>>(hf, w2_t, b2, XF(i0 + 1), XF(i0 + 2), ST(i0 + 2), nullptr);
    else
      k_ffn2<true><<<224, 256, 0, stream>>>(hf, w2_t, b2, XF(i0 + 1), nullptr, nullptr, out);
  }
}
```

```cpp
#include <hip/hip_runtime.h>
#include <hip/hip_bf16.h>
#include <stdint.h>

typedef __attribute__((ext_vector_type(8))) short bf16x8;
typedef __attribute__((ext_vector_type(4))) short s16x4;
typedef __attribute__((ext_vector_type(16))) float f32x16;
typedef __attribute__((ext_vector_type(4))) float f32x4;
typedef __attribute__((ext_vector_type(2))) float f32x2;
typedef __attribute__((ext_vector_type(4))) unsigned u32x4;
typedef __attribute__((ext_vector_type(2))) unsigned u32x2;
typedef __attribute__((ext_vector_type(2))) __bf16 bf16x2_t;

#define DI __device__ __forceinline__
#define SCHED() __builtin_amdgcn_sched_barrier(0)
#define PIN(x) asm volatile("" : "+a"(x))
#define PINV(x) asm volatile("" : "+v"(x))
#define MFMA32(a, b, c) __builtin_amdgcn_mfma_f32_32x32x16_bf16((a), (b), (c), 0, 0, 0)

#define STAMP_BEGIN()
#define STAMP_AT(kid, i)
#define STAMP_END(kid, n)
#define DBG_INC(i)
#define XS_AT(i)

namespace {

constexpr int S_ = 2048, D_ = 448, H_ = 16, DH_ = 28, F_ = 1792;
constexpr int NTB = S_ / 32;
constexpr int KSD = D_ / 16;
constexpr int KSF = F_ / 16;
constexpr int NNT = D_ / 32;
constexpr int NFT = F_ / 32;
constexpr int KSA = KSD + 1;

constexpr size_t XF_BYTES   = (size_t)NTB * KSD * 1024;
constexpr size_t ST_BYTES   = (size_t)S_ * 8;
constexpr size_t OFF_XF     = 0;
constexpr size_t OFF_ST     = OFF_XF + 4 * XF_BYTES;
constexpr size_t OFF_WQKV   = OFF_ST + 4 * ST_BYTES;
constexpr size_t OFF_WO     = OFF_WQKV + (size_t)H_ * 3 * KSA * 1024;
constexpr size_t OFF_W1     = OFF_WO + (size_t)H_ * 2 * NNT * 1024;
constexpr size_t OFF_W2     = OFF_W1 + (size_t)NFT * KSA * 1024;
constexpr size_t OFF_PHIQ   = OFF_W2 + (size_t)NNT * KSF * 1024;
constexpr size_t OFF_KV     = OFF_PHIQ + (size_t)NTB * H_ * 4 * 1024;
constexpr size_t OFF_HF     = OFF_KV + (size_t)2 * H_ * 2048 * 4;
constexpr size_t OFF_STP    = OFF_HF + (size_t)NTB * KSF * 1024;
constexpr size_t WS_TOTAL   = OFF_STP + (size_t)NTB * 4096;
constexpr unsigned SENT = 0xffffffffu;

DI unsigned pack2(float a, float b) {
  f32x2 v = {a, b};
  bf16x2_t r = __builtin_convertvector(v, bf16x2_t);
  return __builtin_bit_cast(unsigned, r);
}
DI float bflo(unsigned u) { return __uint_as_float(u << 16); }
DI float bfhi(unsigned u) { return __uint_as_float(u & 0xffff0000u); }
DI float bf1(unsigned short u) { return __uint_as_float(((unsigned)u) << 16); }
DI bf16x8 asfrag(u32x4 v) { return __builtin_bit_cast(bf16x8, v); }
DI u32x4 pack8(const f32x16& x, int sp) {
  u32x4 p;
  p[0] = pack2(x[8 * sp + 0], x[8 * sp + 1]);
  p[1] = pack2(x[8 * sp + 2], x[8 * sp + 3]);
  p[2] = pack2(x[8 * sp + 4], x[8 * sp + 5]);
  p[3] = pack2(x[8 * sp + 6], x[8 * sp + 7]);
  return p;
}
DI float xor32(float v) { return __shfl_xor(v, 32, 64); }
DI void halves(float v, float& lo, float& hi) {
  auto r = __builtin_amdgcn_permlane32_swap(__float_as_uint(v), __float_as_uint(v), false, false);
  lo = __uint_as_float(r[0]);
  hi = __uint_as_float(r[1]);
}
DI float sum32(float v) { float a, b; halves(v, a, b); return a + b; }
DI float max32(float v) { float a, b; halves(v, a, b); return fmaxf(a, b); }
DI float min32(float v) { float a, b; halves(v, a, b); return fminf(a, b); }
DI float opq(float v) { asm("" : "+v"(v)); return v; }
DI f32x16 zero16() {
  f32x16 z;
#pragma unroll
  for (int i = 0; i < 16; ++i) z[i] = 0.f;
  return z;
}
template <int N>
DI void glds(unsigned voff, const void* sbase, unsigned lds) {
  unsigned keep;
  if constexpr (N == 4)
    asm volatile("s_mov_b32 %0, m0\n\ts_mov_b32 m0, %3\n\ts_nop 4\n\t"
                 "global_load_lds_dwordx4 %1, %2\n\tglobal_load_lds_dwordx4 %1, %2 offset:1024\n\t"
                 "global_load_lds_dwordx4 %1, %2 offset:2048\n\tglobal_load_lds_dwordx4 %1, %2 offset:3072\n\t"
                 "s_mov_b32 m0, %0" : "=&s"(keep) : "v"(voff), "s"(sbase), "s"(lds) : "memory");
  else if constexpr (N == 3)
    asm volatile("s_mov_b32 %0, m0\n\ts_mov_b32 m0, %3\n\ts_nop 4\n\t"
                 "global_load_lds_dwordx4 %1, %2\n\tglobal_load_lds_dwordx4 %1, %2 offset:1024\n\t"
                 "global_load_lds_dwordx4 %1, %2 offset:2048\n\t"
                 "s_mov_b32 m0, %0" : "=&s"(keep) : "v"(voff), "s"(sbase), "s"(lds) : "memory");
  else if constexpr (N == 2)
    asm volatile("s_mov_b32 %0, m0\n\ts_mov_b32 m0, %3\n\ts_nop 4\n\t"
                 "global_load_lds_dwordx4 %1, %2\n\tglobal_load_lds_dwordx4 %1, %2 offset:1024\n\t"
                 "s_mov_b32 m0, %0" : "=&s"(keep) : "v"(voff), "s"(sbase), "s"(lds) : "memory");
}
template <int OFF>
DI void gload(u32x4& d, unsigned voff, const void* sbase) {
  asm volatile("global_load_dwordx4 %0, %1, %2 offset:%3" : "=v"(d) : "v"(voff), "s"(sbase), "i"(OFF));
}
template <int OFF>
DI void gload2(f32x2& d, unsigned voff, const void* sbase) {
  asm volatile("s_nop 4\n\tglobal_load_dwordx2 %0, %1, %2 offset:%3" : "=v"(d) : "v"(voff), "s"(sbase), "i"(OFF));
}
template <int OFF>
DI void gload_c(u32x4& d, unsigned voff, const void* sbase) {
  asm volatile("global_load_dwordx4 %0, %1, %2 offset:%3 sc1" : "=v"(d) : "v"(voff), "s"(sbase), "i"(OFF));
}
template <int OFF>
DI void gload1_c(unsigned& d, unsigned voff, const void* sbase) {
  asm volatile("global_load_dword %0, %1, %2 offset:%3 sc1" : "=v"(d) : "v"(voff), "s"(sbase), "i"(OFF));
}
DI void gload1(unsigned& d, unsigned voff, const void* sbase) {
  asm volatile("global_load_dword %0, %1, %2" : "=v"(d) : "v"(voff), "s"(sbase));
}
template <int N> DI void vwait1(unsigned& d) { asm volatile("s_waitcnt vmcnt(%1)" : "+v"(d) : "i"(N)); }
DI u32x4 nosent(u32x4 v) {
  return u32x4{min(v[0], SENT - 1), min(v[1], SENT - 1), min(v[2], SENT - 1), min(v[3], SENT - 1)};
}
template <int OFF = 0>
DI void st_wt(u32x4* p, u32x4 v) {
  asm volatile("global_store_dwordx4 %0, %1, off offset:%2 sc1\n\ts_nop 1" :: "v"(p), "v"(v), "i"(OFF) : "memory");
}
DI void st_wt1(float* p, float v) {
  asm volatile("global_store_dword %0, %1, off sc1" :: "v"(p), "v"(v) : "memory");
}
template <int N> DI void vwait(u32x4& d) { asm volatile("s_waitcnt vmcnt(%1)" : "+v"(d) : "i"(N)); }
template <int N> DI void vwait2(f32x2& d) { asm volatile("s_waitcnt vmcnt(%1)" : "+v"(d) : "i"(N)); }
template <int N> DI void vwait_mem() { asm volatile("s_waitcnt vmcnt(%0)" :: "i"(N) : "memory"); }
DI unsigned lds_addr(const void* p) {
  return (unsigned)(uintptr_t)(const __attribute__((address_space(3))) char*)p;
}
DI unsigned bfbits(float v) { return pack2(v, 0.f) & 0xffffu; }
DI void split3(float v, unsigned& p1, unsigned& p2, unsigned& p3) {
  p1 = bfbits(v);
  const float r1 = v - bflo(p1);
  p2 = bfbits(r1);
  p3 = bfbits(r1 - bflo(p2));
}
DI u32x4 aug_w(float cs) {
  unsigned c1, c2, c3;
  split3(cs, c1, c2, c3);
  return u32x4{c1 | (c2 << 16), c3 | (c1 << 16), c2 | (c1 << 16), 0u};
}
DI u32x4 aug_x(float mean, float sd, int hh) {
  unsigned a, b, c;
  split3(hh ? sd : -mean, a, b, c);
  u32x4 lo = {a | (a << 16), a | (b << 16), b | (c << 16), 0u};
  u32x4 hi = {a | (b << 16), c, 0u, 0u};
  return hh ? hi : lo;
}

template <int I> struct IC { static constexpr int v = I; };
template <int B, int E, class Fn>
DI void sfor(Fn&& f) {
  if constexpr (B < E) {
    f(IC<B>{});
    sfor<B + 1, E>(f);
  }
}

#define BARRIER() asm volatile("s_waitcnt lgkmcnt(0)\n\ts_barrier" ::: "memory")

typedef __attribute__((address_space(3))) s16x4* lds_s16x4p;
typedef const __attribute__((address_space(3))) u32x4* lds_u32x4p;
DI s16x4 tr_read(const char* p) { return __builtin_amdgcn_ds_read_tr16_b64_v4i16((lds_s16x4p)p); }
DI u32x4 join(s16x4 a, s16x4 b) {
  bf16x8 v = __builtin_shufflevector(a, b, 0, 1, 2, 3, 4, 5, 6, 7);
  return __builtin_bit_cast(u32x4, v);
}

DI u32x4 frag_from_image(const char* img, int row_stride, int s, int l, int cb) {
  const int i16 = l & 15, q4 = i16 >> 2, p4 = i16 & 3, blk = (l >> 4) & 1, hh = l >> 5;
  const char* p = img + (16 * s + 4 * hh + q4) * row_stride + cb + (4 * blk + p4) * 8;
  return join(tr_read(p), tr_read(p + 8 * row_stride));
}

__global__ __launch_bounds__(256) void k_prep(const unsigned short* __restrict__ x,
                                              const unsigned short* __restrict__ wqkv,
                                              const unsigned short* __restrict__ wphi,
                                              const unsigned short* __restrict__ wo,
                                              const unsigned short* __restrict__ w1,
                                              const unsigned short* __restrict__ b1,
                                              const unsigned short* __restrict__ w2,
                                              char* __restrict__ ws) {
  __shared__ __attribute__((aligned(16))) char img[32 * 912 + 1024];
  float (*red)[32] = (float (*)[32])(img + 32 * 912);
  const int b = blockIdx.x, tid = threadIdx.x, w = tid >> 6, l = tid & 63, r = l & 31, hh = l >> 5;
  STAMP_BEGIN();
  STAMP_AT(0, 0);
  st_wt1((float*)(ws + OFF_KV) + b * 256 + tid, 0.f);

  if (b < 32) {
    const int h = b >> 1, p = b & 1;
    const int cc = r < DH_ ? r : r - DH_;
    u32x4 bfr[2];
    u32x2 lo[4][2], hi[4][2];
#pragma unroll
    for (int it = 0; it < 4; ++it) {
      const int blk = min(w + 4 * it, 13);
      const unsigned short* row = wqkv + (size_t)(32 * blk + r) * (3 * D_) + p * D_ + DH_ * h;
#pragma unroll
      for (int s2 = 0; s2 < 2; ++s2) {
        const int e0 = 16 * s2 + 8 * hh;
        lo[it][s2] = *(const u32x2*)(row + e0);
        hi[it][s2] = *(const u32x2*)(row + min(e0 + 4, 24));
        if (e0 + 4 >= DH_) hi[it][s2] = u32x2{0u, 0u};
      }
    }
#pragma unroll
    for (int s2 = 0; s2 < 2; ++s2) {
      unsigned short v[8];
#pragma unroll
      for (int j = 0; j < 8; ++j) {
        const int e = 16 * s2 + 8 * hh + j;
        v[j] = wphi[(h * DH_ + min(e, DH_ - 1)) * DH_ + cc];
        if (e >= DH_) v[j] = 0;
      }
#pragma unroll
      for (int j = 0; j < 4; ++j) bfr[s2][j] = (unsigned)v[2 * j] | ((unsigned)v[2 * j + 1] << 16);
    }
    float cs = 0.f;
    u32x4* dst = (u32x4*)(ws + OFF_WQKV) + (size_t)(h * 3 + (p ? 0 : 2)) * KSA * 64;
#pragma unroll
    for (int it = 0; it < 4; ++it) {
      const int blk = w + 4 * it;
      f32x16 acc = zero16();
#pragma unroll
      for (int s2 = 0; s2 < 2; ++s2) {
        u32x4 a = {lo[it][s2][0], lo[it][s2][1], hi[it][s2][0], hi[it][s2][1]};
        acc = MFMA32(asfrag(a), asfrag(bfr[s2]), acc);
      }
      if (blk < 14) {
#pragma unroll
        for (int sp = 0; sp < 2; ++sp) {
          u32x4 pk = pack8(acc, sp);
          st_wt(dst + (2 * blk + sp) * 64 + l, pk);
#pragma unroll
          for (int j = 0; j < 4; ++j) cs += bflo(pk[j]) + bfhi(pk[j]);
        }
      }
    }
    cs += xor32(cs);
    if (hh == 0) red[w][r] = cs;
    __syncthreads();
    if (tid < 64) {
      u32x4 a = aug_w(red[0][r] + red[1][r] + red[2][r] + red[3][r]);
      if (hh) a = u32x4{0u, 0u, 0u, 0u};
      st_wt(dst + KSD * 64 + l, a);
    }
  } else if (b < 48 || (b >= 64 && b < 176)) {
    u32x4* dst;
    bool aug = true;
    u32x4 aughi = {0u, 0u, 0u, 0u};
    if (b < 48) {
      const int h = b - 32;
      dst = (u32x4*)(ws + OFF_WQKV) + (size_t)(h * 3 + 1) * KSA * 64;
      aughi[0] = (r == DH_) ? 0x3f80u : (r == DH_ + 1) ? 0x3f800000u : 0u;
      u32x2 v[14];
#pragma unroll
      for (int i = 0; i < 14; ++i) {
        const int idx = tid + 256 * i, row = idx >> 3, c8 = idx & 7;
        v[i] = *(const u32x2*)(wqkv + (size_t)row * (3 * D_) + 2 * D_ + DH_ * h + 4 * min(c8, 6));
        if (c8 == 7) v[i] = u32x2{0u, 0u};
      }
#pragma unroll
      for (int i = 0; i < 14; ++i) *(u32x2*)(img + (tid + 256 * i) * 8) = v[i];
    } else {
      const unsigned short* src;
      int ld;
      if (b < 120) {
        const int ft = b - 64;
        dst = (u32x4*)(ws + OFF_W1) + (size_t)ft * KSA * 64;
        const unsigned bb = b1[32 * ft + r];
        aughi[0] = bb | (bb << 16);
        aughi[1] = bb;
        src = w1 + 32 * ft;
        ld = F_;
      } else {
        const int bb = b - 120, nt = bb >> 2, qd = bb & 3;
        dst = (u32x4*)(ws + OFF_W2) + (size_t)(nt * KSF + qd * 28) * 64;
        aug = false;
        src = w2 + (size_t)qd * 448 * D_ + 32 * nt;
        ld = D_;
      }
      u32x4 v[7];
#pragma unroll
      for (int i = 0; i < 7; ++i) {
        const int idx = tid + 256 * i, row = idx >> 2, c = idx & 3;
        v[i] = *(const u32x4*)(src + (size_t)row * ld + 8 * c);
      }
#pragma unroll
      for (int i = 0; i < 7; ++i) *(u32x4*)(img + (tid + 256 * i) * 16) = v[i];
    }
    __syncthreads();
    float cs = 0.f;
#pragma unroll
    for (int i = 0; i < 7; ++i) {
      const int s = w * 7 + i;
      u32x4 pk = frag_from_image(img, 64, s, l, 0);
      st_wt(dst + s * 64 + l, pk);
#pragma unroll
      for (int j = 0; j < 4; ++j) cs += bflo(pk[j]) + bfhi(pk[j]);
    }
    if (aug) {
      cs += xor32(cs);
      if (hh == 0) red[w][r] = cs;
      __syncthreads();
      if (tid < 64) {
        u32x4 a = aug_w(red[0][r] + red[1][r] + red[2][r] + red[3][r]);
        st_wt(dst + KSD * 64 + l, hh ? aughi : a);
      }
    }
  } else if (b < 64) {
    const int h = b - 48;
    u32x4 v[7];
#pragma unroll
    for (int i = 0; i < 7; ++i) {
      const int idx = tid + 256 * i;
      v[i] = *(const u32x4*)(wo + (size_t)DH_ * h * D_ + min(idx, 28 * 56 - 1) * 8);
      if (idx >= 28 * 56) v[i] = u32x4{0u, 0u, 0u, 0u};
    }
#pragma unroll
    for (int i = 0; i < 7; ++i) *(u32x4*)(img + (tid + 256 * i) * 16) = v[i];
    __syncthreads();
    u32x4* dst = (u32x4*)(ws + OFF_WO) + (size_t)h * 28 * 64;
#pragma unroll
    for (int i = 0; i < 7; ++i) {
      const int fi = w * 7 + i, ks = fi / NNT, nt = fi % NNT;
      st_wt(dst + fi * 64 + l, frag_from_image(img, 896, ks, l, 64 * nt));
    }
  } else if (b < 240) {
    const int tb = b - 176;
    u32x4 v[7];
#pragma unroll
    for (int i = 0; i < 7; ++i) v[i] = *(const u32x4*)(x + (size_t)tb * 32 * D_ + (tid + 256 * i) * 8);
#pragma unroll
    for (int i = 0; i < 7; ++i) {
      const int idx = tid + 256 * i, row = idx / 56, c = idx % 56;
      *(u32x4*)(img + row * 912 + c * 16) = v[i];
    }
    __syncthreads();
    u32x4* dst = (u32x4*)(ws + OFF_XF) + (size_t)tb * KSD * 64;
    float s1 = 0.f, s2 = 0.f;
#pragma unroll
    for (int i = 0; i < 7; ++i) {
      const int s = w * 7 + i;
      const char* p = img + r * 912 + 32 * s + 8 * hh;
      u32x2 lo = *(const u32x2*)p;
      u32x2 hi = *(const u32x2*)(p + 16);
      u32x4 pk = {lo[0], lo[1], hi[0], hi[1]};
      st_wt(dst + s * 64 + l, pk);
#pragma unroll
      for (int j = 0; j < 4; ++j) {
        float a = bflo(pk[j]), c = bfhi(pk[j]);
        s1 += a + c;
        s2 += a * a + c * c;
      }
    }
    s1 = sum32(s1);
    s2 = sum32(s2);
    float* red2 = (float*)(img + 32 * 912);
    red2[w * 64 + l] = hh ? s2 : s1;
    __syncthreads();
    if (tid < 64) {
      const float v = red2[tid] + red2[64 + tid] + red2[128 + tid] + red2[192 + tid];
      st_wt1((float*)(ws + OFF_ST) + (tb * 32 + r) * 2 + hh, v);
    }
  } else {
    float* z = (float*)(ws + OFF_ST + ST_BYTES) + (b - 240) * 768 + tid;
    st_wt1(z, 0.f); st_wt1(z + 256, 0.f); st_wt1(z + 512, 0.f);
  }
  STAMP_AT(0, 1);
  STAMP_END(0, 2);
}

DI void finish_stats(f32x2 v, float& mean, float& rstd, float& sd) {
  mean = v[0] * (1.f / D_);
  const float var = fmaxf(v[1] * (1.f / D_) - mean * mean, 0.f) + 1e-5f;
  rstd = rsqrtf(var);
  sd = var * rstd;
}

constexpr float LOG2E = 1.4426950408889634f;
DI float max3(float a, float b, float c) { return fmaxf(fmaxf(a, b), c); }
DI float min3(float a, float b, float c) { return fminf(fminf(a, b), c); }

struct Smx {
  f32x16 e[2];
  float mx, mn, b[2], s[2], inv[2];
  u32x2 pk[2][4];
};
DI void smx_pre(Smx& z, const f32x16& y, float rs2) {
  z.mx = fmaxf(max3(y[0], y[1], y[2]), y[3]);
  z.mn = fminf(min3(y[0], y[1], y[2]), y[3]);
#pragma unroll
  for (int q = 1; q < 4; ++q) {
    z.mx = max3(max3(z.mx, y[4 * q], y[4 * q + 1]), y[4 * q + 2], y[4 * q + 3]);
    z.mn = min3(min3(z.mn, y[4 * q], y[4 * q + 1]), y[4 * q + 2], y[4 * q + 3]);
  }
  z.b[0] = -max32(z.mx) * rs2;
  z.b[1] = min32(z.mn) * rs2;
}
constexpr int SMX_SLOTS = 25;
template <int N, int Q>
DI void smx_slot(Smx& z, const f32x16& y, float rs2, float scale, int hh) {
  f32x16& e = z.e[N];
  if constexpr (Q < 4) {
#pragma unroll
    for (int i = 4 * Q; i < 4 * Q + 4; ++i) e[i] = opq(__builtin_fmaf(y[i], N ? -rs2 : rs2, z.b[N]));
  } else if constexpr (Q < 12) {
    e[2 * Q - 8] = __builtin_amdgcn_exp2f(e[2 * Q - 8]);
    e[2 * Q - 7] = __builtin_amdgcn_exp2f(e[2 * Q - 7]);
  } else if constexpr (Q < 16) {
    constexpr int g = Q - 12;
    float a = opq(opq(e[4 * g] + e[4 * g + 1]) + opq(e[4 * g + 2] + e[4 * g + 3]));
    if constexpr (g == 3) a = hh ? 0.f : a;
    if constexpr (g == 0) z.s[N] = a;
    else z.s[N] = opq(z.s[N] + a);
  } else if constexpr (Q == 16) {
    z.inv[N] = __builtin_amdgcn_rcpf(sum32(z.s[N])) * scale;
  } else {
    constexpr int g = (Q - 17) >> 1;
    if constexpr ((Q - 17) & 1) {
      z.pk[N][g] = u32x2{pack2(e[4 * g], e[4 * g + 1]), pack2(e[4 * g + 2], e[4 * g + 3])};
    } else {
      const float f = (g == 3 && hh) ? 0.f : z.inv[N];
#pragma unroll
      for (int i = 4 * g; i < 4 * g + 4; ++i) e[i] = opq(e[i] * f);
    }
  }
}

constexpr int K1_W    = 0;
constexpr int K1_IMG  = K1_W + 88 * 1024;
constexpr int K1_RED  = K1_IMG + 16 * 1024;
constexpr int K1_LDS  = K1_RED + 32 * 1024;
#define K1_SIG 7
#define K1_X0 14
#define K1_PD 4
#define K1_PF 0
#define K1_XS 2

__global__ __launch_bounds__(512, 2) void k_qkv(const u32x4* __restrict__ xf, const f32x2* __restrict__ st,
                                                const u32x4* __restrict__ wqkv_t,
                                                u32x4* __restrict__ phiq, float* __restrict__ kvacc,
                                                u32x4* __restrict__ x1) {
  __shared__ __attribute__((aligned(16))) char smem[K1_LDS];
  const int bid = blockIdx.x, xcd = bid & 7, bi = bid >> 3;
  const int t = 4 * (xcd >> 1) + (bi >> 3), h = 8 * (xcd & 1) + (bi & 7);
  const int tid = threadIdx.x, w8 = __builtin_amdgcn_readfirstlane(tid >> 6), w = w8 & 3;
  const int l = tid & 63, r = l & 31, hh = l >> 5;
  const int tb = t * 4 + w;
  const unsigned l16 = l * 16;
  const char* wb = (const char*)wqkv_t + (size_t)h * (3 * KSA * 1024);
  if (w8 >= 4) {
    STAMP_BEGIN();
    STAMP_AT(9, 0);
    asm volatile("s_barrier\n\ts_barrier" ::: "memory");
    STAMP_AT(9, 1);
    glds<3>(l16, wb + (32 + 3 * w) * 1024, lds_addr(smem) + K1_W + (32 + 3 * w) * 1024);
    glds<4>(l16, wb + (44 + 4 * w) * 1024, lds_addr(smem) + K1_W + (44 + 4 * w) * 1024);
    glds<4>(l16, wb + (60 + 7 * w) * 1024, lds_addr(smem) + K1_W + (60 + 7 * w) * 1024);
    glds<3>(l16, wb + (64 + 7 * w) * 1024, lds_addr(smem) + K1_W + (64 + 7 * w) * 1024);
    vwait_mem<11>();
    asm volatile("s_barrier" ::: "memory");
    vwait_mem<7>();
    STAMP_AT(9, 2);
    asm volatile("s_barrier" ::: "memory");
    vwait_mem<0>();
    unsigned pv[4];
    {
      const u32x4 sv = {SENT, SENT, SENT, SENT};
      u32x4* hd = (u32x4*)((char*)phiq + (OFF_HF - OFF_PHIQ)) + (size_t)(bid * 28 + w * 7) * 64 + l;
      st_wt<0>(hd, sv); st_wt<1024>(hd, sv); st_wt<2048>(hd, sv); st_wt<3072>(hd, sv);
      st_wt<0>(hd + 256, sv); st_wt<1024>(hd + 256, sv); st_wt<2048>(hd + 256, sv);
      u32x4* xd = x1 + (size_t)(bid * 7 + w * 2) * 64 + l;
      st_wt<0>(xd, sv);
      if (w == 3) xd = (u32x4*)((char*)phiq + (OFF_STP - OFF_PHIQ)) + (size_t)(bid - 1) * 64 + l;
      st_wt<1024>(xd, sv);
      const char* pb = (const char*)phiq - (OFF_PHIQ - OFF_WO) + (bi * 4 + w) * 8192;
      sfor<0, K1_PF>([&](auto R_) {
        constexpr int rr = decltype(R_)::v;
        const unsigned off = min((unsigned)(l * 128 + rr * 1048576), (unsigned)(OFF_PHIQ - OFF_WO - 128 - (bi * 4 + w) * 8192));
        gload1(pv[rr], off, pb);
      });
    }
    __builtin_amdgcn_s_setprio(3);
    const float* redr = (const float*)(smem + K1_RED) + w * 256 + l;
    float* dst = kvacc + h * 2048 + w * 256 + l;
#pragma unroll
    for (int n = 0; n < 2; ++n) {
      asm volatile("s_barrier" ::: "memory");
      STAMP_AT(9, 3 + n);
      float rv[4][4];
#pragma unroll
      for (int i = 0; i < 4; ++i)
#pragma unroll
        for (int o = 0; o < 4; ++o) rv[i][o] = redr[o * 2048 + n * 1024 + i * 64];
#pragma unroll
      for (int i = 0; i < 2; ++i) atomicAdd(dst + n * 1024 + i * 64, (rv[i][0] + rv[i][1]) + (rv[i][2] + rv[i][3]));
      if (w != 3) {
#pragma unroll
        for (int i = 2; i < 4; ++i) atomicAdd(dst + n * 1024 + i * 64, (rv[i][0] + rv[i][1]) + (rv[i][2] + rv[i][3]));
      }
    }
    sfor<0, K1_PF>([&](auto R_) { vwait1<4>(pv[decltype(R_)::v]); });
    STAMP_AT(9, 5);
    STAMP_AT(9, 6);
    STAMP_END(9, 7);
    return;
  }
  STAMP_BEGIN();
  STAMP_AT(1, 0);
  const char* xb = (const char*)xf + (size_t)tb * (KSD * 1024);
  u32x4 xr[KSD];
  f32x2 stv;
  gload2<0>(stv, r * 8, st + tb * 32);
  {
    const char* g = wb + w * (8 * 1024);
    const unsigned lw = lds_addr(smem) + K1_W + w * (8 * 1024);
    glds<4>(l16, g, lw);
    glds<4>(l16, g + 4096, lw + 4096);
  }
  constexpr int X0 = K1_X0;
  constexpr int XS = K1_XS;
  auto xload = [&](auto S_c) {
    constexpr int s = decltype(S_c)::v;
    gload<(s & 7) * 1024 - 4096>(xr[s], l16, xb + (s >> 3) * 8192 + 4096);
  };
  sfor<0, X0>(xload);
  u32x4* wl = (u32x4*)(smem + K1_W) + l;
  char* img = smem + K1_IMG + w * 4096;
  vwait2<8 + X0>(stv);
  float mean, rstd, sd;
  finish_stats(stv, mean, rstd, sd);
  u32x4 xa = aug_x(mean, sd, hh);
  float rs2 = rstd * LOG2E;
  unsigned qoff = lds_addr(smem) + K1_W + 2 * KSA * 1024 + l16;
  asm volatile("" : "+v"(xa), "+v"(rs2), "+v"(qoff));
  const lds_u32x4p wql = (lds_u32x4p)qoff;
  vwait_mem<X0>();
  BARRIER();
  STAMP_AT(1, 1);
  f32x16 ak = zero16(), av = zero16(), aq = zero16();
  {
    constexpr int PD = 3;
    u32x4 fk[KSA];
    fk[KSD] = wl[KSD * 64];
#pragma unroll
    for (int s = 0; s < PD; ++s) fk[s] = wl[s * 64];
    ak = MFMA32(asfrag(fk[KSD]), asfrag(xa), ak);
    SCHED();
    sfor<0, KSD>([&](auto S_c) {
      constexpr int s = decltype(S_c)::v;
      constexpr int i0 = X0 + XS * s < KSD ? X0 + XS * s : KSD;
      constexpr int issued = X0 + XS * (s + 1) < KSD ? X0 + XS * (s + 1) : KSD;
      sfor<i0, issued>(xload);
      if constexpr (s == K1_SIG) asm volatile("s_barrier" ::: "memory");
      if constexpr (s + PD < KSD) fk[s + PD] = wl[(s + PD) * 64];
      vwait<issued - 1 - s>(xr[s]);
      ak = MFMA32(asfrag(fk[s]), asfrag(xr[s]), ak);
      PINV(ak);
      SCHED();
    });
  }
  STAMP_AT(1, 2);
  {
    Smx z;
    u32x4 vfr[2], pa[2];
    const int i16 = l & 15, q4 = i16 >> 2, p4 = i16 & 3, blk = (l >> 4) & 1;
    f32x4* red = (f32x4*)(smem + K1_RED) + w * 512 + l;
    auto slot = [&](auto N_, auto Q_) {
      constexpr int n = decltype(N_)::v, q = decltype(Q_)::v;
      smx_slot<n, q>(z, ak, rs2, rstd, hh);
      if constexpr (q >= 17 && ((q - 17) & 1)) {
        constexpr int g = (q - 17) >> 1;
        const int ch = (2 * g + hh) ^ ((r >> 1) & 7);
        *(u32x2*)(img + n * 2048 + r * 64 + ch * 8) = z.pk[n][g];
      }
    };
    auto tread = [&](int n) {
#pragma unroll
      for (int s2 = 0; s2 < 2; ++s2) {
        const int t0 = 16 * s2 + 4 * hh + q4, t1 = t0 + 8;
        const int c0 = (4 * blk + p4) ^ ((t0 >> 1) & 7), c1 = (4 * blk + p4) ^ ((t1 >> 1) & 7);
        pa[s2] = join(tr_read(img + n * 2048 + t0 * 64 + c0 * 8), tr_read(img + n * 2048 + t1 * 64 + c1 * 8));
      }
    };
    auto kvout = [&](int n) {
      f32x16 kv = zero16();
      kv = MFMA32(asfrag(pa[0]), asfrag(vfr[0]), kv);
      kv = MFMA32(asfrag(pa[1]), asfrag(vfr[1]), kv);
#pragma unroll
      for (int g = 0; g < 4; ++g) red[(4 * n + g) * 64] = f32x4{kv[4 * g], kv[4 * g + 1], kv[4 * g + 2], kv[4 * g + 3]};
      BARRIER();
      SCHED();
    };
    {
      u32x4 f[KSA];
#pragma unroll
      for (int s = 0; s < 3; ++s) f[s] = wl[(KSA + s) * 64];
      sfor<0, 3>([&](auto S_c) {
        constexpr int s = decltype(S_c)::v;
        av = MFMA32(asfrag(xr[s]), asfrag(f[s]), av);
        PINV(av);
      });
      SCHED();
      smx_pre(z, ak, rs2);
      sfor<0, 4>([&](auto Q_) { slot(IC<0>{}, Q_); });
      SCHED();
      asm volatile("s_barrier" ::: "memory");
      constexpr int PD = K1_PD;
#pragma unroll
      for (int s = 3; s < 3 + PD; ++s) f[s] = wl[(KSA + s) * 64];
      sfor<4, 8>([&](auto Q_) { slot(IC<0>{}, Q_); });
      SCHED();
      sfor<3, KSA>([&](auto S_c) {
        constexpr int s = decltype(S_c)::v;
        if constexpr (s + PD == 15) asm volatile("s_barrier" ::: "memory");
        if constexpr (s + PD < KSA) f[s + PD] = wl[(KSA + s + PD) * 64];
        if constexpr (s + 5 < SMX_SLOTS) slot(IC<0>{}, IC<s + 5>{});
        if constexpr (s + 5 == SMX_SLOTS + 1) tread(0);
        if constexpr (s + 5 >= SMX_SLOTS + 3 && s + 5 < SMX_SLOTS + 7) slot(IC<1>{}, IC<s + 5 - SMX_SLOTS - 3>{});
        if constexpr (s == KSD) av = MFMA32(asfrag(xa), asfrag(f[s]), av);
        else av = MFMA32(asfrag(xr[s]), asfrag(f[s]), av);
        PINV(av);
        SCHED();
      });
    }
    vfr[0] = pack8(av, 0);
    vfr[1] = pack8(av, 1);
    kvout(0);
    STAMP_AT(1, 3);
    constexpr int PD = K1_PD;
    u32x4 f[KSA];
    f[0] = wql[KSD * 64];
#pragma unroll
    for (int s = 1; s < PD; ++s) f[s] = wql[(s - 1) * 64];
    sfor<0, KSA>([&](auto S_c) {
      constexpr int s = decltype(S_c)::v;
      if constexpr (s + PD < KSA) f[s + PD] = wql[(s + PD - 1) * 64];
      if constexpr (s + 4 < SMX_SLOTS) slot(IC<1>{}, IC<s + 4>{});
      if constexpr (s + 4 == SMX_SLOTS + 1) tread(1);
      if constexpr (s == 0) aq = MFMA32(asfrag(f[0]), asfrag(xa), aq);
      else aq = MFMA32(asfrag(f[s]), asfrag(xr[s - 1]), aq);
      PINV(aq);
      SCHED();
      if constexpr (s + 4 == SMX_SLOTS + 3) kvout(1);
    });
    STAMP_AT(1, 4);
  }
  {
    Smx z;
    smx_pre(z, aq, rs2);
    sfor<0, SMX_SLOTS>([&](auto Q_) {
      smx_slot<0, decltype(Q_)::v>(z, aq, rs2, 1.f, hh);
      smx_slot<1, decltype(Q_)::v>(z, aq, rs2, 1.f, hh);
    });
    u32x4* qd = phiq + (size_t)((tb * H_ + h) * 4) * 64 + l;
    st_wt<0>(qd, u32x4{z.pk[0][0][0], z.pk[0][0][1], z.pk[0][1][0], z.pk[0][1][1]});
    st_wt<1024>(qd, u32x4{z.pk[0][2][0], z.pk[0][2][1], z.pk[0][3][0], z.pk[0][3][1]});
    st_wt<2048>(qd, u32x4{z.pk[1][0][0], z.pk[1][0][1], z.pk[1][1][0], z.pk[1][1][1]});
    st_wt<3072>(qd, u32x4{z.pk[1][2][0], z.pk[1][2][1], z.pk[1][3][0], z.pk[1][3][1]});
  }
  STAMP_AT(1, 5);
  STAMP_AT(1, 6);
  STAMP_END(1, 7);
}

DI float row_sums(const u32x4& p0, const u32x4& p1) {
  float s1 = 0.f, s2 = 0.f, t1 = 0.f, t2 = 0.f;
  const bf16x2_t ones = __builtin_bit_cast(bf16x2_t, 0x3f803f80u);
#pragma unroll
  for (int j = 0; j < 4; ++j) {
    const unsigned u0 = p0[j], u1 = p1[j];
    const bf16x2_t a = __builtin_bit_cast(bf16x2_t, u0), c = __builtin_bit_cast(bf16x2_t, u1);
    s1 = __builtin_amdgcn_fdot2_f32_bf16(a, ones, s1, false);
    t1 = __builtin_amdgcn_fdot2_f32_bf16(c, ones, t1, false);
    s2 = __builtin_amdgcn_fdot2_f32_bf16(a, a, s2, false);
    t2 = __builtin_amdgcn_fdot2_f32_bf16(c, c, t2, false);
  }
  s1 += t1;
  s2 += t2;
  auto sw = __builtin_amdgcn_permlane32_swap(__float_as_uint(s1), __float_as_uint(s2), false, false);
  return __uint_as_float(sw[0]) + __uint_as_float(sw[1]);
}
DI float row_sums1(const u32x4& p0) {
  float s1 = 0.f, s2 = 0.f;
  const bf16x2_t ones = __builtin_bit_cast(bf16x2_t, 0x3f803f80u);
#pragma unroll
  for (int j = 0; j < 4; ++j) {
    const unsigned u0 = p0[j];
    const bf16x2_t a = __builtin_bit_cast(bf16x2_t, u0);
    s1 = __builtin_amdgcn_fdot2_f32_bf16(a, ones, s1, false);
    s2 = __builtin_amdgcn_fdot2_f32_bf16(a, a, s2, false);
  }
  auto sw = __builtin_amdgcn_permlane32_swap(__float_as_uint(s1), __float_as_uint(s2), false, false);
  return __uint_as_float(sw[0]) + __uint_as_float(sw[1]);
}

template <bool FINAL>
DI void tile_finish(const f32x16& v, int l, int tb, int nt, u32x4* __restrict__ xout,
                    f32x2* __restrict__ stout, unsigned short* __restrict__ out) {
  const int r = l & 31, hh = l >> 5;
  u32x4 p0 = pack8(v, 0), p1 = pack8(v, 1);
  if (FINAL) {
    unsigned short* o = out + (size_t)(tb * 32 + r) * D_ + 32 * nt + 4 * hh;
    *(u32x2*)(o + 0) = u32x2{p0[0], p0[1]};
    *(u32x2*)(o + 8) = u32x2{p0[2], p0[3]};
    *(u32x2*)(o + 16) = u32x2{p1[0], p1[1]};
    *(u32x2*)(o + 24) = u32x2{p1[2], p1[3]};
  } else {
    st_wt<0>(xout + (size_t)(tb * KSD + 2 * nt) * 64 + l, p0);
    st_wt<1024>(xout + (size_t)(tb * KSD + 2 * nt) * 64 + l, p1);
    atomicAdd((float*)stout + (tb * 32 + r) * 2 + hh, row_sums(p0, p1));
  }
}

template <bool FINAL, bool BIAS>
DI void resid_epilogue(char* smem, const f32x16 (&acc)[2][2], int w, int l, int tb, int nt,
                       const u32x4 (&xo)[2], const u32x2 (&bs)[4],
                       u32x4* __restrict__ xout, f32x2* __restrict__ stout,
                       unsigned short* __restrict__ out) {
  f32x4* red = (f32x4*)smem;
  f32x16 own;
#pragma unroll
  for (int tI = 0; tI < 4; ++tI) {
    const f32x16& a = acc[tI >> 1][tI & 1];
    if (tI != w) {
#pragma unroll
      for (int g = 0; g < 4; ++g)
        red[((w * 4 + tI) * 4 + g) * 64 + l] = f32x4{a[4 * g], a[4 * g + 1], a[4 * g + 2], a[4 * g + 3]};
    } else {
      own = a;
    }
  }
  BARRIER();
  f32x16 v;
#pragma unroll
  for (int g = 0; g < 4; ++g) {
    f32x4 s = f32x4{own[4 * g], own[4 * g + 1], own[4 * g + 2], own[4 * g + 3]};
#pragma unroll
    for (int o = 1; o < 4; ++o) s += red[((((w + o) & 3) * 4 + w) * 4 + g) * 64 + l];
    const unsigned x0 = xo[g >> 1][(g & 1) * 2], x1 = xo[g >> 1][(g & 1) * 2 + 1];
    if (BIAS) {
      s[0] += bflo(bs[g][0]); s[1] += bfhi(bs[g][0]); s[2] += bflo(bs[g][1]); s[3] += bfhi(bs[g][1]);
    }
    v[4 * g + 0] = s[0] + bflo(x0);
    v[4 * g + 1] = s[1] + bfhi(x0);
    v[4 * g + 2] = s[2] + bflo(x1);
    v[4 * g + 3] = s[3] + bfhi(x1);
  }
  tile_finish<FINAL>(v, l, tb, nt, xout, stout, out);
}

#define K2_PR 2
constexpr int K2_O = 0, K2_W = 64 * 1024, K2_LDS = 128 * 1024;
__global__ __launch_bounds__(512, 2) void k_attn_out(const u32x4* __restrict__ phiq,
                                                     const float* __restrict__ kvacc,
                                                     const u32x4* __restrict__ wo_t,
                                                     const u32x4* __restrict__ xin,
                                                     u32x4* __restrict__ xout, f32x2* __restrict__ stout) {
  __shared__ __attribute__((aligned(16))) char smem[K2_LDS];
  const int bid = blockIdx.x, xcd = bid & 7, bi = bid >> 3;
  const int tp = 4 * xcd + bi / 7, ns = bi % 7;
  const int tid = threadIdx.x, w8 = __builtin_amdgcn_readfirstlane(tid >> 6), w = w8 & 3, l = tid & 63;
  const unsigned l16 = l * 16;
  const unsigned l16p = (l & 31) * 16;
  const char* kg = (const char*)kvacc + w * 8192 + 4096;
  const char* pg = (const char*)phiq + (size_t)(2 * tp * H_ + w) * 4096;
  u32x4* ow = (u32x4*)(smem + K2_O) + w * 256 + l;
  auto load = [&](u32x4 (&ld)[16], int j) {
    sfor<0, 16>([&](auto K_) {
      constexpr int k = decltype(K_)::v;
      if constexpr (k < 8) gload<k * 1024 - 4096>(ld[k], (k & 3) == 3 ? l16p : l16, kg + j * 32768);
      else gload<(k & 3) * 1024>(ld[k], l16, pg + j * 16384 + ((k >> 2) & 1) * (H_ * 4096));
    });
  };
  auto produce = [&](u32x4 (&ld)[16], auto J_, auto N_) {
    constexpr int j = decltype(J_)::v, n = decltype(N_)::v;
    u32x4 kvf[4];
    sfor<0, 8>([&](auto K_) { vwait<8 + n>(ld[decltype(K_)::v]); });
#pragma unroll
    for (int i = 0; i < 4; ++i) {
      const f32x4 a = __builtin_bit_cast(f32x4, ld[2 * i]), b = __builtin_bit_cast(f32x4, ld[2 * i + 1]);
      kvf[i] = u32x4{pack2(a[0], a[1]), pack2(a[2], a[3]), pack2(b[0], b[1]), pack2(b[2], b[3])};
    }
    SCHED();
    sfor<0, 2>([&](auto A_) {
      constexpr int a = decltype(A_)::v;
      f32x16 o = zero16();
      sfor<0, 4>([&](auto K_) {
        constexpr int k = decltype(K_)::v;
        vwait<n + 7 - 4 * a - k>(ld[8 + 4 * a + k]);
        o = MFMA32(asfrag(kvf[k]), asfrag(ld[8 + 4 * a + k]), o);
      });
      PINV(o);
      SCHED();
      float dlo, dhi;
      halves(o[12] + o[13], dlo, dhi);
      const float inv = __builtin_amdgcn_rcpf(dhi + 1e-6f);
#pragma unroll
      for (int i = 0; i < 16; ++i) o[i] *= inv;
      ow[(j * 16 + a * 2) * 64] = pack8(o, 0);
      ow[(j * 16 + a * 2 + 1) * 64] = pack8(o, 1);
      SCHED();
    });
  };
  u32x4 la[16], lb[16];
  if (w8 >= 4) {
    STAMP_BEGIN();
    STAMP_AT(10, 0);
    const char* wg = (const char*)wo_t + (size_t)(w * 2 * NNT + 2 * ns) * 1024;
    const unsigned ww = lds_addr(smem) + K2_W + w * 4096;
    auto wdma = [&](int j) {
      glds<2>(l16, wg + j * (8 * NNT * 1024), ww + j * 16384);
      glds<2>(l16, wg + j * (8 * NNT * 1024) + NNT * 1024, ww + j * 16384 + 2048);
    };
    __builtin_amdgcn_s_setprio(2);
    load(la, 1);
    wdma(0);
    wdma(1);
    __builtin_amdgcn_s_setprio(0);
    produce(la, IC<1>{}, IC<8>{});
    vwait_mem<0>();
    STAMP_AT(10, 1);
    BARRIER();
    STAMP_AT(10, 2);
    __builtin_amdgcn_s_setprio(2);
    wdma(2);
    wdma(3);
    load(lb, 3);
    STAMP_AT(10, 3);
    vwait_mem<16>();
    BARRIER();
    __builtin_amdgcn_s_setprio(K2_PR);
    produce(lb, IC<3>{}, IC<0>{});
    BARRIER();
    STAMP_AT(10, 4);
    STAMP_END(10, 5);
    return;
  }
  STAMP_BEGIN();
  STAMP_AT(2, 0);
  const int a = w >> 1, nt = w & 1, tbo = 2 * tp + a, nto = 2 * ns + nt;
  u32x4 xo[2];
  __builtin_amdgcn_s_setprio(3);
  load(la, 0);
  __builtin_amdgcn_s_setprio(1);
  const u32x4* ol = (const u32x4*)(smem + K2_O) + a * 128 + l;
  const u32x4* wl = (const u32x4*)(smem + K2_W) + nt * 64 + l;
  f32x16 acc = zero16();
  auto wread = [&](u32x4 (&wf)[8], int j) {
#pragma unroll
    for (int i = 0; i < 8; ++i) wf[i] = wl[(j * 8 + i) * 128];
  };
  auto consume = [&](u32x4 (&wf)[8], int j) {
    u32x4 of[8];
#pragma unroll
    for (int i = 0; i < 8; ++i) of[i] = ol[((j * 4 + (i >> 1)) * 4 + (i & 1)) * 64];
    SCHED();
#pragma unroll
    for (int i = 0; i < 8; ++i) acc = MFMA32(asfrag(wf[i]), asfrag(of[i]), acc);
    PINV(acc);
    SCHED();
  };
  produce(la, IC<0>{}, IC<0>{});
  load(lb, 2);
  gload<0>(xo[0], l16, (const char*)xin + (size_t)(tbo * KSD + 2 * nto) * 1024);
  gload<1024>(xo[1], l16, (const char*)xin + (size_t)(tbo * KSD + 2 * nto) * 1024);
  STAMP_AT(2, 1);
  BARRIER();
  STAMP_AT(2, 2);
  {
    u32x4 wf[8];
    wread(wf, 0);
    consume(wf, 0);
    wread(wf, 1);
    consume(wf, 1);
  }
  STAMP_AT(2, 3);
  produce(lb, IC<2>{}, IC<2>{});
  BARRIER();
  STAMP_AT(2, 4);
  u32x4 w3[8];
  {
    u32x4 wf[8];
    wread(wf, 2);
    consume(wf, 2);
  }
  wread(w3, 3);
  vwait<0>(xo[0]);
  vwait<0>(xo[1]);
#pragma unroll
  for (int i = 0; i < 8; ++i) {
    acc[2 * i] += bflo(xo[i >> 2][i & 3]);
    acc[2 * i + 1] += bfhi(xo[i >> 2][i & 3]);
  }
  PINV(acc);
  BARRIER();
  STAMP_AT(2, 5);
  consume(w3, 3);
  STAMP_AT(2, 6);
  tile_finish<false>(acc, l, tbo, nto, xout, stout, nullptr);
  STAMP_AT(2, 7);
  STAMP_END(2, 8);
}

#define K3_SIG 16
constexpr int K3_X  = 0;
constexpr int K3_LDS = 112 * 1024;

__global__ __launch_bounds__(512, 2) void k_ffn1(const u32x4* __restrict__ xf, const f32x2* __restrict__ st,
                                                 const u32x4* __restrict__ w1_t, u32x4* __restrict__ hf) {
  __shared__ __attribute__((aligned(16))) char smem[K3_LDS];
  const int bid = blockIdx.x, xcd = bid & 7, bi = bid >> 3;
  const int tt = 4 * (xcd >> 1) + bi / 7, fg = 7 * (xcd & 1) + bi % 7;
  const int tid = threadIdx.x, w8 = __builtin_amdgcn_readfirstlane(tid >> 6), w = w8 & 3;
  const int l = tid & 63, r = l & 31, hh = l >> 5;
  const int ft = 4 * fg + w;
  const unsigned l16 = l * 16;
  const char* xg = (const char*)xf + (size_t)(tt * 4 * KSD + w * 7) * 1024;
  const unsigned xw = lds_addr(smem) + K3_X + w * (7 * 1024);
  auto xdma = [&](int a) {
    glds<4>(l16, xg + a * (KSD * 1024), xw + a * (KSD * 1024));
    glds<3>(l16, xg + a * (KSD * 1024) + 4096, xw + a * (KSD * 1024) + 4096);
  };
  if (w8 >= 4) {
    asm volatile("s_barrier\n\ts_barrier" ::: "memory");
    xdma(2);
    xdma(3);
    vwait_mem<7>();
    asm volatile("s_barrier" ::: "memory");
    vwait_mem<0>();
    asm volatile("s_barrier" ::: "memory");
    return;
  }
  STAMP_BEGIN();
  STAMP_AT(3, 0);
  const char* wg = (const char*)w1_t + (size_t)ft * (KSA * 1024);
  u32x4 wr[KSA];
  f32x2 stv[4];
  sfor<0, 4>([&](auto A_) {
    constexpr int a = decltype(A_)::v;
    gload2<a * 256>(stv[a], r * 8, st + tt * 128);
  });
  xdma(0);
  xdma(1);
  auto wload = [&](auto I_) {
    constexpr int i = decltype(I_)::v;
    if constexpr (i == 0) gload<0>(wr[0], l16, wg + KSD * 1024);
    else gload<((i - 1) & 7) * 1024 - 4096>(wr[i], l16, wg + ((i - 1) >> 3) * 8192 + 4096);
  };
  constexpr int W0 = 11;
  sfor<0, W0>(wload);
  unsigned xo0 = lds_addr(smem) + K3_X + l16, xo1 = xo0 + 2 * KSD * 1024;
  asm volatile("" : "+v"(xo1));
  const lds_u32x4p xl0 = (lds_u32x4p)xo0, xl1 = (lds_u32x4p)xo1;
  u32x4 xa[4];
  float k0[4], k1[4], sd[4];
  {
    const float c0 = -2.f * 0.7978845608028654f * LOG2E;
    const float c1 = c0 * 0.044715f;
#pragma unroll
    for (int a = 0; a < 4; ++a) {
      vwait2<14 + W0>(stv[a]);
      float mean, rstd;
      finish_stats(stv[a], mean, rstd, sd[a]);
      xa[a] = aug_x(mean, sd[a], hh);
      k0[a] = c0 * rstd;
      k1[a] = c1 * rstd * rstd * rstd;
      asm volatile("" : "+v"(xa[a]), "+v"(k0[a]), "+v"(k1[a]), "+v"(sd[a]));
    }
  }
  vwait_mem<W0>();
  BARRIER();
  STAMP_AT(3, 1);
  f32x16 acc[4];
  f32x16 u;
  auto epi = [&](auto A_, auto Q_) {
    constexpr int a = decltype(A_)::v, q = decltype(Q_)::v;
    constexpr int k = q >> 2, g = q & 3;
    const f32x16& y = acc[a];
#pragma unroll
    for (int i = 4 * g; i < 4 * g + 4; ++i) {
      if constexpr (k == 0) u[i] = opq(y[i] * y[i]);
      else if constexpr (k == 1) u[i] = opq(__builtin_fmaf(u[i], k1[a], k0[a]));
      else if constexpr (k == 2) u[i] = opq(u[i] * y[i]);
      else if constexpr (k == 3) u[i] = __builtin_amdgcn_exp2f(u[i]);
      else if constexpr (k == 4) u[i] = opq(__builtin_fmaf(u[i], sd[a], sd[a]));
      else if constexpr (k == 5) u[i] = __builtin_amdgcn_rcpf(u[i]);
      else if constexpr (k == 6) u[i] = opq(u[i] * y[i]);
    }
    if constexpr (k == 6 && (g & 1)) {
      u32x4* dst = hf + (size_t)((tt * 4 + a) * KSF + 2 * ft) * 64 + l;
      st_wt<(g >> 1) * 1024>(dst, pack8(u, g >> 1));
    }
  };
  {
    constexpr int PD = 2;
    u32x4 xb0[KSD], xb1[KSD];
    acc[0] = zero16();
    acc[1] = zero16();
#pragma unroll
    for (int s = 0; s < PD - 1; ++s) { xb0[s] = xl0[s * 64]; xb1[s] = xl0[(KSD + s) * 64]; }
    sfor<0, KSA>([&](auto S_c) {
      constexpr int s = decltype(S_c)::v;
      constexpr int NS = (KSA - W0) / 2;
      if constexpr (s < NS) sfor<W0 + 2 * s, W0 + 2 * s + 2>(wload);
      if constexpr (s == K3_SIG) asm volatile("s_barrier" ::: "memory");
      constexpr int issued = (s < NS ? W0 + 2 * s + 2 : KSA);
      if constexpr (s + PD - 1 < KSD) { xb0[s + PD - 1] = xl0[(s + PD - 1) * 64]; xb1[s + PD - 1] = xl0[(KSD + s + PD - 1) * 64]; }
      vwait<issued - 1 - s>(wr[s]);
      if constexpr (s == 0) {
        acc[0] = MFMA32(asfrag(wr[0]), asfrag(xa[0]), acc[0]);
        acc[1] = MFMA32(asfrag(wr[0]), asfrag(xa[1]), acc[1]);
      } else {
        acc[0] = MFMA32(asfrag(wr[s]), asfrag(xb0[s - 1]), acc[0]);
        acc[1] = MFMA32(asfrag(wr[s]), asfrag(xb1[s - 1]), acc[1]);
      }
      PINV(acc[0]); PINV(acc[1]);
      SCHED();
    });
  }
  STAMP_AT(3, 2);
  sfor<0, 28>([&](auto Q_) { epi(IC<0>{}, Q_); });
  SCHED();
  BARRIER();
  STAMP_AT(3, 3);
  sfor<2, 4>([&](auto A_) {
    constexpr int a = decltype(A_)::v;
    constexpr int PD = 3;
    u32x4 xb[KSD];
    acc[a] = zero16();
#pragma unroll
    for (int s = 0; s < PD - 1; ++s) xb[s] = xl1[((a - 2) * KSD + s) * 64];
    sfor<0, KSA>([&](auto S_c) {
      constexpr int s = decltype(S_c)::v;
      if constexpr (s + PD - 1 < KSD) xb[s + PD - 1] = xl1[((a - 2) * KSD + s + PD - 1) * 64];
      if constexpr (s == 0) acc[a] = MFMA32(asfrag(wr[0]), asfrag(xa[a]), acc[a]);
      else acc[a] = MFMA32(asfrag(wr[s]), asfrag(xb[s - 1]), acc[a]);
      if constexpr (s >= 1) epi(IC<a - 1>{}, IC<s - 1>{});
      SCHED();
    });
    if constexpr (a == 2) BARRIER();
    STAMP_AT(3, 2 + a);
  });
  sfor<0, 28>([&](auto Q_) { epi(IC<3>{}, Q_); });
  STAMP_AT(3, 6);
  STAMP_END(3, 7);
}

template <bool FINAL>
__global__ __launch_bounds__(256, 1) void k_ffn2(const u32x4* __restrict__ hf, const u32x4* __restrict__ w2_t,
                                                 const unsigned short* __restrict__ b2,
                                                 const u32x4* __restrict__ xin, u32x4* __restrict__ xout,
                                                 f32x2* __restrict__ stout, unsigned short* __restrict__ out) {
  __shared__ __attribute__((aligned(16))) char smem[64 * 1024];
  const int bid = blockIdx.x, xcd = bid & 7, bi = bid >> 3;
  const int tp = 4 * xcd + bi / 7, ns = bi % 7;
  const int tid = threadIdx.x, w = __builtin_amdgcn_readfirstlane(tid >> 6), l = tid & 63, hh = l >> 5;
  const int tbo = 2 * tp + (w >> 1), nto = 2 * ns + (w & 1);
  STAMP_BEGIN();
  STAMP_AT(4, 0);
  f32x16 acc[2][2];
#pragma unroll
  for (int a = 0; a < 2; ++a)
#pragma unroll
    for (int b = 0; b < 2; ++b) acc[a][b] = zero16();
  const u32x4* h0 = hf + (size_t)((2 * tp) * KSF + 28 * w) * 64 + l;
  const u32x4* h1 = h0 + (size_t)KSF * 64;
  const u32x4* w0 = w2_t + (size_t)((2 * ns) * KSF + 28 * w) * 64 + l;
  const u32x4* w1p = w0 + (size_t)KSF * 64;
  u32x4 xo[2];
  u32x2 bs[4];
  {
    constexpr int PD = 5;
    u32x4 a0[28], a1[28], b0[28], b1[28];
#pragma unroll
    for (int s = 0; s < PD; ++s) {
      a0[s] = w0[s * 64]; b0[s] = h0[s * 64]; a1[s] = w1p[s * 64]; b1[s] = h1[s * 64];
      SCHED();
    }
#pragma unroll
    for (int s = 0; s < 28; ++s) {
      if (s + PD < 28) {
        a0[s + PD] = w0[(s + PD) * 64]; b0[s + PD] = h0[(s + PD) * 64];
        a1[s + PD] = w1p[(s + PD) * 64]; b1[s + PD] = h1[(s + PD) * 64];
      }
      if (s + PD == 28) {
        xo[0] = xin[(size_t)(tbo * KSD + 2 * nto) * 64 + l];
        xo[1] = xin[(size_t)(tbo * KSD + 2 * nto + 1) * 64 + l];
#pragma unroll
        for (int g = 0; g < 4; ++g) bs[g] = *(const u32x2*)(b2 + 32 * nto + 8 * g + 4 * hh);
      }
      SCHED();
      acc[0][0] = MFMA32(asfrag(a0[s]), asfrag(b0[s]), acc[0][0]);
      acc[0][1] = MFMA32(asfrag(a1[s]), asfrag(b0[s]), acc[0][1]);
      acc[1][0] = MFMA32(asfrag(a0[s]), asfrag(b1[s]), acc[1][0]);
      acc[1][1] = MFMA32(asfrag(a1[s]), asfrag(b1[s]), acc[1][1]);
      SCHED();
    }
  }
  STAMP_AT(4, 1);
  resid_epilogue<FINAL, true>(smem, acc, w, l, tbo, nto, xo, bs, xout, stout, out);
  STAMP_AT(4, 2);
  STAMP_END(4, 3);
}

#define KB_PR 2
#define KB_WG1 8
#define KB_WG2 12
#define KB_XD 7
#define KB_W0 10
#define KB_MID 4
#define KB_N2 14
#define KB_N3 6
constexpr int KB_O = 0, KB_W = 64 * 1024, KB_X = 64 * 1024, KB_FA = 0, KB_SA = 32 * 1024, KB_FB = 64 * 1024, KB_SB = 96 * 1024, KB_ST = 128 * 1024;
constexpr int KB_LDS = 132 * 1024;
constexpr int SPIN_MAX = 1 << 16;
#define KB_TA 99
#define KB_TB 9
#define KB_PF0 0
#define KB_PF1 0
#define KB_HQ 16
constexpr int hq(int i) { return KB_HQ + i < 27 ? KB_HQ + i : 27; }
static_assert(KB_HQ >= 28 - KB_N2 - KB_N3, "w2 requests precede the h requests");
DI unsigned umax3(unsigned a, unsigned b, unsigned c) { return max(max(a, b), c); }
DI unsigned smax(unsigned m, const u32x4& d) { return umax3(umax3(m, d[0], d[1]), d[2], d[3]); }
DI bool has_sent(unsigned m) { return __builtin_amdgcn_ballot_w64(m == SENT) != 0; }

template <bool FINAL>
__global__ __launch_bounds__(512, 2) void k_fused(char* __restrict__ ws, const float* __restrict__ kvacc,
                                                  const u32x4* __restrict__ xin, u32x4* __restrict__ x1,
                                                  u32x4* __restrict__ xout, f32x2* __restrict__ stout,
                                                  const unsigned short* __restrict__ b2,
                                                  unsigned short* __restrict__ out) {
  __shared__ __attribute__((aligned(16))) char smem[KB_LDS];
  const int bid = blockIdx.x, xcd = bid & 7, bi = bid >> 3;
  const int tp = 4 * xcd + bi / 7, ns = bi % 7;
  const int tid = threadIdx.x, w8 = __builtin_amdgcn_readfirstlane(tid >> 6), w = w8 & 3, l = tid & 63;
  const int r = l & 31, hh = l >> 5;
  if (bid >= 224) {
    unsigned d = 0;
    for (int c = KB_PF0 + (bi - 28) * 8 + w8; c < KB_PF1; c += 32) {
      const char* base;
      unsigned off = l * 128;
      if (c < 8) {
        base = (const char*)kvacc + 65536 + c * 8192;
      } else if (c < 40) {
        const int q = c - 8;
        base = ws + OFF_PHIQ + (size_t)((8 * xcd + (q >> 2)) * H_ + 8) * 4096 + (q & 3) * 8192;
      } else if (c < 68) {
        base = ws + OFF_WO + 8 * 28 * 1024 + (c - 40) * 8192;
      } else if (c < 271) {
        const int q = c - 68, sq = q / 7, g = q % 7;
        base = ws + OFF_W1 + (size_t)(g * 8 * KSA + sq) * 1024;
        off = (l >> 3) * (KSA * 1024) + (l & 7) * 128;
      } else {
        const int q = c - 271, j = q / 28, rr = q % 28, nt = rr >> 1, pp = ((nt >> 1) + j) % 7;
        base = ws + OFF_W2 + (size_t)(nt * KSF + 16 * pp) * 1024 + (rr & 1) * 8192;
      }
      asm volatile("global_load_dword %0, %1, %2" : "+v"(d) : "v"(off), "s"(base));
    }
    vwait1<0>(d);
    return;
  }
  const unsigned l16 = l * 16;
  const unsigned l16p = (l & 31) * 16;
  const char* kg = (const char*)kvacc + w * 8192 + 4096;
  const char* pg = ws + OFF_PHIQ + (size_t)(2 * tp * H_ + w) * 4096;
  u32x4* ow = (u32x4*)(smem + KB_O) + w * 256 + l;
  const int fn = (w8 >> 1) & 1, fs = w8 & 1;
  u32x4 bq0 = ((const u32x4*)(b2 + 16 * (4 * ns + 2 * fn + fs)))[0], bq1 = ((const u32x4*)(b2 + 16 * (4 * ns + 2 * fn + fs)))[1];
  auto load = [&](u32x4 (&ld)[16], int j) {
    sfor<0, 16>([&](auto K_) {
      constexpr int k = decltype(K_)::v;
      if constexpr (k < 8) gload<k * 1024 - 4096>(ld[k], (k & 3) == 3 ? l16p : l16, kg + j * 32768);
      else gload<(k & 3) * 1024>(ld[k], l16, pg + j * 16384 + ((k >> 2) & 1) * (H_ * 4096));
    });
  };
  auto produce = [&](u32x4 (&ld)[16], auto J_, auto N_) {
    constexpr int j = decltype(J_)::v, n = decltype(N_)::v;
    u32x4 kvf[4];
    sfor<0, 8>([&](auto K_) { vwait<8 + n>(ld[decltype(K_)::v]); });
#pragma unroll
    for (int i = 0; i < 4; ++i) {
      const f32x4 a = __builtin_bit_cast(f32x4, ld[2 * i]), b = __builtin_bit_cast(f32x4, ld[2 * i + 1]);
      kvf[i] = u32x4{pack2(a[0], a[1]), pack2(a[2], a[3]), pack2(b[0], b[1]), pack2(b[2], b[3])};
    }
    SCHED();
    sfor<0, 2>([&](auto A_) {
      constexpr int a = decltype(A_)::v;
      f32x16 o = zero16();
      sfor<0, 4>([&](auto K_) {
        constexpr int k = decltype(K_)::v;
        vwait<n + 7 - 4 * a - k>(ld[8 + 4 * a + k]);
        o = MFMA32(asfrag(kvf[k]), asfrag(ld[8 + 4 * a + k]), o);
      });
      PINV(o);
      SCHED();
      float dlo, dhi;
      halves(o[12] + o[13], dlo, dhi);
      const float inv = __builtin_amdgcn_rcpf(dhi + 1e-6f);
#pragma unroll
      for (int i = 0; i < 16; ++i) o[i] *= inv;
      ow[(j * 16 + a * 2) * 64] = pack8(o, 0);
      ow[(j * 16 + a * 2 + 1) * 64] = pack8(o, 1);
      SCHED();
    });
  };
  u32x4 wr[KSA];
  const char* w1g = ws + OFF_W1 + (size_t)(8 * ns + w8) * (KSA * 1024);
  auto w1load = [&](auto I_) {
    constexpr int i = decltype(I_)::v;
    gload<(i & 7) * 1024 - 4096>(wr[i], l16, w1g + (i >> 3) * 8192 + 4096);
  };
  constexpr int W0 = KB_W0;
  STAMP_BEGIN();
  STAMP_AT(2, 0);
  if (w8 >= 4) {
    u32x4 la[16], lb[16];
    const char* wg = ws + OFF_WO + (size_t)(w * 2 * NNT + 2 * ns) * 1024;
    const unsigned ww = lds_addr(smem) + KB_W + w * 4096;
    auto wdma = [&](int j) {
      glds<2>(l16, wg + j * (8 * NNT * 1024), ww + j * 16384);
      glds<2>(l16, wg + j * (8 * NNT * 1024) + NNT * 1024, ww + j * 16384 + 2048);
    };
    __builtin_amdgcn_s_setprio(2);
    load(la, 1);
    wdma(0);
    wdma(1);
    __builtin_amdgcn_s_setprio(0);
    produce(la, IC<1>{}, IC<8>{});
    vwait_mem<0>();
    BARRIER();
    __builtin_amdgcn_s_setprio(2);
    wdma(2);
    wdma(3);
    load(lb, 3);
    vwait_mem<16>();
    BARRIER();
    __builtin_amdgcn_s_setprio(KB_PR);
    sfor<0, KB_WG1>(w1load);
    produce(lb, IC<3>{}, IC<KB_WG1>{});
    BARRIER();
    STAMP_AT(2, 1);
    __builtin_amdgcn_s_setprio(0);
    sfor<KB_WG1, KB_WG2>(w1load);
    asm volatile("s_barrier" ::: "memory");
    sfor<KB_WG2, KSA>(w1load);
    STAMP_AT(2, 2);
  } else {
    u32x4 la[16], lb[16];
    const int a = w >> 1, nt = w & 1, tbo = 2 * tp + a, nto = 2 * ns + nt;
    u32x4 xo[2];
    __builtin_amdgcn_s_setprio(3);
    load(la, 0);
    __builtin_amdgcn_s_setprio(1);
    const u32x4* ol = (const u32x4*)(smem + KB_O) + a * 128 + l;
    const u32x4* wl = (const u32x4*)(smem + KB_W) + nt * 64 + l;
    f32x16 acc = zero16();
    auto wread = [&](u32x4 (&wf)[8], int j) {
#pragma unroll
      for (int i = 0; i < 8; ++i) wf[i] = wl[(j * 8 + i) * 128];
    };
    auto consume = [&](u32x4 (&wf)[8], int j) {
      u32x4 of[8];
#pragma unroll
      for (int i = 0; i < 8; ++i) of[i] = ol[((j * 4 + (i >> 1)) * 4 + (i & 1)) * 64];
      SCHED();
#pragma unroll
      for (int i = 0; i < 8; ++i) acc = MFMA32(asfrag(wf[i]), asfrag(of[i]), acc);
      PINV(acc);
      SCHED();
    };
    produce(la, IC<0>{}, IC<0>{});
    load(lb, 2);
    gload<0>(xo[0], l16, (const char*)xin + (size_t)(tbo * KSD + 2 * nto) * 1024);
    gload<1024>(xo[1], l16, (const char*)xin + (size_t)(tbo * KSD + 2 * nto) * 1024);
    BARRIER();
    {
      u32x4 wf[8];
      wread(wf, 0);
      consume(wf, 0);
      wread(wf, 1);
      consume(wf, 1);
    }
    produce(lb, IC<2>{}, IC<2>{});
    BARRIER();
    u32x4 w3[8];
    {
      u32x4 wf[8];
      wread(wf, 2);
      consume(wf, 2);
    }
    wread(w3, 3);
    vwait<0>(xo[0]);
    vwait<0>(xo[1]);
#pragma unroll
    for (int i = 0; i < 8; ++i) {
      acc[2 * i] += bflo(xo[i >> 2][i & 3]);
      acc[2 * i + 1] += bfhi(xo[i >> 2][i & 3]);
    }
    PINV(acc);
    BARRIER();
    __builtin_amdgcn_s_setprio(3);
    consume(w3, 3);
    const u32x4 p0 = nosent(pack8(acc, 0)), p1 = nosent(pack8(acc, 1));
    st_wt<0>(x1 + (size_t)(tbo * KSD + 2 * nto) * 64 + l, p0);
    st_wt<1024>(x1 + (size_t)(tbo * KSD + 2 * nto) * 64 + l, p1);
    float* stp = (float*)(ws + OFF_STP) + (size_t)tbo * 1024;
    st_wt1(stp + nto * 64 + l, __uint_as_float(min(__float_as_uint(row_sums(p0, p1)), SENT - 1)));
    STAMP_AT(2, 1);
    u32x4 xr[14];
    unsigned sv[14];
    const char* xg = (const char*)x1 + (size_t)(tbo * KSD + 2 * nt) * 1024;
    u32x4* xw = (u32x4*)(smem + KB_X) + (a * KSD + 2 * nt) * 64 + l;
    constexpr int XS = 6;
    auto xld = [&](auto I_) {
      constexpr int i = decltype(I_)::v, p = i >> 1;
      gload_c<(p & 1) * 4096 + (i & 1) * 1024 - 4096>(xr[i], l16, xg + (p >> 1) * 8192 + 4096);
    };
    auto xissue = [&]() {
      sfor<0, XS>(xld);
      sfor<0, 14>([&](auto I_) { gload1_c<decltype(I_)::v * 256>(sv[decltype(I_)::v], l * 4, stp); });
      sfor<XS, 14>(xld);
    };
    auto xtake = [&](auto N_, auto F_) {
      constexpr int n = decltype(N_)::v;
      constexpr bool first = decltype(F_)::v;
      unsigned m = 0;
      auto put = [&](auto I_) {
        constexpr int i = decltype(I_)::v;
        vwait<n + 13 - i + (i < XS ? 14 : 0)>(xr[i]);
        xw[((i >> 1) * 4 + (i & 1)) * 64] = xr[i];
        m = smax(m, xr[i]);
        if constexpr (i == 0) XS_AT(13);
      };
      sfor<0, XS>(put);
      sfor<0, 14>([&](auto I_) { vwait1<n + 14 - XS>(sv[decltype(I_)::v]); });
      XS_AT(14);
#pragma unroll
      for (int i = 0; i < 7; ++i) m = umax3(m, sv[2 * i], sv[2 * i + 1]);
      {
        float t[7];
#pragma unroll
        for (int i = 0; i < 7; ++i) t[i] = __uint_as_float(sv[2 * i]) + __uint_as_float(sv[2 * i + 1]);
        float s1, s2, mean, rstd, sd;
        halves(((t[0] + t[1]) + (t[2] + t[3])) + ((t[4] + t[5]) + t[6]), s1, s2);
        finish_stats(f32x2{s1, s2}, mean, rstd, sd);
        const float c0 = -2.f * 0.7978845608028654f * LOG2E;
        const float c1 = c0 * 0.044715f;
        const u32x4 kv = {__float_as_uint(c0 * rstd), __float_as_uint(c1 * rstd * rstd * rstd), __float_as_uint(sd), 0u};
        ((u32x4*)(smem + KB_ST))[w * 64 + l] = nt ? kv : aug_x(mean, sd, hh);
      }
      SCHED();
      sfor<XS, 14>(put);
      XS_AT(15);
      return has_sent(m);
    };
    __builtin_amdgcn_s_sleep(KB_XD);
    xissue();
    asm volatile("s_barrier" ::: "memory");
    sfor<0, W0>(w1load);
    if (__builtin_expect(xtake(IC<W0>{}, IC<1>{}), 0)) {
      for (int it = 0; it < SPIN_MAX; ++it) {
        DBG_INC(0);
        xissue();
        if (!xtake(IC<0>{}, IC<0>{})) break;
      }
    }
    __builtin_amdgcn_s_setprio(0);
    STAMP_AT(2, 2);
  }
  asm volatile("" : "+s"(bq0), "+s"(bq1));
  BARRIER();
  STAMP_AT(2, 3);
  const bool lag = w8 < 4;
  const unsigned l16b = l16 + KSF * 1024;
  const char* w2g = ws + OFF_W2 + (size_t)(2 * ns * KSF + 2 * w8) * 1024;
  char* hg = ws + OFF_HF + (size_t)(2 * tp * KSF + 2 * w8) * 1024;
  int pofs[7];
#pragma unroll
  for (int j = 0; j < 7; ++j) pofs[j] = (ns + j >= 7 ? ns + j - 7 : ns + j) * 16384;
  u32x4 w2r[28];
  auto w2load = [&](auto I_) {
    constexpr int i = decltype(I_)::v;
    gload<((i >> 1) & 1) * 1024>(w2r[i], (i & 1) ? l16b : l16, w2g + pofs[i >> 2]);
  };
  unsigned xo0 = lds_addr(smem) + KB_X + l16;
  asm volatile("" : "+v"(xo0));
  const lds_u32x4p xl = (lds_u32x4p)xo0;
  constexpr int PD = 3;
  u32x4 xb[2][KSA];
#pragma unroll
  for (int s = 0; s < PD; ++s) xb[0][s] = xl[s * 64];
  if (lag) {
    sfor<W0, KSA>(w1load);
    BARRIER();
  }
  const int so = 4 * ns + 2 * fn + fs;
  u32x4 xres;
  f32x16 acc1, u;
  u32x4 hown[2][2], kc;
  auto epi = [&](auto A_, auto Q_) {
    constexpr int a = decltype(A_)::v, q = decltype(Q_)::v;
    constexpr int k = q >> 2, g = q & 3;
    const f32x16& y = acc1;
    const unsigned c0 = kc[0], c1 = kc[1], c2 = kc[2];
    const float k0 = __uint_as_float(c0), k1 = __uint_as_float(c1), sd = __uint_as_float(c2);
#pragma unroll
    for (int i = 4 * g; i < 4 * g + 4; ++i) {
      if constexpr (k == 0) u[i] = opq(y[i] * y[i]);
      else if constexpr (k == 1) u[i] = opq(__builtin_fmaf(u[i], k1, k0));
      else if constexpr (k == 2) u[i] = opq(u[i] * y[i]);
      else if constexpr (k == 3) u[i] = __builtin_amdgcn_exp2f(u[i]);
      else if constexpr (k == 4) u[i] = opq(__builtin_fmaf(u[i], sd, sd));
      else if constexpr (k == 5) u[i] = __builtin_amdgcn_rcpf(u[i]);
      else if constexpr (k == 6) u[i] = opq(u[i] * y[i]);
    }
    if constexpr (k == 6 && (g & 1)) {
      hown[a][g >> 1] = nosent(pack8(u, g >> 1));
      st_wt<(g >> 1) * 1024>((u32x4*)(hg + pofs[0] + a * (KSF * 1024)) + l, hown[a][g >> 1]);
    }
  };
  constexpr int N2 = KB_N2, N3 = KB_N3;
  u32x4 hr[12];
  unsigned tch[2];
  auto hload1 = [&](auto A_, auto I_) {
    constexpr int a = decltype(A_)::v, i = decltype(I_)::v;
    gload_c<(i & 1) * 1024>(hr[i], a ? l16b : l16, hg + pofs[1 + (i >> 1)]);
  };
  auto hload = [&](auto A_, auto J_) {
    hload1(A_, IC<2 * decltype(J_)::v - 2>{});
    hload1(A_, IC<2 * decltype(J_)::v - 1>{});
  };
  sfor<0, 2>([&](auto A_) {
    constexpr int a = decltype(A_)::v;
    acc1 = zero16();
    sfor<0, KSA>([&](auto S_c) {
      constexpr int s = decltype(S_c)::v;
      if constexpr (s + PD < KSD) xb[a][s + PD] = xl[(a * KSD + s + PD) * 64];
      if constexpr (s + PD == KSD) xb[a][KSD] = ((const u32x4*)(smem + KB_ST))[2 * a * 64 + l];
      if constexpr (s + PD == KSA) kc = ((const u32x4*)(smem + KB_ST))[(2 * a + 1) * 64 + l];
      if constexpr (a == 0) vwait<KSD - s>(wr[s]);
      if constexpr (a == 1 && N3 > 0 && s >= 2 && (s - 2) % (26 / (N3 > 0 ? N3 : 1)) == 0 && (s - 2) / (26 / (N3 > 0 ? N3 : 1)) < N3)
        w2load(IC<N2 + (s - 2) / (26 / (N3 > 0 ? N3 : 1))>{});
      if constexpr (a == 1 && s == KB_TA) gload1_c<0>(tch[0], (l & 15) * 128, hg + pofs[0]);
      acc1 = MFMA32(asfrag(wr[s]), asfrag(xb[a][s]), acc1);
      PINV(acc1);
      SCHED();
    });
    BARRIER();
    SCHED();
    STAMP_AT(2, 4 + 2 * a);
    sfor<0, 28>([&](auto Q_) {
      constexpr int q = decltype(Q_)::v;
      epi(A_, Q_);
      if constexpr (a == 0 && (q & 1) && (q >> 1) < N2) { w2load(IC<(q >> 1)>{}); SCHED(); }
      if constexpr (a == 1 && N2 + N3 + q < 28) { w2load(IC<N2 + N3 + q>{}); SCHED(); }
      if constexpr (a == 1) sfor<0, 12>([&](auto I_) { if constexpr (hq(decltype(I_)::v) == q) { hload1(IC<0>{}, I_); SCHED(); } });
    });
    SCHED();
    if constexpr (a == 0) {
#pragma unroll
      for (int s = 0; s < PD; ++s) xb[1][s] = xl[(KSD + s) * 64];
      xres = xl[((lag ? 0 : KSD) + so) * 64];
      BARRIER();
    }
    SCHED();
    if constexpr (a == 0) STAMP_AT(2, 5);
  });
  BARRIER();
  SCHED();
  STAMP_AT(2, 7);
  static_assert(N2 >= 4 && N2 <= 14, "w2 schedule");
  sfor<0, 4>([&](auto I_) { vwait<40>(w2r[decltype(I_)::v]); });
  f32x16 c0, c1;
  auto gemm2 = [&](auto A_) {
    constexpr int a = decltype(A_)::v;
    bool bad;
    auto check = [&](auto J_) {
      constexpr int j = decltype(J_)::v;
      constexpr int n = 12 - 2 * j + (a == 0 ? (hq(2 * j - 1) < 25) + (hq(2 * j - 1) < 27) + (j > KB_TB) : 0);
      vwait<n>(hr[2 * j - 2]);
      vwait<n>(hr[2 * j - 1]);
      if constexpr (j == 1 && (a ? KB_TB < 7 : KB_TA < KSA)) PINV(tch[a]);
      bad = has_sent(max(smax(0u, hr[2 * j - 2]), smax(0u, hr[2 * j - 1])));
    };
    c0 = MFMA32(asfrag(w2r[0]), asfrag(hown[a][0]), zero16());
    c1 = MFMA32(asfrag(w2r[1]), asfrag(hown[a][0]), zero16());
    PINV(c0); PINV(c1);
    SCHED();
    check(IC<1>{});
    SCHED();
    c0 = MFMA32(asfrag(w2r[2]), asfrag(hown[a][1]), c0);
    c1 = MFMA32(asfrag(w2r[3]), asfrag(hown[a][1]), c1);
    PINV(c0); PINV(c1);
    SCHED();
    sfor<1, 7>([&](auto J_) {
      constexpr int j = decltype(J_)::v;
      u32x4 &h0 = hr[2 * j - 2], &h1 = hr[2 * j - 1];
      if (__builtin_expect(bad, 0)) {
        for (int it = 0; it < SPIN_MAX; ++it) {
          DBG_INC(1);
          hload(A_, J_);
          vwait<0>(h0);
          vwait<0>(h1);
          if (!has_sent(smax(smax(0u, h0), h1))) break;
        }
      }
      c0 = MFMA32(asfrag(w2r[4 * j]), asfrag(h0), c0);
      c1 = MFMA32(asfrag(w2r[4 * j + 1]), asfrag(h0), c1);
      PINV(c0); PINV(c1);
      SCHED();
      if constexpr (a == 0 && j == KB_TB) gload1_c<0>(tch[1], (l & 15) * 128 + KSF * 1024, hg + pofs[0]);
      if constexpr (j < 6) check(IC<j + 1>{});
      if constexpr (a == 1 && j == KB_MID) { if (lag) asm volatile("s_barrier" ::: "memory"); }
      SCHED();
      c0 = MFMA32(asfrag(w2r[4 * j + 2]), asfrag(h1), c0);
      c1 = MFMA32(asfrag(w2r[4 * j + 3]), asfrag(h1), c1);
      PINV(c0); PINV(c1);
      SCHED();
    });
  };
  auto pwrite = [&](int off) {
    f32x4* pw = (f32x4*)(smem + off) + w * 512 + l;
#pragma unroll
    for (int g = 0; g < 4; ++g) {
      pw[g * 64] = f32x4{c0[4 * g], c0[4 * g + 1], c0[4 * g + 2], c0[4 * g + 3]};
      pw[(4 + g) * 64] = f32x4{c1[4 * g], c1[4 * g + 1], c1[4 * g + 2], c1[4 * g + 3]};
    }
  };
  f32x4 sum[2];
  auto padd = [&](int off) {
    const f32x4* pr = (const f32x4*)(smem + off) + (fn * 4 + fs * 2) * 64 + l;
    f32x4 pv[8];
#pragma unroll
    for (int i = 0; i < 8; ++i) pv[i] = pr[((i >> 1) * 8 + (i & 1)) * 64];
    SCHED();
#pragma unroll
    for (int g = 0; g < 2; ++g) sum[g] += (pv[g] + pv[2 + g]) + (pv[4 + g] + pv[6 + g]);
    PINV(sum[0]); PINV(sum[1]);
  };
  gemm2(IC<0>{});
  asm volatile("s_barrier" ::: "memory");
  SCHED();
  STAMP_AT(2, 8);
  sfor<1, 7>([&](auto J_) { hload(IC<1>{}, J_); });
  pwrite(lag ? KB_SA : KB_FA);
  {
    const unsigned b00 = bq0[0], b01 = bq0[1], b02 = bq0[2], b03 = bq0[3], b10 = bq1[0], b11 = bq1[1], b12 = bq1[2], b13 = bq1[3];
    const unsigned m = hh ? ~0u : 0u;
    const u32x2 bs[2] = {u32x2{(b02 & m) | (b00 & ~m), (b03 & m) | (b01 & ~m)}, u32x2{(b12 & m) | (b10 & ~m), (b13 & m) | (b11 & ~m)}};
#pragma unroll
    for (int g = 0; g < 2; ++g) {
      sum[g][0] = bflo(bs[g][0]) + bflo(xres[2 * g]);
      sum[g][1] = bfhi(bs[g][0]) + bfhi(xres[2 * g]);
      sum[g][2] = bflo(bs[g][1]) + bflo(xres[2 * g + 1]);
      sum[g][3] = bfhi(bs[g][1]) + bfhi(xres[2 * g + 1]);
    }
    PINV(sum[0]); PINV(sum[1]);
  }
  if (lag) padd(KB_FA);
  BARRIER();
  SCHED();
  STAMP_AT(2, 9);
  gemm2(IC<1>{});
  if (!lag) asm volatile("s_barrier" ::: "memory");
  SCHED();
  STAMP_AT(2, 10);
  pwrite(lag ? KB_SB : KB_FB);
  BARRIER();
  SCHED();
  if (!lag) {
    padd(KB_FB);
    asm volatile("s_barrier" ::: "memory");
  }
  SCHED();
  STAMP_AT(2, 11);
  padd(lag ? KB_SA : KB_SB);
  {
    const u32x4 pk = {pack2(sum[0][0], sum[0][1]), pack2(sum[0][2], sum[0][3]), pack2(sum[1][0], sum[1][1]), pack2(sum[1][2], sum[1][3])};
    const int tbo = 2 * tp + (lag ? 0 : 1);
    if (FINAL) {
      unsigned short* o = out + (size_t)(tbo * 32 + r) * D_ + 16 * so + 4 * hh;
      *(u32x2*)(o + 0) = u32x2{pk[0], pk[1]};
      *(u32x2*)(o + 8) = u32x2{pk[2], pk[3]};
    } else {
      st_wt<0>(xout + (size_t)(tbo * KSD + so) * 64 + l, pk);
      atomicAdd((float*)stout + (tbo * 32 + r) * 2 + hh, row_sums1(pk));
    }
  }
  STAMP_AT(2, 12);
}

}

extern "C" void kernel_launch(void* const* d_in, const int* in_sizes, int n_in, void* d_out, int out_size,
                              void* d_ws, size_t ws_size, hipStream_t stream) {
  const unsigned short* x = (const unsigned short*)d_in[0];
  const unsigned short* wqkv = (const unsigned short*)d_in[1];
  const unsigned short* wphi = (const unsigned short*)d_in[2];
  const unsigned short* wo = (const unsigned short*)d_in[3];
  const unsigned short* w1 = (const unsigned short*)d_in[5];
  const unsigned short* b1 = (const unsigned short*)d_in[6];
  const unsigned short* w2 = (const unsigned short*)d_in[7];
  const unsigned short* b2 = (const unsigned short*)d_in[8];
  char* ws = (char*)d_ws;
  unsigned short* out = (unsigned short*)d_out;

  auto XF = [&](int i) { return (u32x4*)(ws + OFF_XF + (size_t)i * XF_BYTES); };
  auto ST = [&](int i) { return (f32x2*)(ws + OFF_ST + (size_t)i * ST_BYTES); };
  const u32x4* wqkv_t = (const u32x4*)(ws + OFF_WQKV);
  const u32x4* wo_t = (const u32x4*)(ws + OFF_WO);
  const u32x4* w1_t = (const u32x4*)(ws + OFF_W1);
  const u32x4* w2_t = (const u32x4*)(ws + OFF_W2);
  u32x4* phiq = (u32x4*)(ws + OFF_PHIQ);
  float* kv = (float*)(ws + OFF_KV);
  u32x4* hf = (u32x4*)(ws + OFF_HF);

  int dev = 0, cus = 0;
  (void)hipGetDevice(&dev);
  (void)hipDeviceGetAttribute(&cus, hipDeviceAttributeMultiprocessorCount, dev);
  const bool fused = cus >= 224;
  k_prep<<<256, 256, 0, stream>>>(x, wqkv, wphi, wo, w1, b1, w2, ws);
  for (int layer = 0; layer < 2; ++layer) {
    const int i0 = 2 * layer;
    float* kvl = kv + layer * H_ * 2048;
    k_qkv<<<256, 512, 0, stream>>>(XF(i0), ST(i0), wqkv_t, phiq, kvl, XF(i0 + 1));
    if (fused) {
      if (layer == 0) k_fused<false><<<256, 512, 0, stream>>>(ws, kvl, XF(i0), XF(i0 + 1), XF(i0 + 2), ST(i0 + 2), b2, nullptr);
      else k_fused<true><<<256, 512, 0, stream>>>(ws, kvl, XF(i0), XF(i0 + 1), nullptr, nullptr, b2, out);
      continue;
    }
    k_attn_out<<<224, 512, 0, stream>>>(phiq, kvl, wo_t, XF(i0), XF(i0 + 1), ST(i0 + 1));
    k_ffn1<<<224, 512, 0, stream>>>(XF(i0 + 1), ST(i0 + 1), w1_t, hf);
    if (layer == 0)
      k_ffn2<false><<<224, 256, 0, stream>>>(hf, w2_t, b2, XF(i0 + 1), XF(i0 + 2), ST(i0 + 2), nullptr);
    else
      k_ffn2<true><<<224, 256, 0, stream>>>(hf, w2_t, b2, XF(i0 + 1), nullptr, nullptr, out);
  }
}
```

```cpp
#include <hip/hip_runtime.h>
#include <hip/hip_bf16.h>
#include <stdint.h>

typedef __attribute__((ext_vector_type(8))) short bf16x8;
typedef __attribute__((ext_vector_type(4))) short s16x4;
typedef __attribute__((ext_vector_type(16))) float f32x16;
typedef __attribute__((ext_vector_type(4))) float f32x4;
typedef __attribute__((ext_vector_type(2))) float f32x2;
typedef __attribute__((ext_vector_type(4))) unsigned u32x4;
typedef __attribute__((ext_vector_type(2))) unsigned u32x2;
typedef __attribute__((ext_vector_type(2))) __bf16 bf16x2_t;

#define DI __device__ __forceinline__
#define SCHED() __builtin_amdgcn_sched_barrier(0)
#define PIN(x) asm volatile("" : "+a"(x))
#define PINV(x) asm volatile("" : "+v"(x))
#define MFMA32(a, b, c) __builtin_amdgcn_mfma_f32_32x32x16_bf16((a), (b), (c), 0, 0, 0)

#define STAMP_BEGIN()
#define STAMP_AT(kid, i)
#define STAMP_END(kid, n)

namespace {

constexpr int S_ = 2048, D_ = 448, H_ = 16, DH_ = 28, F_ = 1792;
constexpr int NTB = S_ / 32;
constexpr int KSD = D_ / 16;
constexpr int KSF = F_ / 16;
constexpr int NNT = D_ / 32;
constexpr int NFT = F_ / 32;
constexpr int KSA = KSD + 1;

constexpr size_t XF_BYTES   = (size_t)NTB * KSD * 1024;
constexpr size_t ST_BYTES   = (size_t)S_ * 8;
constexpr size_t OFF_XF     = 0;
constexpr size_t OFF_ST     = OFF_XF + 4 * XF_BYTES;
constexpr size_t OFF_WQKV   = OFF_ST + 4 * ST_BYTES;
constexpr size_t OFF_WO     = OFF_WQKV + (size_t)H_ * 3 * KSA * 1024;
constexpr size_t OFF_W1     = OFF_WO + (size_t)H_ * 2 * NNT * 1024;
constexpr size_t OFF_W2     = OFF_W1 + (size_t)NFT * KSA * 1024;
constexpr size_t OFF_PHIQ   = OFF_W2 + (size_t)NNT * KSF * 1024;
constexpr size_t OFF_KV     = OFF_PHIQ + (size_t)NTB * H_ * 4 * 1024;
constexpr size_t OFF_HF     = OFF_KV + (size_t)2 * H_ * 2048 * 4;
constexpr size_t WS_TOTAL   = OFF_HF + (size_t)NTB * KSF * 1024;

DI unsigned pack2(float a, float b) {
  f32x2 v = {a, b};
  bf16x2_t r = __builtin_convertvector(v, bf16x2_t);
  return __builtin_bit_cast(unsigned, r);
}
DI float bflo(unsigned u) { return __uint_as_float(u << 16); }
DI float bfhi(unsigned u) { return __uint_as_float(u & 0xffff0000u); }
DI float bf1(unsigned short u) { return __uint_as_float(((unsigned)u) << 16); }
DI bf16x8 asfrag(u32x4 v) { return __builtin_bit_cast(bf16x8, v); }
DI u32x4 pack8(const f32x16& x, int sp) {
  u32x4 p;
  p[0] = pack2(x[8 * sp + 0], x[8 * sp + 1]);
  p[1] = pack2(x[8 * sp + 2], x[8 * sp + 3]);
  p[2] = pack2(x[8 * sp + 4], x[8 * sp + 5]);
  p[3] = pack2(x[8 * sp + 6], x[8 * sp + 7]);
  return p;
}
DI float xor32(float v) { return __shfl_xor(v, 32, 64); }
DI void halves(float v, float& lo, float& hi) {
  auto r = __builtin_amdgcn_permlane32_swap(__float_as_uint(v), __float_as_uint(v), false, false);
  lo = __uint_as_float(r[0]);
  hi = __uint_as_float(r[1]);
}
DI float sum32(float v) { float a, b; halves(v, a, b); return a + b; }
DI float max32(float v) { float a, b; halves(v, a, b); return fmaxf(a, b); }
DI float min32(float v) { float a, b; halves(v, a, b); return fminf(a, b); }
DI float opq(float v) { asm("" : "+v"(v)); return v; }
DI f32x16 zero16() {
  f32x16 z;
#pragma unroll
  for (int i = 0; i < 16; ++i) z[i] = 0.f;
  return z;
}
template <int N>
DI void glds(unsigned voff, const void* sbase, unsigned lds) {
  unsigned keep;
  if constexpr (N == 4)
    asm volatile("s_mov_b32 %0, m0\n\ts_mov_b32 m0, %3\n\ts_nop 4\n\t"
                 "global_load_lds_dwordx4 %1, %2\n\tglobal_load_lds_dwordx4 %1, %2 offset:1024\n\t"
                 "global_load_lds_dwordx4 %1, %2 offset:2048\n\tglobal_load_lds_dwordx4 %1, %2 offset:3072\n\t"
                 "s_mov_b32 m0, %0" : "=&s"(keep) : "v"(voff), "s"(sbase), "s"(lds) : "memory");
  else if constexpr (N == 3)
    asm volatile("s_mov_b32 %0, m0\n\ts_mov_b32 m0, %3\n\ts_nop 4\n\t"
                 "global_load_lds_dwordx4 %1, %2\n\tglobal_load_lds_dwordx4 %1, %2 offset:1024\n\t"
                 "global_load_lds_dwordx4 %1, %2 offset:2048\n\t"
                 "s_mov_b32 m0, %0" : "=&s"(keep) : "v"(voff), "s"(sbase), "s"(lds) : "memory");
  else if constexpr (N == 2)
    asm volatile("s_mov_b32 %0, m0\n\ts_mov_b32 m0, %3\n\ts_nop 4\n\t"
                 "global_load_lds_dwordx4 %1, %2\n\tglobal_load_lds_dwordx4 %1, %2 offset:1024\n\t"
                 "s_mov_b32 m0, %0" : "=&s"(keep) : "v"(voff), "s"(sbase), "s"(lds) : "memory");
}
template <int OFF>
DI void gload(u32x4& d, unsigned voff, const void* sbase) {
  asm volatile("global_load_dwordx4 %0, %1, %2 offset:%3" : "=v"(d) : "v"(voff), "s"(sbase), "i"(OFF));
}
template <int OFF>
DI void gload2(f32x2& d, unsigned voff, const void* sbase) {
  asm volatile("s_nop 4\n\tglobal_load_dwordx2 %0, %1, %2 offset:%3" : "=v"(d) : "v"(voff), "s"(sbase), "i"(OFF));
}
template <int OFF = 0>
DI void st_wt(u32x4* p, u32x4 v) {
  asm volatile("global_store_dwordx4 %0, %1, off offset:%2 sc1\n\ts_nop 1" :: "v"(p), "v"(v), "i"(OFF) : "memory");
}
DI void st_wt1(float* p, float v) {
  asm volatile("global_store_dword %0, %1, off sc1" :: "v"(p), "v"(v) : "memory");
}
template <int N> DI void vwait(u32x4& d) { asm volatile("s_waitcnt vmcnt(%1)" : "+v"(d) : "i"(N)); }
template <int N> DI void vwait2(f32x2& d) { asm volatile("s_waitcnt vmcnt(%1)" : "+v"(d) : "i"(N)); }
template <int N> DI void vwait_mem() { asm volatile("s_waitcnt vmcnt(%0)" :: "i"(N) : "memory"); }
DI unsigned lds_addr(const void* p) {
  return (unsigned)(uintptr_t)(const __attribute__((address_space(3))) char*)p;
}
DI unsigned bfbits(float v) { return pack2(v, 0.f) & 0xffffu; }
DI void split3(float v, unsigned& p1, unsigned& p2, unsigned& p3) {
  p1 = bfbits(v);
  const float r1 = v - bflo(p1);
  p2 = bfbits(r1);
  p3 = bfbits(r1 - bflo(p2));
}
DI u32x4 aug_w(float cs) {
  unsigned c1, c2, c3;
  split3(cs, c1, c2, c3);
  return u32x4{c1 | (c2 << 16), c3 | (c1 << 16), c2 | (c1 << 16), 0u};
}
DI u32x4 aug_x(float mean, float sd, int hh) {
  unsigned a, b, c;
  split3(hh ? sd : -mean, a, b, c);
  u32x4 lo = {a | (a << 16), a | (b << 16), b | (c << 16), 0u};
  u32x4 hi = {a | (b << 16), c, 0u, 0u};
  return hh ? hi : lo;
}

template <int I> struct IC { static constexpr int v = I; };
template <int B, int E, class Fn>
DI void sfor(Fn&& f) {
  if constexpr (B < E) {
    f(IC<B>{});
    sfor<B + 1, E>(f);
  }
}

#define BARRIER() asm volatile("s_waitcnt lgkmcnt(0)\n\ts_barrier" ::: "memory")

typedef __attribute__((address_space(3))) s16x4* lds_s16x4p;
typedef const __attribute__((address_space(3))) u32x4* lds_u32x4p;
DI s16x4 tr_read(const char* p) { return __builtin_amdgcn_ds_read_tr16_b64_v4i16((lds_s16x4p)p); }
DI u32x4 join(s16x4 a, s16x4 b) {
  bf16x8 v = __builtin_shufflevector(a, b, 0, 1, 2, 3, 4, 5, 6, 7);
  return __builtin_bit_cast(u32x4, v);
}

DI u32x4 frag_from_image(const char* img, int row_stride, int s, int l, int cb) {
  const int i16 = l & 15, q4 = i16 >> 2, p4 = i16 & 3, blk = (l >> 4) & 1, hh = l >> 5;
  const char* p = img + (16 * s + 4 * hh + q4) * row_stride + cb + (4 * blk + p4) * 8;
  return join(tr_read(p), tr_read(p + 8 * row_stride));
}

__global__ __launch_bounds__(256) void k_prep(const unsigned short* __restrict__ x,
                                              const unsigned short* __restrict__ wqkv,
                                              const unsigned short* __restrict__ wphi,
                                              const unsigned short* __restrict__ wo,
                                              const unsigned short* __restrict__ w1,
                                              const unsigned short* __restrict__ b1,
                                              const unsigned short* __restrict__ w2,
                                              char* __restrict__ ws) {
  __shared__ __attribute__((aligned(16))) char img[32 * 912 + 1024];
  float (*red)[32] = (float (*)[32])(img + 32 * 912);
  const int b = blockIdx.x, tid = threadIdx.x, w = tid >> 6, l = tid & 63, r = l & 31, hh = l >> 5;
  STAMP_BEGIN();
  STAMP_AT(0, 0);
  st_wt1((float*)(ws + OFF_KV) + b * 256 + tid, 0.f);

  if (b < 32) {
    const int h = b >> 1, p = b & 1;
    const int cc = r < DH_ ? r : r - DH_;
    u32x4 bfr[2];
    u32x2 lo[4][2], hi[4][2];
#pragma unroll
    for (int it = 0; it < 4; ++it) {
      const int blk = min(w + 4 * it, 13);
      const unsigned short* row = wqkv + (size_t)(32 * blk + r) * (3 * D_) + p * D_ + DH_ * h;
#pragma unroll
      for (int s2 = 0; s2 < 2; ++s2) {
        const int e0 = 16 * s2 + 8 * hh;
        lo[it][s2] = *(const u32x2*)(row + e0);
        hi[it][s2] = *(const u32x2*)(row + min(e0 + 4, 24));
        if (e0 + 4 >= DH_) hi[it][s2] = u32x2{0u, 0u};
      }
    }
#pragma unroll
    for (int s2 = 0; s2 < 2; ++s2) {
      unsigned short v[8];
#pragma unroll
      for (int j = 0; j < 8; ++j) {
        const int e = 16 * s2 + 8 * hh + j;
        v[j] = wphi[(h * DH_ + min(e, DH_ - 1)) * DH_ + cc];
        if (e >= DH_) v[j] = 0;
      }
#pragma unroll
      for (int j = 0; j < 4; ++j) bfr[s2][j] = (unsigned)v[2 * j] | ((unsigned)v[2 * j + 1] << 16);
    }
    float cs = 0.f;
    u32x4* dst = (u32x4*)(ws + OFF_WQKV) + (size_t)(h * 3 + (p ? 0 : 2)) * KSA * 64;
#pragma unroll
    for (int it = 0; it < 4; ++it) {
      const int blk = w + 4 * it;
      f32x16 acc = zero16();
#pragma unroll
      for (int s2 = 0; s2 < 2; ++s2) {
        u32x4 a = {lo[it][s2][0], lo[it][s2][1], hi[it][s2][0], hi[it][s2][1]};
        acc = MFMA32(asfrag(a), asfrag(bfr[s2]), acc);
      }
      if (blk < 14) {
#pragma unroll
        for (int sp = 0; sp < 2; ++sp) {
          u32x4 pk = pack8(acc, sp);
          st_wt(dst + (2 * blk + sp) * 64 + l, pk);
#pragma unroll
          for (int j = 0; j < 4; ++j) cs += bflo(pk[j]) + bfhi(pk[j]);
        }
      }
    }
    cs += xor32(cs);
    if (hh == 0) red[w][r] = cs;
    __syncthreads();
    if (tid < 64) {
      u32x4 a = aug_w(red[0][r] + red[1][r] + red[2][r] + red[3][r]);
      if (hh) a = u32x4{0u, 0u, 0u, 0u};
      st_wt(dst + KSD * 64 + l, a);
    }
  } else if (b < 48 || (b >= 64 && b < 176)) {
    u32x4* dst;
    bool aug = true;
    u32x4 aughi = {0u, 0u, 0u, 0u};
    if (b < 48) {
      const int h = b - 32;
      dst = (u32x4*)(ws + OFF_WQKV) + (size_t)(h * 3 + 1) * KSA * 64;
      aughi[0] = (r == DH_) ? 0x3f80u : (r == DH_ + 1) ? 0x3f800000u : 0u;
      u32x2 v[14];
#pragma unroll
      for (int i = 0; i < 14; ++i) {
        const int idx = tid + 256 * i, row = idx >> 3, c8 = idx & 7;
        v[i] = *(const u32x2*)(wqkv + (size_t)row * (3 * D_) + 2 * D_ + DH_ * h + 4 * min(c8, 6));
        if (c8 == 7) v[i] = u32x2{0u, 0u};
      }
#pragma unroll
      for (int i = 0; i < 14; ++i) *(u32x2*)(img + (tid + 256 * i) * 8) = v[i];
    } else {
      const unsigned short* src;
      int ld;
      if (b < 120) {
        const int ft = b - 64;
        dst = (u32x4*)(ws + OFF_W1) + (size_t)ft * KSA * 64;
        const unsigned bb = b1[32 * ft + r];
        aughi[0] = bb | (bb << 16);
        aughi[1] = bb;
        src = w1 + 32 * ft;
        ld = F_;
      } else {
        const int bb = b - 120, nt = bb >> 2, qd = bb & 3;
        dst = (u32x4*)(ws + OFF_W2) + (size_t)(nt * KSF + qd * 28) * 64;
        aug = false;
        src = w2 + (size_t)qd * 448 * D_ + 32 * nt;
        ld = D_;
      }
      u32x4 v[7];
#pragma unroll
      for (int i = 0; i < 7; ++i) {
        const int idx = tid + 256 * i, row = idx >> 2, c = idx & 3;
        v[i] = *(const u32x4*)(src + (size_t)row * ld + 8 * c);
      }
#pragma unroll
      for (int i = 0; i < 7; ++i) *(u32x4*)(img + (tid + 256 * i) * 16) = v[i];
    }
    __syncthreads();
    float cs = 0.f;
#pragma unroll
    for (int i = 0; i < 7; ++i) {
      const int s = w * 7 + i;
      u32x4 pk = frag_from_image(img, 64, s, l, 0);
      st_wt(dst + s * 64 + l, pk);
#pragma unroll
      for (int j = 0; j < 4; ++j) cs += bflo(pk[j]) + bfhi(pk[j]);
    }
    if (aug) {
      cs += xor32(cs);
      if (hh == 0) red[w][r] = cs;
      __syncthreads();
      if (tid < 64) {
        u32x4 a = aug_w(red[0][r] + red[1][r] + red[2][r] + red[3][r]);
        st_wt(dst + KSD * 64 + l, hh ? aughi : a);
      }
    }
  } else if (b < 64) {
    const int h = b - 48;
    u32x4 v[7];
#pragma unroll
    for (int i = 0; i < 7; ++i) {
      const int idx = tid + 256 * i;
      v[i] = *(const u32x4*)(wo + (size_t)DH_ * h * D_ + min(idx, 28 * 56 - 1) * 8);
      if (idx >= 28 * 56) v[i] = u32x4{0u, 0u, 0u, 0u};
    }
#pragma unroll
    for (int i = 0; i < 7; ++i) *(u32x4*)(img + (tid + 256 * i) * 16) = v[i];
    __syncthreads();
    u32x4* dst = (u32x4*)(ws + OFF_WO) + (size_t)h * 28 * 64;
#pragma unroll
    for (int i = 0; i < 7; ++i) {
      const int fi = w * 7 + i, ks = fi / NNT, nt = fi % NNT;
      st_wt(dst + fi * 64 + l, frag_from_image(img, 896, ks, l, 64 * nt));
    }
  } else if (b < 240) {
    const int tb = b - 176;
    u32x4 v[7];
#pragma unroll
    for (int i = 0; i < 7; ++i) v[i] = *(const u32x4*)(x + (size_t)tb * 32 * D_ + (tid + 256 * i) * 8);
#pragma unroll
    for (int i = 0; i < 7; ++i) {
      const int idx = tid + 256 * i, row = idx / 56, c = idx % 56;
      *(u32x4*)(img + row * 912 + c * 16) = v[i];
    }
    __syncthreads();
    u32x4* dst = (u32x4*)(ws + OFF_XF) + (size_t)tb * KSD * 64;
    float s1 = 0.f, s2 = 0.f;
#pragma unroll
    for (int i = 0; i < 7; ++i) {
      const int s = w * 7 + i;
      const char* p = img + r * 912 + 32 * s + 8 * hh;
      u32x2 lo = *(const u32x2*)p;
      u32x2 hi = *(const u32x2*)(p + 16);
      u32x4 pk = {lo[0], lo[1], hi[0], hi[1]};
      st_wt(dst + s * 64 + l, pk);
#pragma unroll
      for (int j = 0; j < 4; ++j) {
        float a = bflo(pk[j]), c = bfhi(pk[j]);
        s1 += a + c;
        s2 += a * a + c * c;
      }
    }
    s1 = sum32(s1);
    s2 = sum32(s2);
    float* red2 = (float*)(img + 32 * 912);
    red2[w * 64 + l] = hh ? s2 : s1;
    __syncthreads();
    if (tid < 64) {
      const float v = red2[tid] + red2[64 + tid] + red2[128 + tid] + red2[192 + tid];
      st_wt1((float*)(ws + OFF_ST) + (tb * 32 + r) * 2 + hh, v);
    }
  } else {
    float* z = (float*)(ws + OFF_ST + ST_BYTES) + (b - 240) * 768 + tid;
    st_wt1(z, 0.f); st_wt1(z + 256, 0.f); st_wt1(z + 512, 0.f);
  }
  STAMP_AT(0, 1);
  STAMP_END(0, 2);
}

DI void finish_stats(f32x2 v, float& mean, float& rstd, float& sd) {
  mean = v[0] * (1.f / D_);
  const float var = fmaxf(v[1] * (1.f / D_) - mean * mean, 0.f) + 1e-5f;
  rstd = rsqrtf(var);
  sd = var * rstd;
}

constexpr float LOG2E = 1.4426950408889634f;
DI float max3(float a, float b, float c) { return fmaxf(fmaxf(a, b), c); }
DI float min3(float a, float b, float c) { return fminf(fminf(a, b), c); }

struct Smx {
  f32x16 e[2];
  float mx, mn, b[2], s[2], inv[2];
  u32x2 pk[2][4];
};
DI void smx_pre(Smx& z, const f32x16& y, float rs2) {
  z.mx = fmaxf(max3(y[0], y[1], y[2]), y[3]);
  z.mn = fminf(min3(y[0], y[1], y[2]), y[3]);
#pragma unroll
  for (int q = 1; q < 4; ++q) {
    z.mx = max3(max3(z.mx, y[4 * q], y[4 * q + 1]), y[4 * q + 2], y[4 * q + 3]);
    z.mn = min3(min3(z.mn, y[4 * q], y[4 * q + 1]), y[4 * q + 2], y[4 * q + 3]);
  }
  z.b[0] = -max32(z.mx) * rs2;
  z.b[1] = min32(z.mn) * rs2;
}
constexpr int SMX_SLOTS = 25;
template <int N, int Q>
DI void smx_slot(Smx& z, const f32x16& y, float rs2, float scale, int hh) {
  f32x16& e = z.e[N];
  if constexpr (Q < 4) {
#pragma unroll
    for (int i = 4 * Q; i < 4 * Q + 4; ++i) e[i] = opq(__builtin_fmaf(y[i], N ? -rs2 : rs2, z.b[N]));
  } else if constexpr (Q < 12) {
    e[2 * Q - 8] = __builtin_amdgcn_exp2f(e[2 * Q - 8]);
    e[2 * Q - 7] = __builtin_amdgcn_exp2f(e[2 * Q - 7]);
  } else if constexpr (Q < 16) {
    constexpr int g = Q - 12;
    float a = opq(opq(e[4 * g] + e[4 * g + 1]) + opq(e[4 * g + 2] + e[4 * g + 3]));
    if constexpr (g == 3) a = hh ? 0.f : a;
    if constexpr (g == 0) z.s[N] = a;
    else z.s[N] = opq(z.s[N] + a);
  } else if constexpr (Q == 16) {
    z.inv[N] = __builtin_amdgcn_rcpf(sum32(z.s[N])) * scale;
  } else {
    constexpr int g = (Q - 17) >> 1;
    if constexpr ((Q - 17) & 1) {
      z.pk[N][g] = u32x2{pack2(e[4 * g], e[4 * g + 1]), pack2(e[4 * g + 2], e[4 * g + 3])};
    } else {
      const float f = (g == 3 && hh) ? 0.f : z.inv[N];
#pragma unroll
      for (int i = 4 * g; i < 4 * g + 4; ++i) e[i] = opq(e[i] * f);
    }
  }
}

constexpr int K1_W    = 0;
constexpr int K1_IMG  = K1_W + 88 * 1024;
constexpr int K1_RED  = K1_IMG + 16 * 1024;
constexpr int K1_LDS  = K1_RED + 32 * 1024;
#define K1_SIG 7
#define K1_X0 14
#define K1_PD 4
#define K1_XS 2

__global__ __launch_bounds__(512, 2) void k_qkv(const u32x4* __restrict__ xf, const f32x2* __restrict__ st,
                                                const u32x4* __restrict__ wqkv_t,
                                                u32x4* __restrict__ phiq, float* __restrict__ kvacc) {
  __shared__ __attribute__((aligned(16))) char smem[K1_LDS];
  const int bid = blockIdx.x, xcd = bid & 7, bi = bid >> 3;
  const int t = 4 * (xcd >> 1) + (bi >> 3), h = 8 * (xcd & 1) + (bi & 7);
  const int tid = threadIdx.x, w8 = __builtin_amdgcn_readfirstlane(tid >> 6), w = w8 & 3;
  const int l = tid & 63, r = l & 31, hh = l >> 5;
  const int tb = t * 4 + w;
  const unsigned l16 = l * 16;
  const char* wb = (const char*)wqkv_t + (size_t)h * (3 * KSA * 1024);
  if (w8 >= 4) {
    STAMP_BEGIN();
    STAMP_AT(9, 0);
    asm volatile("s_barrier\n\ts_barrier" ::: "memory");
    STAMP_AT(9, 1);
    glds<3>(l16, wb + (32 + 3 * w) * 1024, lds_addr(smem) + K1_W + (32 + 3 * w) * 1024);
    glds<4>(l16, wb + (44 + 4 * w) * 1024, lds_addr(smem) + K1_W + (44 + 4 * w) * 1024);
    glds<4>(l16, wb + (60 + 7 * w) * 1024, lds_addr(smem) + K1_W + (60 + 7 * w) * 1024);
    glds<3>(l16, wb + (64 + 7 * w) * 1024, lds_addr(smem) + K1_W + (64 + 7 * w) * 1024);
    vwait_mem<11>();
    asm volatile("s_barrier" ::: "memory");
    vwait_mem<7>();
    STAMP_AT(9, 2);
    asm volatile("s_barrier" ::: "memory");
    vwait_mem<0>();
    __builtin_amdgcn_s_setprio(3);
    const float* redr = (const float*)(smem + K1_RED) + w * 256 + l;
    float* dst = kvacc + h * 2048 + w * 256 + l;
#pragma unroll
    for (int n = 0; n < 2; ++n) {
      asm volatile("s_barrier" ::: "memory");
      STAMP_AT(9, 3 + n);
      float rv[4][4];
#pragma unroll
      for (int i = 0; i < 4; ++i)
#pragma unroll
        for (int o = 0; o < 4; ++o) rv[i][o] = redr[o * 2048 + n * 1024 + i * 64];
#pragma unroll
      for (int i = 0; i < 2; ++i) atomicAdd(dst + n * 1024 + i * 64, (rv[i][0] + rv[i][1]) + (rv[i][2] + rv[i][3]));
      if (w != 3) {
#pragma unroll
        for (int i = 2; i < 4; ++i) atomicAdd(dst + n * 1024 + i * 64, (rv[i][0] + rv[i][1]) + (rv[i][2] + rv[i][3]));
      }
    }
    STAMP_AT(9, 5);
    STAMP_AT(9, 6);
    STAMP_END(9, 7);
    return;
  }
  STAMP_BEGIN();
  STAMP_AT(1, 0);
  const char* xb = (const char*)xf + (size_t)tb * (KSD * 1024);
  u32x4 xr[KSD];
  f32x2 stv;
  gload2<0>(stv, r * 8, st + tb * 32);
  {
    const char* g = wb + w * (8 * 1024);
    const unsigned lw = lds_addr(smem) + K1_W + w * (8 * 1024);
    glds<4>(l16, g, lw);
    glds<4>(l16, g + 4096, lw + 4096);
  }
  constexpr int X0 = K1_X0;
  constexpr int XS = K1_XS;
  auto xload = [&](auto S_c) {
    constexpr int s = decltype(S_c)::v;
    gload<(s & 7) * 1024 - 4096>(xr[s], l16, xb + (s >> 3) * 8192 + 4096);
  };
  sfor<0, X0>(xload);
  u32x4* wl = (u32x4*)(smem + K1_W) + l;
  char* img = smem + K1_IMG + w * 4096;
  vwait2<8 + X0>(stv);
  float mean, rstd, sd;
  finish_stats(stv, mean, rstd, sd);
  u32x4 xa = aug_x(mean, sd, hh);
  float rs2 = rstd * LOG2E;
  unsigned qoff = lds_addr(smem) + K1_W + 2 * KSA * 1024 + l16;
  asm volatile("" : "+v"(xa), "+v"(rs2), "+v"(qoff));
  const lds_u32x4p wql = (lds_u32x4p)qoff;
  vwait_mem<X0>();
  BARRIER();
  STAMP_AT(1, 1);
  f32x16 ak = zero16(), av = zero16(), aq = zero16();
  {
    constexpr int PD = 3;
    u32x4 fk[KSA];
    fk[KSD] = wl[KSD * 64];
#pragma unroll
    for (int s = 0; s < PD; ++s) fk[s] = wl[s * 64];
    ak = MFMA32(asfrag(fk[KSD]), asfrag(xa), ak);
    SCHED();
    sfor<0, KSD>([&](auto S_c) {
      constexpr int s = decltype(S_c)::v;
      constexpr int i0 = X0 + XS * s < KSD ? X0 + XS * s : KSD;
      constexpr int issued = X0 + XS * (s + 1) < KSD ? X0 + XS * (s + 1) : KSD;
      sfor<i0, issued>(xload);
      if constexpr (s == K1_SIG) asm volatile("s_barrier" ::: "memory");
      if constexpr (s + PD < KSD) fk[s + PD] = wl[(s + PD) * 64];
      vwait<issued - 1 - s>(xr[s]);
      ak = MFMA32(asfrag(fk[s]), asfrag(xr[s]), ak);
      PINV(ak);
      SCHED();
    });
  }
  STAMP_AT(1, 2);
  {
    Smx z;
    u32x4 vfr[2], pa[2];
    const int i16 = l & 15, q4 = i16 >> 2, p4 = i16 & 3, blk = (l >> 4) & 1;
    f32x4* red = (f32x4*)(smem + K1_RED) + w * 512 + l;
    auto slot = [&](auto N_, auto Q_) {
      constexpr int n = decltype(N_)::v, q = decltype(Q_)::v;
      smx_slot<n, q>(z, ak, rs2, rstd, hh);
      if constexpr (q >= 17 && ((q - 17) & 1)) {
        constexpr int g = (q - 17) >> 1;
        const int ch = (2 * g + hh) ^ ((r >> 1) & 7);
        *(u32x2*)(img + n * 2048 + r * 64 + ch * 8) = z.pk[n][g];
      }
    };
    auto tread = [&](int n) {
#pragma unroll
      for (int s2 = 0; s2 < 2; ++s2) {
        const int t0 = 16 * s2 + 4 * hh + q4, t1 = t0 + 8;
        const int c0 = (4 * blk + p4) ^ ((t0 >> 1) & 7), c1 = (4 * blk + p4) ^ ((t1 >> 1) & 7);
        pa[s2] = join(tr_read(img + n * 2048 + t0 * 64 + c0 * 8), tr_read(img + n * 2048 + t1 * 64 + c1 * 8));
      }
    };
    auto kvout = [&](int n) {
      f32x16 kv = zero16();
      kv = MFMA32(asfrag(pa[0]), asfrag(vfr[0]), kv);
      kv = MFMA32(asfrag(pa[1]), asfrag(vfr[1]), kv);
#pragma unroll
      for (int g = 0; g < 4; ++g) red[(4 * n + g) * 64] = f32x4{kv[4 * g], kv[4 * g + 1], kv[4 * g + 2], kv[4 * g + 3]};
      BARRIER();
      SCHED();
    };
    {
      u32x4 f[KSA];
#pragma unroll
      for (int s = 0; s < 3; ++s) f[s] = wl[(KSA + s) * 64];
      sfor<0, 3>([&](auto S_c) {
        constexpr int s = decltype(S_c)::v;
        av = MFMA32(asfrag(xr[s]), asfrag(f[s]), av);
        PINV(av);
      });
      SCHED();
      smx_pre(z, ak, rs2);
      sfor<0, 4>([&](auto Q_) { slot(IC<0>{}, Q_); });
      SCHED();
      asm volatile("s_barrier" ::: "memory");
      constexpr int PD = K1_PD;
#pragma unroll
      for (int s = 3; s < 3 + PD; ++s) f[s] = wl[(KSA + s) * 64];
      sfor<4, 8>([&](auto Q_) { slot(IC<0>{}, Q_); });
      SCHED();
      sfor<3, KSA>([&](auto S_c) {
        constexpr int s = decltype(S_c)::v;
        if constexpr (s + PD == 15) asm volatile("s_barrier" ::: "memory");
        if constexpr (s + PD < KSA) f[s + PD] = wl[(KSA + s + PD) * 64];
        if constexpr (s + 5 < SMX_SLOTS) slot(IC<0>{}, IC<s + 5>{});
        if constexpr (s + 5 == SMX_SLOTS + 1) tread(0);
        if constexpr (s + 5 >= SMX_SLOTS + 3 && s + 5 < SMX_SLOTS + 7) slot(IC<1>{}, IC<s + 5 - SMX_SLOTS - 3>{});
        if constexpr (s == KSD) av = MFMA32(asfrag(xa), asfrag(f[s]), av);
        else av = MFMA32(asfrag(xr[s]), asfrag(f[s]), av);
        PINV(av);
        SCHED();
      });
    }
    vfr[0] = pack8(av, 0);
    vfr[1] = pack8(av, 1);
    kvout(0);
    STAMP_AT(1, 3);
    constexpr int PD = K1_PD;
    u32x4 f[KSA];
    f[0] = wql[KSD * 64];
#pragma unroll
    for (int s = 1; s < PD; ++s) f[s] = wql[(s - 1) * 64];
    sfor<0, KSA>([&](auto S_c) {
      constexpr int s = decltype(S_c)::v;
      if constexpr (s + PD < KSA) f[s + PD] = wql[(s + PD - 1) * 64];
      if constexpr (s + 4 < SMX_SLOTS) slot(IC<1>{}, IC<s + 4>{});
      if constexpr (s + 4 == SMX_SLOTS + 1) tread(1);
      if constexpr (s == 0) aq = MFMA32(asfrag(f[0]), asfrag(xa), aq);
      else aq = MFMA32(asfrag(f[s]), asfrag(xr[s - 1]), aq);
      PINV(aq);
      SCHED();
      if constexpr (s + 4 == SMX_SLOTS + 3) kvout(1);
    });
    STAMP_AT(1, 4);
  }
  {
    Smx z;
    smx_pre(z, aq, rs2);
    sfor<0, SMX_SLOTS>([&](auto Q_) {
      smx_slot<0, decltype(Q_)::v>(z, aq, rs2, 1.f, hh);
      smx_slot<1, decltype(Q_)::v>(z, aq, rs2, 1.f, hh);
    });
    u32x4* qd = phiq + (size_t)((tb * H_ + h) * 4) * 64 + l;
    st_wt<0>(qd, u32x4{z.pk[0][0][0], z.pk[0][0][1], z.pk[0][1][0], z.pk[0][1][1]});
    st_wt<1024>(qd, u32x4{z.pk[0][2][0], z.pk[0][2][1], z.pk[0][3][0], z.pk[0][3][1]});
    st_wt<2048>(qd, u32x4{z.pk[1][0][0], z.pk[1][0][1], z.pk[1][1][0], z.pk[1][1][1]});
    st_wt<3072>(qd, u32x4{z.pk[1][2][0], z.pk[1][2][1], z.pk[1][3][0], z.pk[1][3][1]});
  }
  STAMP_AT(1, 5);
  STAMP_AT(1, 6);
  STAMP_END(1, 7);
}

template <bool FINAL>
DI void tile_finish(const f32x16& v, int l, int tb, int nt, u32x4* __restrict__ xout,
                    f32x2* __restrict__ stout, unsigned short* __restrict__ out) {
  const int r = l & 31, hh = l >> 5;
  u32x4 p0 = pack8(v, 0), p1 = pack8(v, 1);
  if (FINAL) {
    unsigned short* o = out + (size_t)(tb * 32 + r) * D_ + 32 * nt + 4 * hh;
    *(u32x2*)(o + 0) = u32x2{p0[0], p0[1]};
    *(u32x2*)(o + 8) = u32x2{p0[2], p0[3]};
    *(u32x2*)(o + 16) = u32x2{p1[0], p1[1]};
    *(u32x2*)(o + 24) = u32x2{p1[2], p1[3]};
  } else {
    st_wt<0>(xout + (size_t)(tb * KSD + 2 * nt) * 64 + l, p0);
    st_wt<1024>(xout + (size_t)(tb * KSD + 2 * nt) * 64 + l, p1);
    float s1 = 0.f, s2 = 0.f, t1 = 0.f, t2 = 0.f;
    const bf16x2_t ones = __builtin_bit_cast(bf16x2_t, 0x3f803f80u);
#pragma unroll
    for (int j = 0; j < 4; ++j) {
      const unsigned u0 = p0[j], u1 = p1[j];
      const bf16x2_t a = __builtin_bit_cast(bf16x2_t, u0), c = __builtin_bit_cast(bf16x2_t, u1);
      s1 = __builtin_amdgcn_fdot2_f32_bf16(a, ones, s1, false);
      t1 = __builtin_amdgcn_fdot2_f32_bf16(c, ones, t1, false);
      s2 = __builtin_amdgcn_fdot2_f32_bf16(a, a, s2, false);
      t2 = __builtin_amdgcn_fdot2_f32_bf16(c, c, t2, false);
    }
    s1 += t1;
    s2 += t2;
    auto sw = __builtin_amdgcn_permlane32_swap(__float_as_uint(s1), __float_as_uint(s2), false, false);
    s1 = s2 = __uint_as_float(sw[0]) + __uint_as_float(sw[1]);
    atomicAdd((float*)stout + (tb * 32 + r) * 2 + hh, hh ? s2 : s1);
  }
}

template <bool FINAL, bool BIAS>
DI void resid_epilogue(char* smem, const f32x16 (&acc)[2][2], int w, int l, int tb, int nt,
                       const u32x4 (&xo)[2], const u32x2 (&bs)[4],
                       u32x4* __restrict__ xout, f32x2* __restrict__ stout,
                       unsigned short* __restrict__ out) {
  f32x4* red = (f32x4*)smem;
  f32x16 own;
#pragma unroll
  for (int tI = 0; tI < 4; ++tI) {
    const f32x16& a = acc[tI >> 1][tI & 1];
    if (tI != w) {
#pragma unroll
      for (int g = 0; g < 4; ++g)
        red[((w * 4 + tI) * 4 + g) * 64 + l] = f32x4{a[4 * g], a[4 * g + 1], a[4 * g + 2], a[4 * g + 3]};
    } else {
      own = a;
    }
  }
  BARRIER();
  f32x16 v;
#pragma unroll
  for (int g = 0; g < 4; ++g) {
    f32x4 s = f32x4{own[4 * g], own[4 * g + 1], own[4 * g + 2], own[4 * g + 3]};
#pragma unroll
    for (int o = 1; o < 4; ++o) s += red[((((w + o) & 3) * 4 + w) * 4 + g) * 64 + l];
    const unsigned x0 = xo[g >> 1][(g & 1) * 2], x1 = xo[g >> 1][(g & 1) * 2 + 1];
    if (BIAS) {
      s[0] += bflo(bs[g][0]); s[1] += bfhi(bs[g][0]); s[2] += bflo(bs[g][1]); s[3] += bfhi(bs[g][1]);
    }
    v[4 * g + 0] = s[0] + bflo(x0);
    v[4 * g + 1] = s[1] + bfhi(x0);
    v[4 * g + 2] = s[2] + bflo(x1);
    v[4 * g + 3] = s[3] + bfhi(x1);
  }
  tile_finish<FINAL>(v, l, tb, nt, xout, stout, out);
}

#define K2_PR 2
constexpr int K2_O = 0, K2_W = 64 * 1024, K2_LDS = 128 * 1024;
__global__ __launch_bounds__(512, 2) void k_attn_out(const u32x4* __restrict__ phiq,
                                                     const float* __restrict__ kvacc,
                                                     const u32x4* __restrict__ wo_t,
                                                     const u32x4* __restrict__ xin,
                                                     u32x4* __restrict__ xout, f32x2* __restrict__ stout) {
  __shared__ __attribute__((aligned(16))) char smem[K2_LDS];
  const int bid = blockIdx.x, xcd = bid & 7, bi = bid >> 3;
  const int tp = 4 * xcd + bi / 7, ns = bi % 7;
  const int tid = threadIdx.x, w8 = __builtin_amdgcn_readfirstlane(tid >> 6), w = w8 & 3, l = tid & 63;
  const unsigned l16 = l * 16;
  const unsigned l16p = (l & 31) * 16;
  const char* kg = (const char*)kvacc + w * 8192 + 4096;
  const char* pg = (const char*)phiq + (size_t)(2 * tp * H_ + w) * 4096;
  u32x4* ow = (u32x4*)(smem + K2_O) + w * 256 + l;
  auto load = [&](u32x4 (&ld)[16], int j) {
    sfor<0, 16>([&](auto K_) {
      constexpr int k = decltype(K_)::v;
      if constexpr (k < 8) gload<k * 1024 - 4096>(ld[k], (k & 3) == 3 ? l16p : l16, kg + j * 32768);
      else gload<(k & 3) * 1024>(ld[k], l16, pg + j * 16384 + ((k >> 2) & 1) * (H_ * 4096));
    });
  };
  auto produce = [&](u32x4 (&ld)[16], auto J_, auto N_) {
    constexpr int j = decltype(J_)::v, n = decltype(N_)::v;
    u32x4 kvf[4];
    sfor<0, 8>([&](auto K_) { vwait<8 + n>(ld[decltype(K_)::v]); });
#pragma unroll
    for (int i = 0; i < 4; ++i) {
      const f32x4 a = __builtin_bit_cast(f32x4, ld[2 * i]), b = __builtin_bit_cast(f32x4, ld[2 * i + 1]);
      kvf[i] = u32x4{pack2(a[0], a[1]), pack2(a[2], a[3]), pack2(b[0], b[1]), pack2(b[2], b[3])};
    }
    SCHED();
    sfor<0, 2>([&](auto A_) {
      constexpr int a = decltype(A_)::v;
      f32x16 o = zero16();
      sfor<0, 4>([&](auto K_) {
        constexpr int k = decltype(K_)::v;
        vwait<n + 7 - 4 * a - k>(ld[8 + 4 * a + k]);
        o = MFMA32(asfrag(kvf[k]), asfrag(ld[8 + 4 * a + k]), o);
      });
      PINV(o);
      SCHED();
      float dlo, dhi;
      halves(o[12] + o[13], dlo, dhi);
      const float inv = __builtin_amdgcn_rcpf(dhi + 1e-6f);
#pragma unroll
      for (int i = 0; i < 16; ++i) o[i] *= inv;
      ow[(j * 16 + a * 2) * 64] = pack8(o, 0);
      ow[(j * 16 + a * 2 + 1) * 64] = pack8(o, 1);
      SCHED();
    });
  };
  u32x4 la[16], lb[16];
  if (w8 >= 4) {
    STAMP_BEGIN();
    STAMP_AT(10, 0);
    const char* wg = (const char*)wo_t + (size_t)(w * 2 * NNT + 2 * ns) * 1024;
    const unsigned ww = lds_addr(smem) + K2_W + w * 4096;
    auto wdma = [&](int j) {
      glds<2>(l16, wg + j * (8 * NNT * 1024), ww + j * 16384);
      glds<2>(l16, wg + j * (8 * NNT * 1024) + NNT * 1024, ww + j * 16384 + 2048);
    };
    __builtin_amdgcn_s_setprio(2);
    load(la, 1);
    wdma(0);
    wdma(1);
    __builtin_amdgcn_s_setprio(0);
    produce(la, IC<1>{}, IC<8>{});
    vwait_mem<0>();
    STAMP_AT(10, 1);
    BARRIER();
    STAMP_AT(10, 2);
    __builtin_amdgcn_s_setprio(2);
    wdma(2);
    wdma(3);
    load(lb, 3);
    STAMP_AT(10, 3);
    vwait_mem<16>();
    BARRIER();
    __builtin_amdgcn_s_setprio(K2_PR);
    produce(lb, IC<3>{}, IC<0>{});
    BARRIER();
    STAMP_AT(10, 4);
    STAMP_END(10, 5);
    return;
  }
  STAMP_BEGIN();
  STAMP_AT(2, 0);
  const int a = w >> 1, nt = w & 1, tbo = 2 * tp + a, nto = 2 * ns + nt;
  u32x4 xo[2];
  __builtin_amdgcn_s_setprio(3);
  load(la, 0);
  __builtin_amdgcn_s_setprio(1);
  const u32x4* ol = (const u32x4*)(smem + K2_O) + a * 128 + l;
  const u32x4* wl = (const u32x4*)(smem + K2_W) + nt * 64 + l;
  f32x16 acc = zero16();
  auto wread = [&](u32x4 (&wf)[8], int j) {
#pragma unroll
    for (int i = 0; i < 8; ++i) wf[i] = wl[(j * 8 + i) * 128];
  };
  auto consume = [&](u32x4 (&wf)[8], int j) {
    u32x4 of[8];
#pragma unroll
    for (int i = 0; i < 8; ++i) of[i] = ol[((j * 4 + (i >> 1)) * 4 + (i & 1)) * 64];
    SCHED();
#pragma unroll
    for (int i = 0; i < 8; ++i) acc = MFMA32(asfrag(wf[i]), asfrag(of[i]), acc);
    PINV(acc);
    SCHED();
  };
  produce(la, IC<0>{}, IC<0>{});
  load(lb, 2);
  gload<0>(xo[0], l16, (const char*)xin + (size_t)(tbo * KSD + 2 * nto) * 1024);
  gload<1024>(xo[1], l16, (const char*)xin + (size_t)(tbo * KSD + 2 * nto) * 1024);
  STAMP_AT(2, 1);
  BARRIER();
  STAMP_AT(2, 2);
  {
    u32x4 wf[8];
    wread(wf, 0);
    consume(wf, 0);
    wread(wf, 1);
    consume(wf, 1);
  }
  STAMP_AT(2, 3);
  produce(lb, IC<2>{}, IC<2>{});
  BARRIER();
  STAMP_AT(2, 4);
  u32x4 w3[8];
  {
    u32x4 wf[8];
    wread(wf, 2);
    consume(wf, 2);
  }
  wread(w3, 3);
  vwait<0>(xo[0]);
  vwait<0>(xo[1]);
#pragma unroll
  for (int i = 0; i < 8; ++i) {
    acc[2 * i] += bflo(xo[i >> 2][i & 3]);
    acc[2 * i + 1] += bfhi(xo[i >> 2][i & 3]);
  }
  PINV(acc);
  BARRIER();
  STAMP_AT(2, 5);
  consume(w3, 3);
  STAMP_AT(2, 6);
  tile_finish<false>(acc, l, tbo, nto, xout, stout, nullptr);
  STAMP_AT(2, 7);
  STAMP_END(2, 8);
}

#define K3_SIG 16
constexpr int K3_X  = 0;
constexpr int K3_LDS = 112 * 1024;

__global__ __launch_bounds__(512, 2) void k_ffn1(const u32x4* __restrict__ xf, const f32x2* __restrict__ st,
                                                 const u32x4* __restrict__ w1_t, u32x4* __restrict__ hf) {
  __shared__ __attribute__((aligned(16))) char smem[K3_LDS];
  const int bid = blockIdx.x, xcd = bid & 7, bi = bid >> 3;
  const int tt = 4 * (xcd >> 1) + bi / 7, fg = 7 * (xcd & 1) + bi % 7;
  const int tid = threadIdx.x, w8 = __builtin_amdgcn_readfirstlane(tid >> 6), w = w8 & 3;
  const int l = tid & 63, r = l & 31, hh = l >> 5;
  const int ft = 4 * fg + w;
  const unsigned l16 = l * 16;
  const char* xg = (const char*)xf + (size_t)(tt * 4 * KSD + w * 7) * 1024;
  const unsigned xw = lds_addr(smem) + K3_X + w * (7 * 1024);
  auto xdma = [&](int a) {
    glds<4>(l16, xg + a * (KSD * 1024), xw + a * (KSD * 1024));
    glds<3>(l16, xg + a * (KSD * 1024) + 4096, xw + a * (KSD * 1024) + 4096);
  };
  if (w8 >= 4) {
    asm volatile("s_barrier\n\ts_barrier" ::: "memory");
    xdma(2);
    xdma(3);
    vwait_mem<7>();
    asm volatile("s_barrier" ::: "memory");
    vwait_mem<0>();
    asm volatile("s_barrier" ::: "memory");
    return;
  }
  STAMP_BEGIN();
  STAMP_AT(3, 0);
  const char* wg = (const char*)w1_t + (size_t)ft * (KSA * 1024);
  u32x4 wr[KSA];
  f32x2 stv[4];
  sfor<0, 4>([&](auto A_) {
    constexpr int a = decltype(A_)::v;
    gload2<a * 256>(stv[a], r * 8, st + tt * 128);
  });
  xdma(0);
  xdma(1);
  auto wload = [&](auto I_) {
    constexpr int i = decltype(I_)::v;
    if constexpr (i == 0) gload<0>(wr[0], l16, wg + KSD * 1024);
    else gload<((i - 1) & 7) * 1024 - 4096>(wr[i], l16, wg + ((i - 1) >> 3) * 8192 + 4096);
  };
  constexpr int W0 = 11;
  sfor<0, W0>(wload);
  unsigned xo0 = lds_addr(smem) + K3_X + l16, xo1 = xo0 + 2 * KSD * 1024;
  asm volatile("" : "+v"(xo1));
  const lds_u32x4p xl0 = (lds_u32x4p)xo0, xl1 = (lds_u32x4p)xo1;
  u32x4 xa[4];
  float k0[4], k1[4], sd[4];
  {
    const float c0 = -2.f * 0.7978845608028654f * LOG2E;
    const float c1 = c0 * 0.044715f;
#pragma unroll
    for (int a = 0; a < 4; ++a) {
      vwait2<14 + W0>(stv[a]);
      float mean, rstd;
      finish_stats(stv[a], mean, rstd, sd[a]);
      xa[a] = aug_x(mean, sd[a], hh);
      k0[a] = c0 * rstd;
      k1[a] = c1 * rstd * rstd * rstd;
      asm volatile("" : "+v"(xa[a]), "+v"(k0[a]), "+v"(k1[a]), "+v"(sd[a]));
    }
  }
  vwait_mem<W0>();
  BARRIER();
  STAMP_AT(3, 1);
  f32x16 acc[4];
  f32x16 u;
  auto epi = [&](auto A_, auto Q_) {
    constexpr int a = decltype(A_)::v, q = decltype(Q_)::v;
    constexpr int k = q >> 2, g = q & 3;
    const f32x16& y = acc[a];
#pragma unroll
    for (int i = 4 * g; i < 4 * g + 4; ++i) {
      if constexpr (k == 0) u[i] = opq(y[i] * y[i]);
      else if constexpr (k == 1) u[i] = opq(__builtin_fmaf(u[i], k1[a], k0[a]));
      else if constexpr (k == 2) u[i] = opq(u[i] * y[i]);
      else if constexpr (k == 3) u[i] = __builtin_amdgcn_exp2f(u[i]);
      else if constexpr (k == 4) u[i] = opq(__builtin_fmaf(u[i], sd[a], sd[a]));
      else if constexpr (k == 5) u[i] = __builtin_amdgcn_rcpf(u[i]);
      else if constexpr (k == 6) u[i] = opq(u[i] * y[i]);
    }
    if constexpr (k == 6 && (g & 1)) {
      u32x4* dst = hf + (size_t)((tt * 4 + a) * KSF + 2 * ft) * 64 + l;
      st_wt<(g >> 1) * 1024>(dst, pack8(u, g >> 1));
    }
  };
  {
    constexpr int PD = 2;
    u32x4 xb0[KSD], xb1[KSD];
    acc[0] = zero16();
    acc[1] = zero16();
#pragma unroll
    for (int s = 0; s < PD - 1; ++s) { xb0[s] = xl0[s * 64]; xb1[s] = xl0[(KSD + s) * 64]; }
    sfor<0, KSA>([&](auto S_c) {
      constexpr int s = decltype(S_c)::v;
      constexpr int NS = (KSA - W0) / 2;
      if constexpr (s < NS) sfor<W0 + 2 * s, W0 + 2 * s + 2>(wload);
      if constexpr (s == K3_SIG) asm volatile("s_barrier" ::: "memory");
      constexpr int issued = (s < NS ? W0 + 2 * s + 2 : KSA);
      if constexpr (s + PD - 1 < KSD) { xb0[s + PD - 1] = xl0[(s + PD - 1) * 64]; xb1[s + PD - 1] = xl0[(KSD + s + PD - 1) * 64]; }
      vwait<issued - 1 - s>(wr[s]);
      if constexpr (s == 0) {
        acc[0] = MFMA32(asfrag(wr[0]), asfrag(xa[0]), acc[0]);
        acc[1] = MFMA32(asfrag(wr[0]), asfrag(xa[1]), acc[1]);
      } else {
        acc[0] = MFMA32(asfrag(wr[s]), asfrag(xb0[s - 1]), acc[0]);
        acc[1] = MFMA32(asfrag(wr[s]), asfrag(xb1[s - 1]), acc[1]);
      }
      PINV(acc[0]); PINV(acc[1]);
      SCHED();
    });
  }
  STAMP_AT(3, 2);
  sfor<0, 28>([&](auto Q_) { epi(IC<0>{}, Q_); });
  SCHED();
  BARRIER();
  STAMP_AT(3, 3);
  sfor<2, 4>([&](auto A_) {
    constexpr int a = decltype(A_)::v;
    constexpr int PD = 3;
    u32x4 xb[KSD];
    acc[a] = zero16();
#pragma unroll
    for (int s = 0; s < PD - 1; ++s) xb[s] = xl1[((a - 2) * KSD + s) * 64];
    sfor<0, KSA>([&](auto S_c) {
      constexpr int s = decltype(S_c)::v;
      if constexpr (s + PD - 1 < KSD) xb[s + PD - 1] = xl1[((a - 2) * KSD + s + PD - 1) * 64];
      if constexpr (s == 0) acc[a] = MFMA32(asfrag(wr[0]), asfrag(xa[a]), acc[a]);
      else acc[a] = MFMA32(asfrag(wr[s]), asfrag(xb[s - 1]), acc[a]);
      if constexpr (s >= 1) epi(IC<a - 1>{}, IC<s - 1>{});
      SCHED();
    });
    if constexpr (a == 2) BARRIER();
    STAMP_AT(3, 2 + a);
  });
  sfor<0, 28>([&](auto Q_) { epi(IC<3>{}, Q_); });
  STAMP_AT(3, 6);
  STAMP_END(3, 7);
}

template <bool FINAL>
__global__ __launch_bounds__(256, 1) void k_ffn2(const u32x4* __restrict__ hf, const u32x4* __restrict__ w2_t,
                                                 const unsigned short* __restrict__ b2,
                                                 const u32x4* __restrict__ xin, u32x4* __restrict__ xout,
                                                 f32x2* __restrict__ stout, unsigned short* __restrict__ out) {
  __shared__ __attribute__((aligned(16))) char smem[64 * 1024];
  const int bid = blockIdx.x, xcd = bid & 7, bi = bid >> 3;
  const int tp = 4 * xcd + bi / 7, ns = bi % 7;
  const int tid = threadIdx.x, w = __builtin_amdgcn_readfirstlane(tid >> 6), l = tid & 63, hh = l >> 5;
  const int tbo = 2 * tp + (w >> 1), nto = 2 * ns + (w & 1);
  STAMP_BEGIN();
  STAMP_AT(4, 0);
  f32x16 acc[2][2];
#pragma unroll
  for (int a = 0; a < 2; ++a)
#pragma unroll
    for (int b = 0; b < 2; ++b) acc[a][b] = zero16();
  const u32x4* h0 = hf + (size_t)((2 * tp) * KSF + 28 * w) * 64 + l;
  const u32x4* h1 = h0 + (size_t)KSF * 64;
  const u32x4* w0 = w2_t + (size_t)((2 * ns) * KSF + 28 * w) * 64 + l;
  const u32x4* w1p = w0 + (size_t)KSF * 64;
  u32x4 xo[2];
  u32x2 bs[4];
  {
    constexpr int PD = 5;
    u32x4 a0[28], a1[28], b0[28], b1[28];
#pragma unroll
    for (int s = 0; s < PD; ++s) {
      a0[s] = w0[s * 64]; b0[s] = h0[s * 64]; a1[s] = w1p[s * 64]; b1[s] = h1[s * 64];
      SCHED();
    }
#pragma unroll
    for (int s = 0; s < 28; ++s) {
      if (s + PD < 28) {
        a0[s + PD] = w0[(s + PD) * 64]; b0[s + PD] = h0[(s + PD) * 64];
        a1[s + PD] = w1p[(s + PD) * 64]; b1[s + PD] = h1[(s + PD) * 64];
      }
      if (s + PD == 28) {
        xo[0] = xin[(size_t)(tbo * KSD + 2 * nto) * 64 + l];
        xo[1] = xin[(size_t)(tbo * KSD + 2 * nto + 1) * 64 + l];
#pragma unroll
        for (int g = 0; g < 4; ++g) bs[g] = *(const u32x2*)(b2 + 32 * nto + 8 * g + 4 * hh);
      }
      SCHED();
      acc[0][0] = MFMA32(asfrag(a0[s]), asfrag(b0[s]), acc[0][0]);
      acc[0][1] = MFMA32(asfrag(a1[s]), asfrag(b0[s]), acc[0][1]);
      acc[1][0] = MFMA32(asfrag(a0[s]), asfrag(b1[s]), acc[1][0]);
      acc[1][1] = MFMA32(asfrag(a1[s]), asfrag(b1[s]), acc[1][1]);
      SCHED();
    }
  }
  STAMP_AT(4, 1);
  resid_epilogue<FINAL, true>(smem, acc, w, l, tbo, nto, xo, bs, xout, stout, out);
  STAMP_AT(4, 2);
  STAMP_END(4, 3);
}

}

extern "C" void kernel_launch(void* const* d_in, const int* in_sizes, int n_in, void* d_out, int out_size,
                              void* d_ws, size_t ws_size, hipStream_t stream) {
  const unsigned short* x = (const unsigned short*)d_in[0];
  const unsigned short* wqkv = (const unsigned short*)d_in[1];
  const unsigned short* wphi = (const unsigned short*)d_in[2];
  const unsigned short* wo = (const unsigned short*)d_in[3];
  const unsigned short* w1 = (const unsigned short*)d_in[5];
  const unsigned short* b1 = (const unsigned short*)d_in[6];
  const unsigned short* w2 = (const unsigned short*)d_in[7];
  const unsigned short* b2 = (const unsigned short*)d_in[8];
  char* ws = (char*)d_ws;
  unsigned short* out = (unsigned short*)d_out;

  auto XF = [&](int i) { return (u32x4*)(ws + OFF_XF + (size_t)i * XF_BYTES); };
  auto ST = [&](int i) { return (f32x2*)(ws + OFF_ST + (size_t)i * ST_BYTES); };
  const u32x4* wqkv_t = (const u32x4*)(ws + OFF_WQKV);
  const u32x4* wo_t = (const u32x4*)(ws + OFF_WO);
  const u32x4* w1_t = (const u32x4*)(ws + OFF_W1);
  const u32x4* w2_t = (const u32x4*)(ws + OFF_W2);
  u32x4* phiq = (u32x4*)(ws + OFF_PHIQ);
  float* kv = (float*)(ws + OFF_KV);
  u32x4* hf = (u32x4*)(ws + OFF_HF);

  k_prep<<<256, 256, 0, stream>>>(x, wqkv, wphi, wo, w1, b1, w2, ws);
  for (int layer = 0; layer < 2; ++layer) {
    const int i0 = 2 * layer;
    float* kvl = kv + layer * H_ * 2048;
    k_qkv<<<256, 512, 0, stream>>>(XF(i0), ST(i0), wqkv_t, phiq, kvl);
    k_attn_out<<<224, 512, 0, stream>>>(phiq, kvl, wo_t, XF(i0), XF(i0 + 1), ST(i0 + 1));
    k_ffn1<<<224, 512, 0, stream>>>(XF(i0 + 1), ST(i0 + 1), w1_t, hf);
    if (layer == 0)
      k_ffn2<false><<<224, 256, 0, stream>>>(hf, w2_t, b2, XF(i0 + 1), XF(i0 + 2), ST(i0 + 2), nullptr);
    else
      k_ffn2<true><<<224, 256, 0, stream>>>(hf, w2_t, b2, XF(i0 + 1), nullptr, nullptr, out);
  }
}
```

```cpp
#include <hip/hip_runtime.h>
#include <hip/hip_bf16.h>
#include <stdint.h>

typedef __attribute__((ext_vector_type(8))) short bf16x8;
typedef __attribute__((ext_vector_type(4))) short s16x4;
typedef __attribute__((ext_vector_type(16))) float f32x16;
typedef __attribute__((ext_vector_type(4))) float f32x4;
typedef __attribute__((ext_vector_type(2))) float f32x2;
typedef __attribute__((ext_vector_type(4))) unsigned u32x4;
typedef __attribute__((ext_vector_type(2))) unsigned u32x2;
typedef __attribute__((ext_vector_type(2))) __bf16 bf16x2_t;

#define DI __device__ __forceinline__
#define SCHED() __builtin_amdgcn_sched_barrier(0)
#define PIN(x) asm volatile("" : "+a"(x))
#define PINV(x) asm volatile("" : "+v"(x))
#define MFMA32(a, b, c) __builtin_amdgcn_mfma_f32_32x32x16_bf16((a), (b), (c), 0, 0, 0)

#define STAMP_BEGIN()
#define STAMP_AT(kid, i)
#define STAMP_END(kid, n)
#define MS_BEGIN()
#define MS(i)
#define MDBG(i)
#define MS_LAYER()
#define MS_END()

namespace {

constexpr int S_ = 2048, D_ = 448, H_ = 16, DH_ = 28, F_ = 1792;
constexpr int NTB = S_ / 32;
constexpr int KSD = D_ / 16;
constexpr int KSF = F_ / 16;
constexpr int NNT = D_ / 32;
constexpr int NFT = F_ / 32;
constexpr int KSA = KSD + 1;

constexpr size_t XF_BYTES   = (size_t)NTB * KSD * 1024;
constexpr size_t HF_BYTES   = (size_t)NTB * KSF * 1024;
constexpr size_t OF_BYTES   = (size_t)NTB * H_ * 2048;
constexpr size_t KVP_BYTES  = (size_t)H_ * 16 * 16 * 512;
constexpr size_t KVF_BYTES  = (size_t)H_ * 16 * 256;
constexpr size_t EX_BYTES   = OF_BYTES + KVP_BYTES + KVF_BYTES;
constexpr int    WQF        = 3 * KSA;
constexpr size_t OFF_XF     = 0;
constexpr size_t OFF_WQKV   = OFF_XF + 4 * XF_BYTES;
constexpr size_t OFF_WO     = OFF_WQKV + (size_t)H_ * WQF * 1024;
constexpr size_t OFF_W1     = OFF_WO + (size_t)H_ * 2 * NNT * 1024;
constexpr size_t OFF_W2     = OFF_W1 + (size_t)NFT * KSA * 1024;
constexpr size_t OFF_HF     = OFF_W2 + (size_t)NNT * KSF * 1024;
constexpr size_t OFF_EX     = OFF_HF + 2 * HF_BYTES;
constexpr size_t WS_TOTAL   = OFF_EX + 2 * EX_BYTES;
constexpr unsigned SENT = 0xffffffffu;

DI unsigned pack2(float a, float b) {
  f32x2 v = {a, b};
  bf16x2_t r = __builtin_convertvector(v, bf16x2_t);
  return __builtin_bit_cast(unsigned, r);
}
DI float bflo(unsigned u) { return __uint_as_float(u << 16); }
DI float bfhi(unsigned u) { return __uint_as_float(u & 0xffff0000u); }
DI float bf1(unsigned short u) { return __uint_as_float(((unsigned)u) << 16); }
DI bf16x8 asfrag(u32x4 v) { return __builtin_bit_cast(bf16x8, v); }
DI u32x4 pack8(const f32x16& x, int sp) {
  u32x4 p;
  p[0] = pack2(x[8 * sp + 0], x[8 * sp + 1]);
  p[1] = pack2(x[8 * sp + 2], x[8 * sp + 3]);
  p[2] = pack2(x[8 * sp + 4], x[8 * sp + 5]);
  p[3] = pack2(x[8 * sp + 6], x[8 * sp + 7]);
  return p;
}
DI float xor32(float v) { return __shfl_xor(v, 32, 64); }
DI void halves(float v, float& lo, float& hi) {
  auto r = __builtin_amdgcn_permlane32_swap(__float_as_uint(v), __float_as_uint(v), false, false);
  lo = __uint_as_float(r[0]);
  hi = __uint_as_float(r[1]);
}
DI float sum32(float v) { float a, b; halves(v, a, b); return a + b; }
DI float max32(float v) { float a, b; halves(v, a, b); return fmaxf(a, b); }
DI float min32(float v) { float a, b; halves(v, a, b); return fminf(a, b); }
DI float opq(float v) { asm("" : "+v"(v)); return v; }
DI f32x16 zero16() {
  f32x16 z;
#pragma unroll
  for (int i = 0; i < 16; ++i) z[i] = 0.f;
  return z;
}
template <int N>
DI void glds(unsigned voff, const void* sbase, unsigned lds) {
  unsigned keep;
  if constexpr (N == 4)
    asm volatile("s_mov_b32 %0, m0\n\ts_mov_b32 m0, %3\n\ts_nop 4\n\t"
                 "global_load_lds_dwordx4 %1, %2\n\tglobal_load_lds_dwordx4 %1, %2 offset:1024\n\t"
                 "global_load_lds_dwordx4 %1, %2 offset:2048\n\tglobal_load_lds_dwordx4 %1, %2 offset:3072\n\t"
                 "s_mov_b32 m0, %0" : "=&s"(keep) : "v"(voff), "s"(sbase), "s"(lds) : "memory");
  else if constexpr (N == 3)
    asm volatile("s_mov_b32 %0, m0\n\ts_mov_b32 m0, %3\n\ts_nop 4\n\t"
                 "global_load_lds_dwordx4 %1, %2\n\tglobal_load_lds_dwordx4 %1, %2 offset:1024\n\t"
                 "global_load_lds_dwordx4 %1, %2 offset:2048\n\t"
                 "s_mov_b32 m0, %0" : "=&s"(keep) : "v"(voff), "s"(sbase), "s"(lds) : "memory");
  else if constexpr (N == 2)
    asm volatile("s_mov_b32 %0, m0\n\ts_mov_b32 m0, %3\n\ts_nop 4\n\t"
                 "global_load_lds_dwordx4 %1, %2\n\tglobal_load_lds_dwordx4 %1, %2 offset:1024\n\t"
                 "s_mov_b32 m0, %0" : "=&s"(keep) : "v"(voff), "s"(sbase), "s"(lds) : "memory");
  else
    asm volatile("s_mov_b32 %0, m0\n\ts_mov_b32 m0, %3\n\ts_nop 4\n\t"
                 "global_load_lds_dwordx4 %1, %2\n\t"
                 "s_mov_b32 m0, %0" : "=&s"(keep) : "v"(voff), "s"(sbase), "s"(lds) : "memory");
}
template <int OFF>
DI void gload(u32x4& d, unsigned voff, const void* sbase) {
  asm volatile("global_load_dwordx4 %0, %1, %2 offset:%3" : "=v"(d) : "v"(voff), "s"(sbase), "i"(OFF));
}
template <int OFF>
DI void gload2(f32x2& d, unsigned voff, const void* sbase) {
  asm volatile("s_nop 4\n\tglobal_load_dwordx2 %0, %1, %2 offset:%3" : "=v"(d) : "v"(voff), "s"(sbase), "i"(OFF));
}
template <int OFF>
DI void gload_c(u32x4& d, unsigned voff, const void* sbase) {
  asm volatile("global_load_dwordx4 %0, %1, %2 offset:%3 sc1" : "=v"(d) : "v"(voff), "s"(sbase), "i"(OFF));
}
template <int OFF>
DI void greload_c(u32x4& d, unsigned voff, const void* sbase) {
  asm volatile("global_load_dwordx4 %0, %1, %2 offset:%3 sc1" : "+v"(d) : "v"(voff), "s"(sbase), "i"(OFF));
}
template <int OFF>
DI void gload2_c(u32x2& d, unsigned voff, const void* sbase) {
  asm volatile("global_load_dwordx2 %0, %1, %2 offset:%3 sc1" : "=v"(d) : "v"(voff), "s"(sbase), "i"(OFF));
}
template <int N> DI void vwait2u(u32x2& d) { asm volatile("s_waitcnt vmcnt(%1)" : "+v"(d) : "i"(N)); }
template <int OFF>
DI void gload1_c(unsigned& d, unsigned voff, const void* sbase) {
  asm volatile("global_load_dword %0, %1, %2 offset:%3 sc1" : "=v"(d) : "v"(voff), "s"(sbase), "i"(OFF));
}
DI void gload1(unsigned& d, unsigned voff, const void* sbase) {
  asm volatile("global_load_dword %0, %1, %2" : "=v"(d) : "v"(voff), "s"(sbase));
}
template <int N> DI void vwait1(unsigned& d) { asm volatile("s_waitcnt vmcnt(%1)" : "+v"(d) : "i"(N)); }
DI u32x4 nosent(u32x4 v) {
  return u32x4{min(v[0], SENT - 1), min(v[1], SENT - 1), min(v[2], SENT - 1), min(v[3], SENT - 1)};
}
template <int OFF = 0>
DI void st_wt(u32x4* p, u32x4 v) {
  asm volatile("global_store_dwordx4 %0, %1, off offset:%2 sc1\n\ts_nop 1" :: "v"(p), "v"(v), "i"(OFF) : "memory");
}
DI void st_wt2(void* p, u32x2 v) {
  asm volatile("global_store_dwordx2 %0, %1, off sc1\n\ts_nop 1" :: "v"(p), "v"(v) : "memory");
}
DI void st_wt1(float* p, float v) {
  asm volatile("global_store_dword %0, %1, off sc1" :: "v"(p), "v"(v) : "memory");
}
template <int N> DI void vwait(u32x4& d) { asm volatile("s_waitcnt vmcnt(%1)" : "+v"(d) : "i"(N)); }
template <int N> DI void vwait2(f32x2& d) { asm volatile("s_waitcnt vmcnt(%1)" : "+v"(d) : "i"(N)); }
template <int N> DI void vwait_mem() { asm volatile("s_waitcnt vmcnt(%0)" :: "i"(N) : "memory"); }
DI unsigned lds_addr(const void* p) {
  return (unsigned)(uintptr_t)(const __attribute__((address_space(3))) char*)p;
}
DI unsigned bfbits(float v) { return pack2(v, 0.f) & 0xffffu; }
DI void split3(float v, unsigned& p1, unsigned& p2, unsigned& p3) {
  p1 = bfbits(v);
  const float r1 = v - bflo(p1);
  p2 = bfbits(r1);
  p3 = bfbits(r1 - bflo(p2));
}
DI u32x4 aug_w(float cs) {
  unsigned c1, c2, c3;
  split3(cs, c1, c2, c3);
  return u32x4{c1 | (c2 << 16), c3 | (c1 << 16), c2 | (c1 << 16), 0u};
}
DI u32x4 aug_x(float mean, float sd, int hh) {
  unsigned a, b, c;
  split3(hh ? sd : -mean, a, b, c);
  u32x4 lo = {a | (a << 16), a | (b << 16), b | (c << 16), 0u};
  u32x4 hi = {a | (b << 16), c, 0u, 0u};
  return hh ? hi : lo;
}

template <int I> struct IC { static constexpr int v = I; };
template <int B, int E, class Fn>
DI void sfor(Fn&& f) {
  if constexpr (B < E) {
    f(IC<B>{});
    sfor<B + 1, E>(f);
  }
}

#define BARRIER() asm volatile("s_waitcnt lgkmcnt(0)\n\ts_barrier" ::: "memory")

typedef __attribute__((address_space(3))) s16x4* lds_s16x4p;
typedef const __attribute__((address_space(3))) u32x4* lds_u32x4p;
DI s16x4 tr_read(const char* p) { return __builtin_amdgcn_ds_read_tr16_b64_v4i16((lds_s16x4p)p); }
DI u32x4 join(s16x4 a, s16x4 b) {
  bf16x8 v = __builtin_shufflevector(a, b, 0, 1, 2, 3, 4, 5, 6, 7);
  return __builtin_bit_cast(u32x4, v);
}

DI u32x4 frag_from_image(const char* img, int row_stride, int s, int l, int cb) {
  const int i16 = l & 15, q4 = i16 >> 2, p4 = i16 & 3, blk = (l >> 4) & 1, hh = l >> 5;
  const char* p = img + (16 * s + 4 * hh + q4) * row_stride + cb + (4 * blk + p4) * 8;
  return join(tr_read(p), tr_read(p + 8 * row_stride));
}

__global__ __launch_bounds__(256) void k_prep(const unsigned short* __restrict__ x,
                                              const unsigned short* __restrict__ wqkv,
                                              const unsigned short* __restrict__ wphi,
                                              const unsigned short* __restrict__ wo,
                                              const unsigned short* __restrict__ w1,
                                              const unsigned short* __restrict__ b1,
                                              const unsigned short* __restrict__ w2,
                                              char* __restrict__ ws) {
  __shared__ __attribute__((aligned(16))) char img[32 * 912 + 1024];
  float (*red)[32] = (float (*)[32])(img + 32 * 912);
  const int b = blockIdx.x, tid = threadIdx.x, w = tid >> 6, l = tid & 63, r = l & 31, hh = l >> 5;
  STAMP_BEGIN();
  STAMP_AT(0, 0);
  {
    const u32x4 sv = {SENT, SENT, SENT, SENT};
    u32x4* e = (u32x4*)(ws + OFF_EX) + b * 1024 + tid;
    st_wt<0>(e, sv); st_wt<0>(e + 256, sv); st_wt<0>(e + 512, sv); st_wt<0>(e + 768, sv);
    if (b >= 240) st_wt<0>((u32x4*)(ws + OFF_EX + OF_BYTES + KVP_BYTES) + (b - 240) * 256 + tid, sv);
  }

  if (b < 32) {
    const int h = b >> 1, p = b & 1;
    const int cc = r < DH_ ? r : r - DH_;
    u32x4 bfr[2];
    u32x2 lo[4][2], hi[4][2];
#pragma unroll
    for (int it = 0; it < 4; ++it) {
      const int blk = min(w + 4 * it, 13);
      const unsigned short* row = wqkv + (size_t)(32 * blk + r) * (3 * D_) + p * D_ + DH_ * h;
#pragma unroll
      for (int s2 = 0; s2 < 2; ++s2) {
        const int e0 = 16 * s2 + 8 * hh;
        lo[it][s2] = *(const u32x2*)(row + e0);
        hi[it][s2] = *(const u32x2*)(row + min(e0 + 4, 24));
        if (e0 + 4 >= DH_) hi[it][s2] = u32x2{0u, 0u};
      }
    }
#pragma unroll
    for (int s2 = 0; s2 < 2; ++s2) {
      unsigned short v[8];
#pragma unroll
      for (int j = 0; j < 8; ++j) {
        const int e = 16 * s2 + 8 * hh + j;
        v[j] = wphi[(h * DH_ + min(e, DH_ - 1)) * DH_ + cc];
        if (e >= DH_) v[j] = 0;
      }
#pragma unroll
      for (int j = 0; j < 4; ++j) bfr[s2][j] = (unsigned)v[2 * j] | ((unsigned)v[2 * j + 1] << 16);
    }
    float cs = 0.f;
    u32x4* dst = (u32x4*)(ws + OFF_WQKV) + (size_t)(h * WQF + (p ? 0 : 2 * KSD)) * 64;
    const bool ones = p && r == 31;
#pragma unroll
    for (int it = 0; it < 4; ++it) {
      const int blk = w + 4 * it;
      f32x16 acc = zero16();
#pragma unroll
      for (int s2 = 0; s2 < 2; ++s2) {
        u32x4 a = {lo[it][s2][0], lo[it][s2][1], hi[it][s2][0], hi[it][s2][1]};
        acc = MFMA32(asfrag(a), asfrag(bfr[s2]), acc);
      }
      if (blk < 14) {
#pragma unroll
        for (int sp = 0; sp < 2; ++sp) {
          u32x4 pk = pack8(acc, sp);
          if (ones) pk = u32x4{0x3f803f80u, 0x3f803f80u, 0x3f803f80u, 0x3f803f80u};
          st_wt(dst + (2 * blk + sp) * 64 + l, pk);
#pragma unroll
          for (int j = 0; j < 4; ++j) cs += bflo(pk[j]) + bfhi(pk[j]);
        }
      }
    }
    cs += xor32(cs);
    if (hh == 0) red[w][r] = cs;
    __syncthreads();
    if (tid < 64) {
      u32x4 a = aug_w(red[0][r] + red[1][r] + red[2][r] + red[3][r]);
      if (hh || ones) a = u32x4{0u, 0u, 0u, 0u};
      st_wt((u32x4*)(ws + OFF_WQKV) + (size_t)(h * WQF + 3 * KSD + (p ? 0 : 2)) * 64 + l, a);
    }
  } else if (b < 48 || (b >= 64 && b < 176)) {
    u32x4 *dst, *adst;
    bool aug = true;
    u32x4 aughi = {0u, 0u, 0u, 0u};
    if (b < 48) {
      const int h = b - 32;
      dst = (u32x4*)(ws + OFF_WQKV) + (size_t)(h * WQF + KSD) * 64;
      adst = (u32x4*)(ws + OFF_WQKV) + (size_t)(h * WQF + 3 * KSD + 1) * 64;
      aughi[0] = (r == DH_) ? 0x3f80u : (r == DH_ + 1) ? 0x3f800000u : 0u;
      u32x2 v[14];
#pragma unroll
      for (int i = 0; i < 14; ++i) {
        const int idx = tid + 256 * i, row = idx >> 3, c8 = idx & 7;
        v[i] = *(const u32x2*)(wqkv + (size_t)row * (3 * D_) + 2 * D_ + DH_ * h + 4 * min(c8, 6));
        if (c8 == 7) v[i] = u32x2{0u, 0u};
      }
#pragma unroll
      for (int i = 0; i < 14; ++i) *(u32x2*)(img + (tid + 256 * i) * 8) = v[i];
    } else {
      const unsigned short* src;
      int ld;
      if (b < 120) {
        const int ft = b - 64;
        dst = (u32x4*)(ws + OFF_W1) + (size_t)ft * KSA * 64;
        adst = dst + KSD * 64;
        const unsigned bb = b1[32 * ft + r];
        aughi[0] = bb | (bb << 16);
        aughi[1] = bb;
        src = w1 + 32 * ft;
        ld = F_;
      } else {
        const int bb = b - 120, nt = bb >> 2, qd = bb & 3;
        dst = (u32x4*)(ws + OFF_W2) + (size_t)(nt * KSF + qd * 28) * 64;
        aug = false;
        adst = dst;
        src = w2 + (size_t)qd * 448 * D_ + 32 * nt;
        ld = D_;
      }
      u32x4 v[7];
#pragma unroll
      for (int i = 0; i < 7; ++i) {
        const int idx = tid + 256 * i, row = idx >> 2, c = idx & 3;
        v[i] = *(const u32x4*)(src + (size_t)row * ld + 8 * c);
      }
#pragma unroll
      for (int i = 0; i < 7; ++i) *(u32x4*)(img + (tid + 256 * i) * 16) = v[i];
    }
    __syncthreads();
    float cs = 0.f;
#pragma unroll
    for (int i = 0; i < 7; ++i) {
      const int s = w * 7 + i;
      u32x4 pk = frag_from_image(img, 64, s, l, 0);
      st_wt(dst + s * 64 + l, pk);
#pragma unroll
      for (int j = 0; j < 4; ++j) cs += bflo(pk[j]) + bfhi(pk[j]);
    }
    if (aug) {
      cs += xor32(cs);
      if (hh == 0) red[w][r] = cs;
      __syncthreads();
      if (tid < 64) {
        u32x4 a = aug_w(red[0][r] + red[1][r] + red[2][r] + red[3][r]);
        st_wt(adst + l, hh ? aughi : a);
      }
    }
  } else if (b < 64) {
    const int h = b - 48;
    u32x4 v[7];
#pragma unroll
    for (int i = 0; i < 7; ++i) {
      const int idx = tid + 256 * i;
      v[i] = *(const u32x4*)(wo + (size_t)DH_ * h * D_ + min(idx, 28 * 56 - 1) * 8);
      if (idx >= 28 * 56) v[i] = u32x4{0u, 0u, 0u, 0u};
    }
#pragma unroll
    for (int i = 0; i < 7; ++i) *(u32x4*)(img + (tid + 256 * i) * 16) = v[i];
    __syncthreads();
    u32x4* dst = (u32x4*)(ws + OFF_WO) + (size_t)h * 28 * 64;
#pragma unroll
    for (int i = 0; i < 7; ++i) {
      const int fi = w * 7 + i, ks = fi / NNT, nt = fi % NNT;
      st_wt(dst + fi * 64 + l, frag_from_image(img, 896, ks, l, 64 * nt));
    }
  } else if (b < 240) {
    const int tb = b - 176;
    u32x4 v[7];
#pragma unroll
    for (int i = 0; i < 7; ++i) v[i] = *(const u32x4*)(x + (size_t)tb * 32 * D_ + (tid + 256 * i) * 8);
#pragma unroll
    for (int i = 0; i < 7; ++i) {
      const int idx = tid + 256 * i, row = idx / 56, c = idx % 56;
      *(u32x4*)(img + row * 912 + c * 16) = v[i];
    }
    __syncthreads();
    u32x4* dst = (u32x4*)(ws + OFF_XF) + (size_t)tb * KSD * 64;
#pragma unroll
    for (int i = 0; i < 7; ++i) {
      const int s = w * 7 + i;
      const char* p = img + r * 912 + 32 * s + 8 * hh;
      u32x2 lo = *(const u32x2*)p;
      u32x2 hi = *(const u32x2*)(p + 16);
      st_wt(dst + s * 64 + l, nosent(u32x4{lo[0], lo[1], hi[0], hi[1]}));
    }
  }
  STAMP_AT(0, 1);
  STAMP_END(0, 2);
}

DI void finish_stats(f32x2 v, float& mean, float& rstd, float& sd) {
  mean = v[0] * (1.f / D_);
  const float var = fmaxf(v[1] * (1.f / D_) - mean * mean, 0.f) + 1e-5f;
  rstd = rsqrtf(var);
  sd = var * rstd;
}

constexpr float LOG2E = 1.4426950408889634f;
DI float max3(float a, float b, float c) { return fmaxf(fmaxf(a, b), c); }
DI float min3(float a, float b, float c) { return fminf(fminf(a, b), c); }

struct Smx {
  f32x16 e[2];
  float mx, mn, b[2], s[2], inv[2];
  u32x2 pk[2][4];
};
DI void smx_pre(Smx& z, const f32x16& y, float rs2) {
  z.mx = fmaxf(max3(y[0], y[1], y[2]), y[3]);
  z.mn = fminf(min3(y[0], y[1], y[2]), y[3]);
#pragma unroll
  for (int q = 1; q < 4; ++q) {
    z.mx = max3(max3(z.mx, y[4 * q], y[4 * q + 1]), y[4 * q + 2], y[4 * q + 3]);
    z.mn = min3(min3(z.mn, y[4 * q], y[4 * q + 1]), y[4 * q + 2], y[4 * q + 3]);
  }
  z.b[0] = -max32(z.mx) * rs2;
  z.b[1] = min32(z.mn) * rs2;
}
constexpr int SMX_SLOTS = 25;
template <int N, int Q>
DI void smx_slot(Smx& z, const f32x16& y, float rs2, float scale, int hh) {
  f32x16& e = z.e[N];
  if constexpr (Q < 4) {
#pragma unroll
    for (int i = 4 * Q; i < 4 * Q + 4; ++i) e[i] = opq(__builtin_fmaf(y[i], N ? -rs2 : rs2, z.b[N]));
  } else if constexpr (Q < 12) {
    e[2 * Q - 8] = __builtin_amdgcn_exp2f(e[2 * Q - 8]);
    e[2 * Q - 7] = __builtin_amdgcn_exp2f(e[2 * Q - 7]);
  } else if constexpr (Q < 16) {
    constexpr int g = Q - 12;
    float a = opq(opq(e[4 * g] + e[4 * g + 1]) + opq(e[4 * g + 2] + e[4 * g + 3]));
    if constexpr (g == 3) a = hh ? 0.f : a;
    if constexpr (g == 0) z.s[N] = a;
    else z.s[N] = opq(z.s[N] + a);
  } else if constexpr (Q == 16) {
    z.inv[N] = __builtin_amdgcn_rcpf(sum32(z.s[N])) * scale;
  } else {
    constexpr int g = (Q - 17) >> 1;
    if constexpr ((Q - 17) & 1) {
      z.pk[N][g] = u32x2{pack2(e[4 * g], e[4 * g + 1]), pack2(e[4 * g + 2], e[4 * g + 3])};
    } else {
      const float f = (g == 3 && hh) ? 0.f : z.inv[N];
#pragma unroll
      for (int i = 4 * g; i < 4 * g + 4; ++i) e[i] = opq(e[i] * f);
    }
  }
}

#ifndef M_D1
#define M_D1 7
#endif
#ifndef M_D2
#define M_D2 7
#endif
#ifndef M_D3
#define M_D3 30
#endif
#ifndef M_D3S
#define M_D3S 7
#endif
#ifndef M_D4
#define M_D4 8
#endif
#ifndef M_D5
#define M_D5 6
#endif
#ifndef M_A0
#define M_A0 16
#endif
#ifndef M_A1
#define M_A1 22
#endif
#ifndef M_HW1
#define M_HW1 12
#endif
#ifndef M_NP
#define M_NP 2
#endif
#ifndef M_MID
#define M_MID 4
#endif
#ifndef M_HQ
#define M_HQ 14
#endif
constexpr int M_W = 0, M_R = WQF * 1024, M_ST = M_R + 64 * 1024, M_KV = M_ST + 2048, M_FL = M_KV + 4096, M_LDS = M_FL + 16;
constexpr int SPIN_MAX = 1 << 12;
constexpr int NW2G = 18, N2 = 14, N3 = NW2G - N2;
DI unsigned umax3(unsigned a, unsigned b, unsigned c) { return max(max(a, b), c); }
DI unsigned smax(unsigned m, const u32x4& d) { return umax3(umax3(m, d[0], d[1]), d[2], d[3]); }
DI bool has_sent(unsigned m) { return __builtin_amdgcn_ballot_w64(m == SENT) != 0; }
constexpr int hq(int i) { return M_HQ + i < 27 ? M_HQ + i : 27; }
DI float dot2(unsigned a, unsigned b, float c) {
  return __builtin_amdgcn_fdot2_f32_bf16(__builtin_bit_cast(bf16x2_t, a), __builtin_bit_cast(bf16x2_t, b), c, false);
}

template <bool spare>
DI void mega(char* smem, char* __restrict__ ws_, const unsigned short* __restrict__ b2, unsigned short* __restrict__ out) {
  MS_BEGIN();
#pragma unroll 1
  for (int layer = 0; layer < 2; ++layer) {
    char* ws = ws_;
    int bid = blockIdx.x, tid = threadIdx.x;
    asm volatile("" : "+s"(ws), "+s"(bid), "+v"(tid));
    const int xcd = bid & 7, bi = bid >> 3;
    const int g = 2 * xcd + (bi >> 4), h = bi & 15;
    const int tp = 2 * g + (h >= 7), ns = spare ? h - 14 : h >= 7 ? h - 7 : h;
    const int w8 = __builtin_amdgcn_readfirstlane(tid >> 6), w = w8 & 3, l = tid & 63;
    const int r = l & 31, hh = l >> 5;
    const unsigned l16 = l * 16;
    const bool lag = w8 < 4;
    const int a8 = w8 >> 2;
    const int so = 4 * ns + w;
    const unsigned sm = lds_addr(smem);
    const char* wb = ws + OFF_WQKV + (size_t)h * (WQF * 1024);
    const char* w1g = ws + OFF_W1 + (size_t)(8 * ns + w8) * (KSA * 1024);
    auto wdma_kv = [&]() {
#pragma unroll
      for (int c = 0; c < 2; ++c) {
        const int o = ((w >> 1) * KSD + c * 14 + (w & 1) * 7) * 1024;
        glds<4>(l16, wb + o, sm + M_W + o);
        glds<3>(l16, wb + o + 4096, sm + M_W + o + 4096);
      }
    };
    auto wdma_q = [&]() {
      const char* s = wb + (2 * KSD + 7 * w) * 1024;
      const unsigned d = sm + M_W + (2 * KSD + 7 * w) * 1024;
      glds<4>(l16, s, d);
      glds<3>(l16, s + 4096, d + 4096);
    };
    char* ex = ws + OFF_EX + (size_t)layer * EX_BYTES;
    u32x4 wr[KSA];
    auto w1load = [&](auto I_) {
      constexpr int i = decltype(I_)::v;
      if constexpr (!spare) gload<(i & 7) * 1024 - 4096>(wr[i], l16, w1g + (i >> 3) * 8192 + 4096);
    };
    MS(0);
    if (lag) {
      const int tb = 4 * g + w;
      const char* xb = ws + OFF_XF + (size_t)(2 * layer) * XF_BYTES + (size_t)tb * (KSD * 1024);
      const u32x4* wl = (const u32x4*)(smem + M_W) + l;
      u32x4 xr[KSD];
      f32x16 ak, av, aq;
      float q0, q1;
      const unsigned pbo = min(l, KSD - 1) * 1024 + 1020;
      unsigned pb;
      if (layer) __builtin_amdgcn_s_sleep(M_D1);
      gload1_c<0>(pb, pbo, xb);
      sfor<0, KSD>([&](auto S_c) {
        constexpr int s = decltype(S_c)::v;
        gload_c<(s & 7) * 1024 - 4096>(xr[s], l16, xb + (s >> 3) * 8192 + 4096);
      });
      asm volatile("s_barrier" ::: "memory");
      vwait1<KSD>(pb);
      bool bad = has_sent(pb);
      if (__builtin_expect(bad, 0)) {
#pragma unroll 1
        for (int k = 0; k < SPIN_MAX; ++k) {
          MDBG(0);
          gload1_c<0>(pb, pbo, xb);
          vwait1<0>(pb);
          if (!has_sent(pb)) break;
          __builtin_amdgcn_s_sleep(4);
        }
      }
      MS(1);
      int it = 0;
#pragma unroll 1
      do {
        if (__builtin_expect(bad, 0)) {
          MDBG(1);
          sfor<0, KSD>([&](auto S_c) {
            constexpr int s = decltype(S_c)::v;
            greload_c<(s & 7) * 1024 - 4096>(xr[s], l16, xb + (s >> 3) * 8192 + 4096);
          });
        }
        constexpr int PD = 2;
        u32x4 fk[KSD], fv[KSD];
#pragma unroll
        for (int s = 0; s < PD; ++s) { fk[s] = wl[s * 64]; fv[s] = wl[(KSD + s) * 64]; }
        ak = zero16();
        av = zero16();
        q0 = 0.f;
        q1 = 0.f;
        unsigned m = 0;
        sfor<0, KSD>([&](auto S_c) {
          constexpr int s = decltype(S_c)::v;
          if constexpr (s + PD == 14) { if (it == 0) asm volatile("s_barrier" ::: "memory"); }
          if constexpr (s + PD < KSD) { fk[s + PD] = wl[(s + PD) * 64]; fv[s + PD] = wl[(KSD + s + PD) * 64]; }
          vwait<KSD - 1 - s>(xr[s]);
          ak = MFMA32(asfrag(fk[s]), asfrag(xr[s]), ak);
          PINV(ak);
          q0 = dot2(xr[s][0], xr[s][0], q0);
          q1 = dot2(xr[s][1], xr[s][1], q1);
          m = umax3(m, xr[s][0], xr[s][1]);
          SCHED();
          av = MFMA32(asfrag(xr[s]), asfrag(fv[s]), av);
          PINV(av);
          q0 = dot2(xr[s][2], xr[s][2], q0);
          q1 = dot2(xr[s][3], xr[s][3], q1);
          m = umax3(m, xr[s][2], xr[s][3]);
          SCHED();
        });
        bad = has_sent(m);
      } while (__builtin_expect(bad && ++it < SPIN_MAX, 0));
      MS(2);
      float rstd, rs2;
      u32x4 xa;
      constexpr int PD = 4;
      u32x4 f[KSA];
      unsigned qoff = sm + M_W + 2 * KSD * 1024 + l16;
      asm volatile("" : "+v"(qoff));
      const lds_u32x4p wql = (lds_u32x4p)qoff;
      {
        const u32x4 agk = wql[KSD * 64], agv = wql[(KSD + 1) * 64];
        float lo, sum, mean, sd;
        halves(ak[15], lo, sum);
        finish_stats(f32x2{sum, sum32(q0 + q1)}, mean, rstd, sd);
        xa = aug_x(mean, sd, hh);
        rs2 = rstd * LOG2E;
        ak = MFMA32(asfrag(agk), asfrag(xa), ak);
        av = MFMA32(asfrag(xa), asfrag(agv), av);
        PINV(ak); PINV(av);
      }
      SCHED();
      asm volatile("s_barrier" ::: "memory");
#pragma unroll
      for (int s = 0; s < PD; ++s) f[s] = wql[s * 64];
      {
        Smx z;
        u32x4 vfr[2], pa[4];
        char* img = smem + M_R + 32 * 1024 + w * 4096;
        const int i16 = l & 15, q4 = i16 >> 2, p4 = i16 & 3, blk = (l >> 4) & 1;
        f32x4* red = (f32x4*)(smem + M_R) + w * 128 + l;
        auto slot = [&](auto N_, auto Q_) {
          constexpr int n = decltype(N_)::v, q = decltype(Q_)::v;
          smx_slot<n, q>(z, ak, rs2, rstd, hh);
          if constexpr (q >= 17 && ((q - 17) & 1)) {
            constexpr int gg = (q - 17) >> 1;
            const int ch = (2 * gg + hh) ^ ((r >> 1) & 7);
            *(u32x2*)(img + n * 2048 + r * 64 + ch * 8) = z.pk[n][gg];
          }
        };
        aq = MFMA32(asfrag(f[0]), asfrag(xr[0]), zero16());
        PINV(aq);
        ak[15] = hh ? ak[14] : ak[15];
        smx_pre(z, ak, rs2);
        SCHED();
        sfor<1, KSA>([&](auto S_c) {
          constexpr int s = decltype(S_c)::v;
          if constexpr (s + PD - 1 < KSD) f[s + PD - 1] = wql[(s + PD - 1) * 64];
          if constexpr (s + PD - 1 == KSD) f[KSD] = wql[(KSD + 2) * 64];
          if constexpr (s - 1 < SMX_SLOTS) { slot(IC<0>{}, IC<s - 1>{}); slot(IC<1>{}, IC<s - 1>{}); }
          if constexpr (s == 26) {
#pragma unroll
            for (int i = 0; i < 4; ++i) {
              const int t0 = 16 * (i & 1) + 4 * hh + q4, t1 = t0 + 8;
              const int c0 = (4 * blk + p4) ^ ((t0 >> 1) & 7), c1 = (4 * blk + p4) ^ ((t1 >> 1) & 7);
              pa[i] = join(tr_read(img + (i >> 1) * 2048 + t0 * 64 + c0 * 8), tr_read(img + (i >> 1) * 2048 + t1 * 64 + c1 * 8));
            }
            vfr[0] = pack8(av, 0);
            vfr[1] = pack8(av, 1);
          }
          if constexpr (s == KSD) {
            ak = MFMA32(asfrag(pa[0]), asfrag(vfr[0]), zero16());
            av = MFMA32(asfrag(pa[2]), asfrag(vfr[0]), zero16());
            ak = MFMA32(asfrag(pa[1]), asfrag(vfr[1]), ak);
            av = MFMA32(asfrag(pa[3]), asfrag(vfr[1]), av);
            PINV(ak); PINV(av);
            SCHED();
            aq = MFMA32(asfrag(f[s]), asfrag(xa), aq);
          } else {
            aq = MFMA32(asfrag(f[s]), asfrag(xr[s]), aq);
          }
          PINV(aq);
          SCHED();
        });
#pragma unroll
        for (int gg = 0; gg < 4; ++gg) {
          red[gg * 512] = f32x4{ak[4 * gg], ak[4 * gg + 1], ak[4 * gg + 2], ak[4 * gg + 3]};
          red[gg * 512 + 64] = f32x4{av[4 * gg], av[4 * gg + 1], av[4 * gg + 2], av[4 * gg + 3]};
        }
        BARRIER();
        SCHED();
      }
      MS(3);
      u32x4 pq[4];
      {
        Smx z;
        smx_pre(z, aq, rs2);
        sfor<0, SMX_SLOTS>([&](auto Q_) {
          constexpr int q = decltype(Q_)::v;
          smx_slot<0, q>(z, aq, rs2, 1.f, hh);
          smx_slot<1, q>(z, aq, rs2, 1.f, hh);
          if constexpr (!spare && q < 8) {
            glds<2>(l16, ws + OFF_WO + (size_t)((8 * w + q) * NNT + 2 * ns) * 1024, sm + M_W + w * 16384 + q * 2048);
            SCHED();
          }
          if constexpr (q >= 8 && q - 8 < M_A0) { w1load(IC<q - 8>{}); SCHED(); }
        });
        sfor<SMX_SLOTS - 8, M_A0>(w1load);
#pragma unroll
        for (int i = 0; i < 4; ++i)
          pq[i] = u32x4{z.pk[i >> 1][2 * (i & 1)][0], z.pk[i >> 1][2 * (i & 1)][1], z.pk[i >> 1][2 * (i & 1) + 1][0], z.pk[i >> 1][2 * (i & 1) + 1][1]};
      }
      MS(4);
      {
#pragma unroll 1
        for (int k = 0; k < (1 << 19); ++k) {
          if (__builtin_amdgcn_readfirstlane(*(volatile int*)(smem + M_FL)) == layer + 1) break;
          __builtin_amdgcn_s_sleep(1);
        }
        asm volatile("" ::: "memory");
        MS(5);
        const u32x4* kl = (const u32x4*)(smem + M_KV) + l;
        f32x16 o = zero16();
#pragma unroll
        for (int k = 0; k < 4; ++k) o = MFMA32(asfrag(kl[k * 64]), asfrag(pq[k]), o);
        float dlo, dhi;
        halves(o[12] + o[13], dlo, dhi);
        const float inv = __builtin_amdgcn_rcpf(dhi + 1e-6f);
#pragma unroll
        for (int i = 0; i < 16; ++i) o[i] *= inv;
        const u32x4 o0 = nosent(pack8(o, 0)), o1 = nosent(pack8(o, 1));
        u32x4* od = (u32x4*)ex + (size_t)(tb * H_ + h) * 128 + l;
        st_wt<0>(od, o0);
        st_wt<1024>(od, o1);
      }
    } else {
      if (layer == 0) {
        const u32x4 sv = {SENT, SENT, SENT, SENT};
        char* e2 = ex + EX_BYTES + OF_BYTES;
        u32x4* d = (u32x4*)e2 + (size_t)((h * 16 + g) * 8 + 2 * w) * 64 + l;
        st_wt<0>(d, sv); st_wt<1024>(d, sv);
        if (w == 1 && l < 16) st_wt<0>((u32x4*)(e2 + KVP_BYTES) + (h * 16 + g) * 16 + l, sv);
        if (w == 0) glds<3>(l16, wb + 3 * KSD * 1024, sm + M_W + 3 * KSD * 1024);
        wdma_kv();
        if (tid == 256) *(volatile int*)(smem + M_FL) = 0;
      }
      vwait_mem<7>();
      BARRIER();
      vwait_mem<0>();
      asm volatile("s_barrier" ::: "memory");
      if (!spare || layer == 0) wdma_q();
      if (layer == 0) {
        const u32x4 sv = {SENT, SENT, SENT, SENT};
        const int idx = h * 4 + w;
        u32x4* d = (u32x4*)(ws + OFF_HF) + (size_t)(4 * g * KSF + idx * 7) * 64 + l;
        st_wt<0>(d, sv); st_wt<1024>(d, sv); st_wt<2048>(d, sv); st_wt<3072>(d, sv);
        st_wt<0>(d + 256, sv); st_wt<1024>(d + 256, sv); st_wt<2048>(d + 256, sv);
        d = (u32x4*)(ws + OFF_XF + XF_BYTES) + (size_t)(4 * g * KSD + min(idx, 55) * 2) * 64 + l;
        st_wt<0>(d, sv); st_wt<1024>(d, sv);
        st_wt<0>(d + XF_BYTES / 16, sv); st_wt<1024>(d + XF_BYTES / 16, sv);
        d = (u32x4*)(ex + EX_BYTES) + (size_t)(4 * g * H_ + idx) * 128 + l;
        st_wt<0>(d, sv); st_wt<1024>(d, sv);
      }
      vwait_mem<0>();
      asm volatile("s_barrier" ::: "memory");
      MS(1);
      sfor<0, M_HW1>(w1load);
      __builtin_amdgcn_s_setprio(3);
      const f32x4* redr = (const f32x4*)(smem + M_R) + w * 512 + l;
      u32x4* kd = (u32x4*)(ex + OF_BYTES) + (size_t)((h * 16 + 4 * (w >> 1) + (l >> 4)) * 16 + g) * 32 + (w & 1) * 16 + (l & 15);
      asm volatile("s_barrier" ::: "memory");
      {
        const f32x4 s0 = (redr[0] + redr[128]) + (redr[256] + redr[384]);
        const f32x4 s1 = (redr[64] + redr[192]) + (redr[320] + redr[448]);
        st_wt<0>(kd, nosent(__builtin_bit_cast(u32x4, s0)));
        st_wt<0>(kd + 8 * 16 * 32, nosent(__builtin_bit_cast(u32x4, s1)));
      }
      MS(3);
      char* kf = ex + OF_BYTES + KVP_BYTES + h * 4096;
      if (w < M_NP) {
        const char* kp = ex + OF_BYTES + (size_t)(h * 16 + g) * 8192 + 4096;
        u32x4 pv[8];
        __builtin_amdgcn_s_sleep(M_D2);
        if (w) __builtin_amdgcn_s_sleep(12);
#pragma unroll 1
        for (int it = 0; it < SPIN_MAX; ++it) {
          sfor<0, 8>([&](auto I_) { gload_c<decltype(I_)::v * 1024 - 4096>(pv[decltype(I_)::v], l16, kp); });
          unsigned m = 0;
          sfor<0, 8>([&](auto I_) {
            vwait<0>(pv[decltype(I_)::v]);
            m = smax(m, pv[decltype(I_)::v]);
          });
          if (!has_sent(m)) break;
          MDBG(0);
        }
        f32x4 s[4];
#pragma unroll
        for (int i = 0; i < 4; ++i) s[i] = __builtin_bit_cast(f32x4, pv[2 * i]) + __builtin_bit_cast(f32x4, pv[2 * i + 1]);
        const f32x4 t = (s[0] + s[1]) + (s[2] + s[3]);
        const u32x2 pk = {min(pack2(sum32(t[0]), sum32(t[1])), SENT - 1), min(pack2(sum32(t[2]), sum32(t[3])), SENT - 1)};
        if (hh == 0) st_wt2(kf + g * 256 + (l & 15) * 16 + (l >> 4) * 8, pk);
        MS(4);
        __builtin_amdgcn_s_sleep(M_D3S);
      } else {
        __builtin_amdgcn_s_sleep(M_D3);
        if (w & 1) __builtin_amdgcn_s_sleep(12);
      }
      {
        u32x4 kv[4];
        bool bad = true;
#pragma unroll 1
        for (int it = 0; it < SPIN_MAX; ++it) {
          sfor<0, 4>([&](auto I_) { gload_c<decltype(I_)::v * 1024>(kv[decltype(I_)::v], l16, kf); });
          unsigned m = 0;
          sfor<0, 4>([&](auto I_) {
            vwait<0>(kv[decltype(I_)::v]);
            m = smax(m, kv[decltype(I_)::v]);
          });
          bad = has_sent(m);
          if (!bad) break;
          if (__builtin_amdgcn_readfirstlane(*(volatile int*)(smem + M_FL)) == layer + 1) break;
          MDBG(2);
        }
        if (!bad) {
#pragma unroll
          for (int i = 0; i < 4; ++i) ((u32x4*)(smem + M_KV))[i * 64 + l] = kv[i];
        }
        asm volatile("s_waitcnt lgkmcnt(0)" ::: "memory");
        *(volatile int*)(smem + M_FL) = layer + 1;
      }
      __builtin_amdgcn_s_setprio(0);
      MS(5);
      sfor<M_HW1, M_A0>(w1load);
    }
    asm volatile("s_barrier" ::: "memory");
    MS(6);
    if constexpr (spare) {
      if (layer == 0) {
        const u32x4 sv = {SENT, SENT, SENT, SENT};
        const int idx = ns * 8 + w8;
        u32x4* d = (u32x4*)(ws + OFF_HF + HF_BYTES) + (size_t)(4 * g * KSF + idx * 28) * 64 + l;
#pragma unroll 1
        for (int i = 0; i < 7; ++i) {
          st_wt<0>(d, sv); st_wt<1024>(d, sv); st_wt<2048>(d, sv); st_wt<3072>(d, sv);
          d += 256;
          __builtin_amdgcn_s_sleep(16);
        }
        d = (u32x4*)(ws + OFF_XF + 3 * XF_BYTES) + (size_t)(4 * g * KSD + idx * 7) * 64 + l;
        st_wt<0>(d, sv); st_wt<1024>(d, sv); st_wt<2048>(d, sv); st_wt<3072>(d, sv);
        st_wt<0>(d + 256, sv); st_wt<1024>(d + 256, sv); st_wt<2048>(d + 256, sv);
#pragma unroll 1
        for (int k = 0; k < SPIN_MAX; ++k) {
          unsigned pb;
          gload1_c<0>(pb, (l & 15) * 2048, ex + (size_t)(4 * g * H_) * 2048);
          vwait1<0>(pb);
          if (!has_sent(pb)) break;
          __builtin_amdgcn_s_sleep(8);
        }
#pragma unroll 1
        for (int i = 0; i < 2; ++i) __builtin_amdgcn_s_sleep(127);
      }
      MS_LAYER();
    } else {
    int pofs[7];
#pragma unroll
    for (int j = 0; j < 7; ++j) pofs[j] = ns + j >= 7 ? ns + j - 7 : ns + j;
    f32x16 c0, c1;
    u32x4 xres;
    auto pwrite = [&](int off) {
      f32x4* pw = (f32x4*)(smem + M_R + off) + w * 512 + l;
#pragma unroll
      for (int gg = 0; gg < 4; ++gg) {
        pw[gg * 64] = f32x4{c0[4 * gg], c0[4 * gg + 1], c0[4 * gg + 2], c0[4 * gg + 3]};
        pw[(4 + gg) * 64] = f32x4{c1[4 * gg], c1[4 * gg + 1], c1[4 * gg + 2], c1[4 * gg + 3]};
      }
    };
    f32x4 sum[2];
    auto padd = [&](int off) {
      const f32x4* pr = (const f32x4*)(smem + M_R + off) + w * 128 + l;
      f32x4 pv[8];
#pragma unroll
      for (int i = 0; i < 8; ++i) pv[i] = pr[((i >> 1) * 8 + (i & 1)) * 64];
      SCHED();
#pragma unroll
      for (int gg = 0; gg < 2; ++gg) sum[gg] += (pv[gg] + pv[2 + gg]) + (pv[4 + gg] + pv[6 + gg]);
      PINV(sum[0]); PINV(sum[1]);
    };
    auto psum = [&]() {
      return nosent(u32x4{pack2(sum[0][0], sum[0][1]), pack2(sum[0][2], sum[0][3]), pack2(sum[1][0], sum[1][1]), pack2(sum[1][2], sum[1][3])});
    };
    {
      const char* og = ex + (size_t)((2 * tp + a8) * H_ + 4 * w) * 2048 + 4096;
      const u32x4* wl = (const u32x4*)(smem + M_W) + w * 1024 + l;
      u32x4 of[8];
      auto oissue = [&]() { sfor<0, 8>([&](auto I_) { gload_c<decltype(I_)::v * 1024 - 4096>(of[decltype(I_)::v], l16, og); }); };
      auto otake = [&](auto N_) {
        constexpr int n = decltype(N_)::v;
        unsigned m = 0;
        u32x4 wf[8][2];
        auto wrd = [&](int i) {
          wf[i][0] = wl[i * 128];
          wf[i][1] = wl[i * 128 + 64];
        };
        wrd(0);
        wrd(1);
        c0 = zero16();
        c1 = zero16();
        sfor<0, 8>([&](auto I_) {
          constexpr int i = decltype(I_)::v;
          if constexpr (i + 2 < 8) wrd(i + 2);
          vwait<n + 7 - i>(of[i]);
          c0 = MFMA32(asfrag(wf[i][0]), asfrag(of[i]), c0);
          c1 = MFMA32(asfrag(wf[i][1]), asfrag(of[i]), c1);
          PINV(c0); PINV(c1);
          m = smax(m, of[i]);
          SCHED();
        });
        return has_sent(m);
      };
      gload_c<0>(xres, l16, ws + OFF_XF + (size_t)(2 * layer) * XF_BYTES + (size_t)((2 * tp + a8) * KSD + so) * 1024);
      __builtin_amdgcn_s_sleep(M_D4);
      oissue();
      sfor<M_A0, M_A1>(w1load);
      vwait_mem<11 + M_A1>();
      asm volatile("s_barrier" ::: "memory");
      if (__builtin_expect(otake(IC<M_A1 - M_A0>{}), 0)) {
        for (int it = 0; it < SPIN_MAX; ++it) {
          MDBG(3);
          oissue();
          if (!otake(IC<0>{})) break;
        }
      }
    }
    MS(7);
    pwrite(a8 * 32768);
    PINV(xres);
#pragma unroll
    for (int gg = 0; gg < 2; ++gg)
      sum[gg] = f32x4{bflo(xres[2 * gg]), bfhi(xres[2 * gg]), bflo(xres[2 * gg + 1]), bfhi(xres[2 * gg + 1])};
    BARRIER();
    padd(a8 * 32768);
    xres = psum();
    char* x1 = ws + OFF_XF + (size_t)(2 * layer + 1) * XF_BYTES + (size_t)((2 * tp + a8) * KSD + w) * 1024;
    st_wt<0>((u32x4*)(x1 + ns * 4096) + l, xres);
    MS(8);
    const unsigned l16b = l16 + KSF * 1024;
    const char* w2g = ws + OFF_W2 + (size_t)(2 * ns * KSF + 2 * w8) * 1024;
    {
      u32x4 xr[6];
      u32x4* xw = (u32x4*)(smem + M_R) + (a8 * KSD + w) * 64 + l;
      auto xissue = [&]() { sfor<0, 6>([&](auto I_) { gload_c<0>(xr[decltype(I_)::v], l16, x1 + pofs[decltype(I_)::v + 1] * 4096); }); };
      float o1 = 0.f, o2 = 0.f;
      auto rsum = [&](const u32x4& v, float& s1, float& s2) {
#pragma unroll
        for (int j = 0; j < 4; ++j) {
          s1 = dot2(v[j], 0x3f803f80u, s1);
          s2 = dot2(v[j], v[j], s2);
        }
      };
      auto xtake = [&](auto N_) {
        constexpr int n = decltype(N_)::v;
        unsigned m = 0;
        float s1 = o1, s2 = o2;
        sfor<0, 6>([&](auto I_) {
          constexpr int i = decltype(I_)::v;
          vwait<n + 5 - i>(xr[i]);
          xw[pofs[i + 1] * 256] = xr[i];
          m = smax(m, xr[i]);
          rsum(xr[i], s1, s2);
          SCHED();
        });
        auto sw = __builtin_amdgcn_permlane32_swap(__float_as_uint(s1), __float_as_uint(s2), false, false);
        ((float*)(smem + M_ST))[w8 * 64 + l] = __uint_as_float(sw[0]) + __uint_as_float(sw[1]);
        return has_sent(m);
      };
      rsum(xres, o1, o2);
      __builtin_amdgcn_s_sleep(M_D5);
      xissue();
      asm volatile("s_barrier" ::: "memory");
      xw[ns * 256] = xres;
      sfor<M_A1, KSA>(w1load);
      sfor<NW2G, 28>([&](auto I_) {
        constexpr int i = decltype(I_)::v;
        glds<1>((i & 1) ? l16b : l16, w2g + pofs[i >> 2] * 16384 + ((i >> 1) & 1) * 1024, sm + M_W + (w8 * 10 + i - NW2G) * 1024);
      });
      if (__builtin_expect(xtake(IC<KSA - M_A1 + 28 - NW2G>{}), 0)) {
        for (int it = 0; it < SPIN_MAX; ++it) {
          MDBG(1);
          xissue();
          if (!xtake(IC<0>{})) break;
        }
      }
    }
    MS(9);
    BARRIER();
    MS(10);
    u32x4 bq0 = ((const u32x4*)(b2 + 16 * so))[0], bq1 = ((const u32x4*)(b2 + 16 * so))[1];
    char* hg = ws + OFF_HF + (size_t)layer * HF_BYTES + (size_t)(2 * tp * KSF + 2 * w8) * 1024;
    u32x4 w2r[28];
    auto w2load = [&](auto I_) {
      constexpr int i = decltype(I_)::v;
      gload<((i >> 1) & 1) * 1024>(w2r[i], (i & 1) ? l16b : l16, w2g + pofs[i >> 2] * 16384);
    };
    unsigned xo0 = sm + M_R + l16;
    asm volatile("" : "+v"(xo0));
    const lds_u32x4p xl = (lds_u32x4p)xo0;
    constexpr int PD = 3;
    u32x4 xb[2][KSA];
#pragma unroll
    for (int s = 0; s < PD; ++s) xb[0][s] = xl[s * 64];
    if (lag) asm volatile("s_barrier" ::: "memory");
    f32x16 acc1, u;
    u32x4 hown[2][2];
    float k0, k1, sd;
    auto epi = [&](auto A_, auto Q_) {
      constexpr int a = decltype(A_)::v, q = decltype(Q_)::v;
      constexpr int k = q >> 2, gg = q & 3;
      const f32x16& y = acc1;
#pragma unroll
      for (int i = 4 * gg; i < 4 * gg + 4; ++i) {
        if constexpr (k == 0) u[i] = opq(y[i] * y[i]);
        else if constexpr (k == 1) u[i] = opq(__builtin_fmaf(u[i], k1, k0));
        else if constexpr (k == 2) u[i] = opq(u[i] * y[i]);
        else if constexpr (k == 3) u[i] = __builtin_amdgcn_exp2f(u[i]);
        else if constexpr (k == 4) u[i] = opq(__builtin_fmaf(u[i], sd, sd));
        else if constexpr (k == 5) u[i] = __builtin_amdgcn_rcpf(u[i]);
        else if constexpr (k == 6) u[i] = opq(u[i] * y[i]);
      }
      if constexpr (k == 6 && (gg & 1)) {
        hown[a][gg >> 1] = nosent(pack8(u, gg >> 1));
        st_wt<(gg >> 1) * 1024>((u32x4*)(hg + ns * 16384 + a * (KSF * 1024)) + l, hown[a][gg >> 1]);
      }
    };
    u32x4 hr[12];
    auto hload1 = [&](auto A_, auto I_) {
      constexpr int a = decltype(A_)::v, i = decltype(I_)::v;
      gload_c<(i & 1) * 1024>(hr[i], a ? l16b : l16, hg + pofs[1 + (i >> 1)] * 16384);
    };
    auto hload = [&](auto A_, auto J_) {
      hload1(A_, IC<2 * decltype(J_)::v - 2>{});
      hload1(A_, IC<2 * decltype(J_)::v - 1>{});
    };
    sfor<0, 2>([&](auto A_) {
      constexpr int a = decltype(A_)::v;
      acc1 = zero16();
      float sp[4], s1, s2, mean, rstd, v, r1;
      unsigned p1, p2, p3;
      sfor<0, KSA>([&](auto S_c) {
        constexpr int s = decltype(S_c)::v;
        if constexpr (s + PD < KSD) xb[a][s + PD] = xl[(a * KSD + s + PD) * 64];
        if constexpr (s == 2) {
#pragma unroll
          for (int i = 0; i < 4; ++i) sp[i] = ((const float*)(smem + M_ST))[(4 * a + i) * 64 + l];
        }
        if constexpr (s == 6) { halves((sp[0] + sp[1]) + (sp[2] + sp[3]), s1, s2); PINV(s1); PINV(s2); }
        if constexpr (s == 8) { finish_stats(f32x2{s1, s2}, mean, rstd, sd); PINV(mean); PINV(rstd); PINV(sd); }
        if constexpr (s == 10) {
          const float e0 = -2.f * 0.7978845608028654f * LOG2E;
          k0 = e0 * rstd;
          k1 = (e0 * 0.044715f) * rstd * rstd * rstd;
          v = hh ? sd : -mean;
          PINV(k0); PINV(k1); PINV(v);
        }
        if constexpr (s == 12) { p1 = bfbits(v); r1 = v - bflo(p1); PINV(p1); PINV(r1); }
        if constexpr (s == 14) { p2 = bfbits(r1); p3 = bfbits(r1 - bflo(p2)); PINV(p2); PINV(p3); }
        if constexpr (s == 16) {
          const unsigned ab = p1 | (p2 << 16);
          xb[a][KSD] = u32x4{hh ? ab : (p1 | (p1 << 16)), hh ? p3 : ab, hh ? 0u : (p2 | (p3 << 16)), 0u};
          PINV(xb[a][KSD]);
        }
        if constexpr (a == 0) vwait<(s >= M_A1 ? KSD - s : KSA - M_A1) + 28 - NW2G>(wr[s]);
        if constexpr (a == 1 && s >= 4 && (s - 4) % 6 == 0 && (s - 4) / 6 < N3) w2load(IC<N2 + (s - 4) / 6>{});
        acc1 = MFMA32(asfrag(wr[s]), asfrag(xb[a][s]), acc1);
        PINV(acc1);
        SCHED();
      });
      BARRIER();
      SCHED();
      MS(11 + 2 * a);
      if constexpr (a == 1) vwait_mem<NW2G + 2>();
      sfor<0, 28>([&](auto Q_) {
        constexpr int q = decltype(Q_)::v;
        epi(A_, Q_);
        if constexpr (a == 0 && (q & 1) && (q >> 1) < N2) { w2load(IC<(q >> 1)>{}); SCHED(); }
        if constexpr (a == 1 && NW2G + q < 28) { w2r[NW2G + q] = ((const u32x4*)(smem + M_W))[(w8 * 10 + q) * 64 + l]; SCHED(); }
        if constexpr (a == 1) sfor<0, 12>([&](auto I_) { if constexpr (hq(decltype(I_)::v) == q) { hload1(IC<0>{}, I_); SCHED(); } });
      });
      SCHED();
      if constexpr (a == 0) {
#pragma unroll
        for (int s = 0; s < PD; ++s) xb[1][s] = xl[(KSD + s) * 64];
      }
      BARRIER();
      SCHED();
      MS(12 + 2 * a);
    });
    sfor<0, 4>([&](auto I_) { vwait<NW2G - 4 + 4 + 12>(w2r[decltype(I_)::v]); });
    auto gemm2 = [&](auto A_) {
      constexpr int a = decltype(A_)::v;
      bool bad;
      auto check = [&](auto J_) {
        constexpr int j = decltype(J_)::v;
        constexpr int n = 12 - 2 * j + (a == 0 ? (hq(2 * j - 1) < 25) + (hq(2 * j - 1) < 27) : 0);
        vwait<n>(hr[2 * j - 2]);
        vwait<n>(hr[2 * j - 1]);
        bad = has_sent(max(smax(0u, hr[2 * j - 2]), smax(0u, hr[2 * j - 1])));
      };
      c0 = MFMA32(asfrag(w2r[0]), asfrag(hown[a][0]), zero16());
      c1 = MFMA32(asfrag(w2r[1]), asfrag(hown[a][0]), zero16());
      PINV(c0); PINV(c1);
      SCHED();
      check(IC<1>{});
      SCHED();
      c0 = MFMA32(asfrag(w2r[2]), asfrag(hown[a][1]), c0);
      c1 = MFMA32(asfrag(w2r[3]), asfrag(hown[a][1]), c1);
      PINV(c0); PINV(c1);
      SCHED();
      sfor<1, 7>([&](auto J_) {
        constexpr int j = decltype(J_)::v;
        u32x4 &h0 = hr[2 * j - 2], &h1 = hr[2 * j - 1];
        if (__builtin_expect(bad, 0)) {
          for (int it = 0; it < SPIN_MAX; ++it) {
            MDBG(2);
            hload(A_, J_);
            vwait<0>(h0);
            vwait<0>(h1);
            if (!has_sent(smax(smax(0u, h0), h1))) break;
          }
        }
        c0 = MFMA32(asfrag(w2r[4 * j]), asfrag(h0), c0);
        c1 = MFMA32(asfrag(w2r[4 * j + 1]), asfrag(h0), c1);
        PINV(c0); PINV(c1);
        SCHED();
        if constexpr (j < 6) check(IC<j + 1>{});
        if constexpr (a == 1 && j == M_MID) { if (lag) asm volatile("s_barrier" ::: "memory"); }
        SCHED();
        c0 = MFMA32(asfrag(w2r[4 * j + 2]), asfrag(h1), c0);
        c1 = MFMA32(asfrag(w2r[4 * j + 3]), asfrag(h1), c1);
        PINV(c0); PINV(c1);
        SCHED();
      });
    };
    gemm2(IC<0>{});
    asm volatile("s_barrier" ::: "memory");
    SCHED();
    MS(15);
    sfor<1, 7>([&](auto J_) { hload(IC<1>{}, J_); });
    pwrite(lag ? 32768 : 0);
    {
      const unsigned b00 = bq0[0], b01 = bq0[1], b02 = bq0[2], b03 = bq0[3], b10 = bq1[0], b11 = bq1[1], b12 = bq1[2], b13 = bq1[3];
      const unsigned m = hh ? ~0u : 0u;
      const u32x2 bs[2] = {u32x2{(b02 & m) | (b00 & ~m), (b03 & m) | (b01 & ~m)}, u32x2{(b12 & m) | (b10 & ~m), (b13 & m) | (b11 & ~m)}};
#pragma unroll
      for (int gg = 0; gg < 2; ++gg) {
        sum[gg][0] = bflo(bs[gg][0]) + bflo(xres[2 * gg]);
        sum[gg][1] = bfhi(bs[gg][0]) + bfhi(xres[2 * gg]);
        sum[gg][2] = bflo(bs[gg][1]) + bflo(xres[2 * gg + 1]);
        sum[gg][3] = bfhi(bs[gg][1]) + bfhi(xres[2 * gg + 1]);
      }
      PINV(sum[0]); PINV(sum[1]);
    }
    if (lag) padd(0);
    BARRIER();
    SCHED();
    MS(16);
    if (lag) padd(32768);
    gemm2(IC<1>{});
    if (!lag) asm volatile("s_barrier" ::: "memory");
    SCHED();
    MS(17);
    pwrite(lag ? 32768 : 0);
    BARRIER();
    SCHED();
    if (!lag) {
      padd(0);
      if (layer == 0) wdma_kv();
      asm volatile("s_barrier" ::: "memory");
      padd(32768);
    }
    SCHED();
    MS(18);
    xres = psum();
    if (layer) {
      unsigned short* o = out + (size_t)((2 * tp + a8) * 32 + r) * D_ + 16 * so + 4 * hh;
      *(u32x2*)(o + 0) = u32x2{xres[0], xres[1]};
      *(u32x2*)(o + 8) = u32x2{xres[2], xres[3]};
    } else {
      st_wt<0>((u32x4*)(ws + OFF_XF + 2 * XF_BYTES) + (size_t)((2 * tp + a8) * KSD + so) * 64 + l, xres);
    }
    MS(19);
    MS_LAYER();
    }
  }
  MS_END();
}

__global__ __launch_bounds__(512, 2) void k_mega(char* __restrict__ ws, const unsigned short* __restrict__ b2,
                                                 unsigned short* __restrict__ out) {
  __shared__ __attribute__((aligned(16))) char smem[M_LDS];
  if (((blockIdx.x >> 3) & 15) >= 14) mega<true>(smem, ws, b2, out);
  else mega<false>(smem, ws, b2, out);
}

}

extern "C" void kernel_launch(void* const* d_in, const int* in_sizes, int n_in, void* d_out, int out_size,
                              void* d_ws, size_t ws_size, hipStream_t stream) {
  const unsigned short* x = (const unsigned short*)d_in[0];
  const unsigned short* wqkv = (const unsigned short*)d_in[1];
  const unsigned short* wphi = (const unsigned short*)d_in[2];
  const unsigned short* wo = (const unsigned short*)d_in[3];
  const unsigned short* w1 = (const unsigned short*)d_in[5];
  const unsigned short* b1 = (const unsigned short*)d_in[6];
  const unsigned short* w2 = (const unsigned short*)d_in[7];
  const unsigned short* b2 = (const unsigned short*)d_in[8];
  char* ws = (char*)d_ws;
  k_prep<<<256, 256, 0, stream>>>(x, wqkv, wphi, wo, w1, b1, w2, ws);
  k_mega<<<256, 512, 0, stream>>>(ws, b2, (unsigned short*)d_out);
}
```

```cpp
#include <hip/hip_runtime.h>
#include <hip/hip_bf16.h>
#include <stdint.h>

typedef __attribute__((ext_vector_type(8))) short bf16x8;
typedef __attribute__((ext_vector_type(4))) short s16x4;
typedef __attribute__((ext_vector_type(16))) float f32x16;
typedef __attribute__((ext_vector_type(4))) float f32x4;
typedef __attribute__((ext_vector_type(2))) float f32x2;
typedef __attribute__((ext_vector_type(4))) unsigned u32x4;
typedef __attribute__((ext_vector_type(2))) unsigned u32x2;
typedef __attribute__((ext_vector_type(2))) __bf16 bf16x2_t;

#define DI __device__ __forceinline__
#define SCHED() __builtin_amdgcn_sched_barrier(0)
#define PIN(x) asm volatile("" : "+a"(x))
#define PINV(x) asm volatile("" : "+v"(x))
#define MFMA32(a, b, c) __builtin_amdgcn_mfma_f32_32x32x16_bf16((a), (b), (c), 0, 0, 0)

#define STAMP_BEGIN()
#define STAMP_AT(kid, i)
#define STAMP_END(kid, n)
#define MS_BEGIN()
#define MS(i)
#define MDBG(i)
#define MS_LAYER()
#define MS_END()

namespace {

constexpr int S_ = 2048, D_ = 448, H_ = 16, DH_ = 28, F_ = 1792;
constexpr int NTB = S_ / 32;
constexpr int KSD = D_ / 16;
constexpr int KSF = F_ / 16;
constexpr int NNT = D_ / 32;
constexpr int NFT = F_ / 32;
constexpr int KSA = KSD + 1;

constexpr size_t XF_BYTES   = (size_t)NTB * KSD * 1024;
constexpr size_t HF_BYTES   = (size_t)NTB * KSF * 1024;
constexpr size_t OF_BYTES   = (size_t)NTB * H_ * 2048;
constexpr size_t KVP_BYTES  = (size_t)H_ * 4 * 16 * 1024;
constexpr size_t EX_BYTES   = OF_BYTES + KVP_BYTES;
constexpr int    WQF        = 3 * KSA;
constexpr size_t OFF_XF     = 0;
constexpr size_t OFF_WQKV   = OFF_XF + 4 * XF_BYTES;
constexpr size_t OFF_WO     = OFF_WQKV + (size_t)H_ * WQF * 1024;
constexpr size_t OFF_W1     = OFF_WO + (size_t)H_ * 2 * NNT * 1024;
constexpr size_t OFF_W2     = OFF_W1 + (size_t)NFT * KSA * 1024;
constexpr size_t OFF_HF     = OFF_W2 + (size_t)NNT * KSF * 1024;
constexpr size_t OFF_EX     = OFF_HF + 2 * HF_BYTES;
constexpr size_t XS_BYTES   = (size_t)NTB * 7 * 1024;
constexpr size_t OFF_XS     = OFF_EX + 2 * EX_BYTES;
constexpr size_t OFF_AUG    = OFF_XS + 2 * XS_BYTES;
constexpr size_t WS_TOTAL   = OFF_AUG + 256 * 8 * 1024;
constexpr unsigned SENT = 0xffffffffu;

DI unsigned pack2(float a, float b) {
  f32x2 v = {a, b};
  bf16x2_t r = __builtin_convertvector(v, bf16x2_t);
  return __builtin_bit_cast(unsigned, r);
}
DI float bflo(unsigned u) { return __uint_as_float(u << 16); }
DI float bfhi(unsigned u) { return __uint_as_float(u & 0xffff0000u); }
DI float bf1(unsigned short u) { return __uint_as_float(((unsigned)u) << 16); }
DI bf16x8 asfrag(u32x4 v) { return __builtin_bit_cast(bf16x8, v); }
DI u32x4 pack8(const f32x16& x, int sp) {
  u32x4 p;
  p[0] = pack2(x[8 * sp + 0], x[8 * sp + 1]);
  p[1] = pack2(x[8 * sp + 2], x[8 * sp + 3]);
  p[2] = pack2(x[8 * sp + 4], x[8 * sp + 5]);
  p[3] = pack2(x[8 * sp + 6], x[8 * sp + 7]);
  return p;
}
DI float xor32(float v) { return __shfl_xor(v, 32, 64); }
DI void halves(float v, float& lo, float& hi) {
  auto r = __builtin_amdgcn_permlane32_swap(__float_as_uint(v), __float_as_uint(v), false, false);
  lo = __uint_as_float(r[0]);
  hi = __uint_as_float(r[1]);
}
DI float sum32(float v) { float a, b; halves(v, a, b); return a + b; }
DI float max32(float v) { float a, b; halves(v, a, b); return fmaxf(a, b); }
DI float min32(float v) { float a, b; halves(v, a, b); return fminf(a, b); }
DI float opq(float v) { asm("" : "+v"(v)); return v; }
DI f32x16 zero16() {
  f32x16 z;
#pragma unroll
  for (int i = 0; i < 16; ++i) z[i] = 0.f;
  return z;
}
template <int N>
DI void glds(unsigned voff, const void* sbase, unsigned lds) {
  unsigned keep;
  if constexpr (N == 4)
    asm volatile("s_mov_b32 %0, m0\n\ts_mov_b32 m0, %3\n\ts_nop 4\n\t"
                 "global_load_lds_dwordx4 %1, %2\n\tglobal_load_lds_dwordx4 %1, %2 offset:1024\n\t"
                 "global_load_lds_dwordx4 %1, %2 offset:2048\n\tglobal_load_lds_dwordx4 %1, %2 offset:3072\n\t"
                 "s_mov_b32 m0, %0" : "=&s"(keep) : "v"(voff), "s"(sbase), "s"(lds) : "memory");
  else if constexpr (N == 3)
    asm volatile("s_mov_b32 %0, m0\n\ts_mov_b32 m0, %3\n\ts_nop 4\n\t"
                 "global_load_lds_dwordx4 %1, %2\n\tglobal_load_lds_dwordx4 %1, %2 offset:1024\n\t"
                 "global_load_lds_dwordx4 %1, %2 offset:2048\n\t"
                 "s_mov_b32 m0, %0" : "=&s"(keep) : "v"(voff), "s"(sbase), "s"(lds) : "memory");
  else if constexpr (N == 2)
    asm volatile("s_mov_b32 %0, m0\n\ts_mov_b32 m0, %3\n\ts_nop 4\n\t"
                 "global_load_lds_dwordx4 %1, %2\n\tglobal_load_lds_dwordx4 %1, %2 offset:1024\n\t"
                 "s_mov_b32 m0, %0" : "=&s"(keep) : "v"(voff), "s"(sbase), "s"(lds) : "memory");
  else
    asm volatile("s_mov_b32 %0, m0\n\ts_mov_b32 m0, %3\n\ts_nop 4\n\t"
                 "global_load_lds_dwordx4 %1, %2\n\t"
                 "s_mov_b32 m0, %0" : "=&s"(keep) : "v"(voff), "s"(sbase), "s"(lds) : "memory");
}
template <int OFF>
DI void gload(u32x4& d, unsigned voff, const void* sbase) {
  asm volatile("global_load_dwordx4 %0, %1, %2 offset:%3" : "=v"(d) : "v"(voff), "s"(sbase), "i"(OFF));
}
template <int OFF>
DI void gload2(f32x2& d, unsigned voff, const void* sbase) {
  asm volatile("s_nop 4\n\tglobal_load_dwordx2 %0, %1, %2 offset:%3" : "=v"(d) : "v"(voff), "s"(sbase), "i"(OFF));
}
template <int OFF>
DI void gload_c(u32x4& d, unsigned voff, const void* sbase) {
  asm volatile("global_load_dwordx4 %0, %1, %2 offset:%3 sc1" : "=v"(d) : "v"(voff), "s"(sbase), "i"(OFF));
}
template <int OFF>
DI void greload_c(u32x4& d, unsigned voff, const void* sbase) {
  asm volatile("global_load_dwordx4 %0, %1, %2 offset:%3 sc1" : "+v"(d) : "v"(voff), "s"(sbase), "i"(OFF));
}
template <int OFF>
DI void gload2_c(u32x2& d, unsigned voff, const void* sbase) {
  asm volatile("global_load_dwordx2 %0, %1, %2 offset:%3 sc1" : "=v"(d) : "v"(voff), "s"(sbase), "i"(OFF));
}
template <int N> DI void vwait2u(u32x2& d) { asm volatile("s_waitcnt vmcnt(%1)" : "+v"(d) : "i"(N)); }
template <int OFF>
DI void gload1_c(unsigned& d, unsigned voff, const void* sbase) {
  asm volatile("global_load_dword %0, %1, %2 offset:%3 sc1" : "=v"(d) : "v"(voff), "s"(sbase), "i"(OFF));
}
DI void gload1(unsigned& d, unsigned voff, const void* sbase) {
  asm volatile("global_load_dword %0, %1, %2" : "=v"(d) : "v"(voff), "s"(sbase));
}
template <int N> DI void vwait1(unsigned& d) { asm volatile("s_waitcnt vmcnt(%1)" : "+v"(d) : "i"(N)); }
DI u32x4 nosent(u32x4 v) {
  return u32x4{min(v[0], SENT - 1), min(v[1], SENT - 1), min(v[2], SENT - 1), min(v[3], SENT - 1)};
}
template <int OFF = 0>
DI void st_wt(u32x4* p, u32x4 v) {
  asm volatile("global_store_dwordx4 %0, %1, off offset:%2 sc1\n\ts_nop 1" :: "v"(p), "v"(v), "i"(OFF) : "memory");
}
template <int OFF = 0>
DI void st_p(u32x4* p, u32x4 v) {
  st_wt<OFF>(p, v);
}
DI void st_wt2(void* p, u32x2 v) {
  asm volatile("global_store_dwordx2 %0, %1, off sc1\n\ts_nop 1" :: "v"(p), "v"(v) : "memory");
}
DI void st_wt1(float* p, float v) {
  asm volatile("global_store_dword %0, %1, off sc1" :: "v"(p), "v"(v) : "memory");
}
template <int N> DI void vwait(u32x4& d) { asm volatile("s_waitcnt vmcnt(%1)" : "+v"(d) : "i"(N)); }
template <int N> DI void vwait2(f32x2& d) { asm volatile("s_waitcnt vmcnt(%1)" : "+v"(d) : "i"(N)); }
template <int N> DI void vwait_mem() { asm volatile("s_waitcnt vmcnt(%0)" :: "i"(N) : "memory"); }
DI unsigned lds_addr(const void* p) {
  return (unsigned)(uintptr_t)(const __attribute__((address_space(3))) char*)p;
}
DI unsigned bfbits(float v) { return pack2(v, 0.f) & 0xffffu; }
DI void split3(float v, unsigned& p1, unsigned& p2, unsigned& p3) {
  p1 = bfbits(v);
  const float r1 = v - bflo(p1);
  p2 = bfbits(r1);
  p3 = bfbits(r1 - bflo(p2));
}
DI u32x4 aug_w(float cs) {
  unsigned c1, c2, c3;
  split3(cs, c1, c2, c3);
  return u32x4{c1 | (c2 << 16), c3 | (c1 << 16), c2 | (c1 << 16), 0u};
}
DI u32x4 aug_x(float mean, float sd, int hh) {
  unsigned a, b, c;
  split3(hh ? sd : -mean, a, b, c);
  u32x4 lo = {a | (a << 16), a | (b << 16), b | (c << 16), 0u};
  u32x4 hi = {a | (b << 16), c, 0u, 0u};
  return hh ? hi : lo;
}

DI float dot2(unsigned a, unsigned b, float c) {
  return __builtin_amdgcn_fdot2_f32_bf16(__builtin_bit_cast(bf16x2_t, a), __builtin_bit_cast(bf16x2_t, b), c, false);
}
DI void rsum(const u32x4& v, float& s1, float& s2) {
#pragma unroll
  for (int j = 0; j < 4; ++j) {
    s1 = dot2(v[j], 0x3f803f80u, s1);
    s2 = dot2(v[j], v[j], s2);
  }
}
DI float rjoin(float s1, float s2) {
  auto sw = __builtin_amdgcn_permlane32_swap(__float_as_uint(s1), __float_as_uint(s2), false, false);
  return __uint_as_float(sw[0]) + __uint_as_float(sw[1]);
}
DI float rstat(const u32x4& v) {
  float s1 = 0.f, s2 = 0.f;
  rsum(v, s1, s2);
  return __uint_as_float(min(__float_as_uint(rjoin(s1, s2)), SENT - 1));
}

template <int I> struct IC { static constexpr int v = I; };
template <int B, int E, class Fn>
DI void sfor(Fn&& f) {
  if constexpr (B < E) {
    f(IC<B>{});
    sfor<B + 1, E>(f);
  }
}

#define BARRIER() asm volatile("s_waitcnt lgkmcnt(0)\n\ts_barrier" ::: "memory")

typedef __attribute__((address_space(3))) s16x4* lds_s16x4p;
typedef const __attribute__((address_space(3))) u32x4* lds_u32x4p;
DI s16x4 tr_read(const char* p) { return __builtin_amdgcn_ds_read_tr16_b64_v4i16((lds_s16x4p)p); }
DI u32x4 join(s16x4 a, s16x4 b) {
  bf16x8 v = __builtin_shufflevector(a, b, 0, 1, 2, 3, 4, 5, 6, 7);
  return __builtin_bit_cast(u32x4, v);
}

DI u32x4 frag_from_image(const char* img, int row_stride, int s, int l, int cb) {
  const int i16 = l & 15, q4 = i16 >> 2, p4 = i16 & 3, blk = (l >> 4) & 1, hh = l >> 5;
  const char* p = img + (16 * s + 4 * hh + q4) * row_stride + cb + (4 * blk + p4) * 8;
  return join(tr_read(p), tr_read(p + 8 * row_stride));
}
constexpr int P_CS1 = 24, P_QKV = P_CS1 + 28, P_W1 = P_QKV + 56, P_W2 = P_W1 + 56, P_X = P_W2 + 56, P_WO = P_X + 64,
              P_BLOCKS = P_WO + 16;
constexpr int P_RS = 1344 + 16, P_PHI = 16 * P_RS, P_LDS = P_PHI + 8 * 1568 + 256;

__global__ __launch_bounds__(256) void k_prep(const unsigned short* __restrict__ x,
                                              const unsigned short* __restrict__ wqkv,
                                              const unsigned short* __restrict__ wphi,
                                              const unsigned short* __restrict__ wo,
                                              const unsigned short* __restrict__ w1,
                                              const unsigned short* __restrict__ b1,
                                              const unsigned short* __restrict__ w2,
                                              char* __restrict__ ws) {
  __shared__ __attribute__((aligned(16))) char img[P_LDS];
  const int b = blockIdx.x, tid = threadIdx.x, w = tid >> 6, l = tid & 63, r = l & 31, hh = l >> 5;
  STAMP_BEGIN();
  auto fill = [&]() {
    {
      const u32x4 sv = {SENT, SENT, SENT, SENT};
#pragma unroll 1
      for (int fb = b - P_QKV; fb < 256; fb += (fb >= 240 ? 8 : 256)) {
        u32x4* e = (u32x4*)(ws + OFF_EX) + fb * 768 + tid;
        st_p<0>(e, sv); st_p<0>(e + 256, sv); st_p<0>(e + 512, sv);
        if (tid < 112) st_p<0>((u32x4*)(ws + OFF_XS + XS_BYTES) + fb * 112 + tid, sv);
      }
    }
  };

  if (b < P_QKV) {
    const bool q = b < P_CS1;
    const int j = q ? b : b - P_CS1, part = j >> 3, hp = j & 7;
    const int c = q ? min(tid & 7, 6) : tid & 7, ld = q ? 3 * D_ : F_;
    const unsigned short* src = (q ? wqkv + part * D_ + 56 * hp : w1 + 64 * j) + (size_t)(tid >> 3) * ld + 8 * c;
    u32x4 v[14];
#pragma unroll
    for (int i = 0; i < 14; ++i) v[i] = *(const u32x4*)(src + (size_t)32 * i * ld);
    const int h = 2 * hp + (w & 1), cc = r < DH_ ? r : r - DH_;
    unsigned short ph[14];
    unsigned bb = 0;
    if (q) {
#pragma unroll
      for (int e = 0; e < 14; ++e) ph[e] = wphi[(h * DH_ + 14 * hh + e) * DH_ + cc];
    } else {
      bb = b1[64 * j + 32 * (w & 1) + r];
    }
    float cs[8];
#pragma unroll
    for (int k = 0; k < 8; ++k) cs[k] = 0.f;
#pragma unroll
    for (int i = 0; i < 14; ++i)
#pragma unroll
      for (int k = 0; k < 4; ++k) { cs[2 * k] += bflo(v[i][k]); cs[2 * k + 1] += bfhi(v[i][k]); }
    float* red = (float*)img;
    *(f32x4*)(red + (tid >> 3) * 64 + (tid & 7) * 8) = f32x4{cs[0], cs[1], cs[2], cs[3]};
    *(f32x4*)(red + (tid >> 3) * 64 + (tid & 7) * 8 + 4) = f32x4{cs[4], cs[5], cs[6], cs[7]};
    __syncthreads();
    float t = 0.f;
#pragma unroll
    for (int i = 0; i < 8; ++i) t += red[(8 * w + i) * 64 + l];
    red[2048 + w * 64 + l] = t;
    __syncthreads();
    if (w < 2) {
      const float* tot = red + 2048;
      u32x4 hi = {0u, 0u, 0u, 0u};
      float a;
      u32x4* dst;
      if (q) {
        dst = (u32x4*)(ws + OFF_WQKV) + (size_t)(h * WQF + 3 * KSD + (part == 0 ? 2 : part - 1)) * 64;
        if (part < 2) {
          a = 0.f;
#pragma unroll
          for (int e = 0; e < 14; ++e) {
            const int col = DH_ * w + 14 * hh + e;
            a += ((tot[col] + tot[64 + col]) + (tot[128 + col] + tot[192 + col])) * bf1(ph[e]);
          }
          a = sum32(a);
        } else {
          const int col = DH_ * w + cc;
          a = r < DH_ ? (tot[col] + tot[64 + col]) + (tot[128 + col] + tot[192 + col]) : 0.f;
          hi[0] = (r == DH_) ? 0x3f80u : (r == DH_ + 1) ? 0x3f800000u : 0u;
        }
      } else {
        dst = (u32x4*)(ws + OFF_W1) + (size_t)((2 * j + w) * KSA + KSD) * 64;
        const int col = 32 * w + r;
        a = (tot[col] + tot[64 + col]) + (tot[128 + col] + tot[192 + col]);
        hi[0] = bb | (bb << 16);
        hi[1] = bb;
      }
      st_p(dst + l, hh ? hi : aug_w(a));
    }
  } else if (b < P_W1) {
    const int s = (b - P_QKV) >> 1, h0 = 8 * (b & 1);
    u32x4 v[6], p[4];
#pragma unroll
    for (int i = 0; i < 6; ++i) {
      const int idx = min(tid + 256 * i, 1343), rp = idx / 28, c = idx % 28;
      v[i] = *(const u32x4*)(wqkv + (size_t)(16 * s * 3 + rp) * D_ + DH_ * h0 + 8 * c);
    }
#pragma unroll
    for (int i = 0; i < 4; ++i) p[i] = ((const u32x4*)(wphi + h0 * DH_ * DH_))[min(tid + 256 * i, 783)];
    fill();
#pragma unroll
    for (int i = 0; i < 6; ++i) {
      const int idx = min(tid + 256 * i, 1343), row = idx / 84, c = idx % 84;
      *(u32x4*)(img + row * P_RS + c * 16) = v[i];
    }
#pragma unroll
    for (int i = 0; i < 4; ++i) *(u32x4*)(img + P_PHI + min(tid + 256 * i, 783) * 16) = p[i];
    __syncthreads();
    const int i16 = l & 15, q4 = i16 >> 2, cg = (4 * ((l >> 4) & 1) + (i16 & 3)) % 7;
#pragma unroll
    for (int i = 0; i < 2; ++i) {
      const int h = 2 * w + i;
      const char* ar = img + (r & 15) * P_RS + (r < 16 ? 448 : 0) + h * 56 + 8 * hh;
      const char* br = img + P_PHI + h * 1568 + (4 * hh + q4) * 56 + cg * 8;
      const u32x2 a0 = *(const u32x2*)ar, a1 = *(const u32x2*)(ar + 16), a2 = *(const u32x2*)(ar + 32);
      u32x2 a3 = *(const u32x2*)(ar + (hh ? 0 : 48));
      if (hh) a3 = u32x2{0u, 0u};
      const u32x4 b0 = join(tr_read(br), tr_read(br + 8 * 56));
      const u32x4 b1f = join(tr_read(br + 16 * 56), tr_read(br + (hh ? 0 : 24 * 56)));
      u32x4 vf = frag_from_image(img, P_RS, 0, l, 2 * 448 + h * 56);
      if (r >= DH_) vf = u32x4{0u, 0u, 0u, 0u};
      f32x16 acc = MFMA32(asfrag(u32x4{a0[0], a0[1], a1[0], a1[1]}), asfrag(b0), zero16());
      acc = MFMA32(asfrag(u32x4{a2[0], a2[1], a3[0], a3[1]}), asfrag(b1f), acc);
      u32x4* dst = (u32x4*)(ws + OFF_WQKV) + (size_t)((h0 + h) * WQF + s) * 64 + l;
      st_p(dst + KSD * 64, vf);
      st_p(dst, pack8(acc, 0));
      st_p(dst + 2 * KSD * 64, pack8(acc, 1));
    }
  } else if (b < P_X) {
    const bool f1 = b < P_W2;
    const int j = f1 ? b - P_W1 : b - P_W2, cpr = f1 ? 112 : 56, ld = f1 ? F_ : D_;
    const unsigned short* src = f1 ? w1 + (size_t)16 * (j >> 1) * F_ + 896 * (j & 1) : w2 + (size_t)32 * j * D_;
    u32x4 v[7];
#pragma unroll
    for (int i = 0; i < 7; ++i) {
      const int idx = tid + 256 * i;
      v[i] = *(const u32x4*)(src + (size_t)(idx / cpr) * ld + (idx % cpr) * 8);
    }
    fill();
    const int rs = cpr * 16 + 16;
#pragma unroll
    for (int i = 0; i < 7; ++i) {
      const int idx = tid + 256 * i;
      *(u32x4*)(img + (idx / cpr) * rs + (idx % cpr) * 16) = v[i];
    }
    __syncthreads();
#pragma unroll
    for (int i = 0; i < 7; ++i) {
      const int fi = w * 7 + i, ks = f1 ? 0 : w >> 1, ct = f1 ? fi : fi - 14 * ks;
      u32x4* dst = f1 ? (u32x4*)(ws + OFF_W1) + (size_t)((28 * (j & 1) + ct) * KSA + (j >> 1)) * 64
                      : (u32x4*)(ws + OFF_W2) + (size_t)(ct * KSF + 2 * j + ks) * 64;
      st_p(dst + l, frag_from_image(img, rs, ks, l, 64 * ct));
    }
  } else if (b < P_WO) {
    const int tb = b - P_X;
    u32x4 v[7];
#pragma unroll
    for (int i = 0; i < 7; ++i) v[i] = *(const u32x4*)(x + (size_t)tb * 32 * D_ + (tid + 256 * i) * 8);
    fill();
#pragma unroll
    for (int i = 0; i < 7; ++i) {
      const int idx = tid + 256 * i, row = idx / 56, c = idx % 56;
      *(u32x4*)(img + row * 912 + c * 16) = v[i];
    }
    __syncthreads();
    u32x4* dst = (u32x4*)(ws + OFF_XF) + (size_t)tb * KSD * 64;
#pragma unroll
    for (int i = 0; i < 7; ++i) {
      const int s = w * 7 + i;
      const char* p = img + r * 912 + 32 * s + 8 * hh;
      u32x2 lo = *(const u32x2*)p;
      u32x2 hi = *(const u32x2*)(p + 16);
      const u32x4 fr = nosent(u32x4{lo[0], lo[1], hi[0], hi[1]});
      st_p(dst + s * 64 + l, fr);
      st_wt1((float*)(ws + OFF_XS) + (tb * KSD + s) * 64 + l, rstat(fr));
    }
  } else {
    const int h = b - P_WO;
    u32x4 v[7];
#pragma unroll
    for (int i = 0; i < 7; ++i) {
      const int idx = tid + 256 * i;
      v[i] = *(const u32x4*)(wo + (size_t)DH_ * h * D_ + min(idx, 28 * 56 - 1) * 8);
      if (idx >= 28 * 56) v[i] = u32x4{0u, 0u, 0u, 0u};
    }
    fill();
#pragma unroll
    for (int i = 0; i < 7; ++i) *(u32x4*)(img + (tid + 256 * i) * 16) = v[i];
    __syncthreads();
    u32x4* dst = (u32x4*)(ws + OFF_WO) + (size_t)h * 28 * 64;
#pragma unroll
    for (int i = 0; i < 7; ++i) {
      const int fi = w * 7 + i, ks = fi / NNT, nt = fi % NNT;
      st_p(dst + fi * 64 + l, frag_from_image(img, 896, ks, l, 64 * nt));
    }
  }
  STAMP_END(0, 2);
}

DI void finish_stats(f32x2 v, float& mean, float& rstd, float& sd) {
  mean = v[0] * (1.f / D_);
  const float var = fmaxf(v[1] * (1.f / D_) - mean * mean, 0.f) + 1e-5f;
  rstd = rsqrtf(var);
  sd = var * rstd;
}

constexpr float LOG2E = 1.4426950408889634f;
DI float max3(float a, float b, float c) { return fmaxf(fmaxf(a, b), c); }
DI float min3(float a, float b, float c) { return fminf(fminf(a, b), c); }

struct Smx {
  f32x16 e[2];
  float mx, mn, b[2], s[2], inv[2];
  u32x2 pk[2][4];
};
DI void smx_pre(Smx& z, const f32x16& y, float rs2) {
  z.mx = fmaxf(max3(y[0], y[1], y[2]), y[3]);
  z.mn = fminf(min3(y[0], y[1], y[2]), y[3]);
#pragma unroll
  for (int q = 1; q < 4; ++q) {
    z.mx = max3(max3(z.mx, y[4 * q], y[4 * q + 1]), y[4 * q + 2], y[4 * q + 3]);
    z.mn = min3(min3(z.mn, y[4 * q], y[4 * q + 1]), y[4 * q + 2], y[4 * q + 3]);
  }
  z.b[0] = -max32(z.mx) * rs2;
  z.b[1] = min32(z.mn) * rs2;
}
constexpr int SMX_SLOTS = 25;
template <int N, int Q>
DI void smx_slot(Smx& z, const f32x16& y, float rs2, float scale, int hh) {
  f32x16& e = z.e[N];
  if constexpr (Q < 4) {
#pragma unroll
    for (int i = 4 * Q; i < 4 * Q + 4; ++i) e[i] = opq(__builtin_fmaf(y[i], N ? -rs2 : rs2, z.b[N]));
  } else if constexpr (Q < 12) {
    e[2 * Q - 8] = __builtin_amdgcn_exp2f(e[2 * Q - 8]);
    e[2 * Q - 7] = __builtin_amdgcn_exp2f(e[2 * Q - 7]);
  } else if constexpr (Q < 16) {
    constexpr int g = Q - 12;
    float a = opq(opq(e[4 * g] + e[4 * g + 1]) + opq(e[4 * g + 2] + e[4 * g + 3]));
    if constexpr (g == 3) a = hh ? 0.f : a;
    if constexpr (g == 0) z.s[N] = a;
    else z.s[N] = opq(z.s[N] + a);
  } else if constexpr (Q == 16) {
    z.inv[N] = __builtin_amdgcn_rcpf(sum32(z.s[N])) * scale;
  } else {
    constexpr int g = (Q - 17) >> 1;
    if constexpr ((Q - 17) & 1) {
      z.pk[N][g] = u32x2{pack2(e[4 * g], e[4 * g + 1]), pack2(e[4 * g + 2], e[4 * g + 3])};
    } else {
      const float f = (g == 3 && hh) ? 0.f : z.inv[N];
#pragma unroll
      for (int i = 4 * g; i < 4 * g + 4; ++i) e[i] = opq(e[i] * f);
    }
  }
}

#ifndef M_D1
#define M_D1 12
#endif
#ifndef M_D2
#define M_D2 13
#endif
#ifndef M_KVP
#define M_KVP 0
#endif
#ifndef M_RK
#define M_RK 3
#endif
#ifndef M_CSV
#define M_CSV 0
#endif
#ifndef M_SS
#define M_SS 8
#endif
#ifndef M_RS
#define M_RS 4
#endif
#ifndef M_D3
#define M_D3 30
#endif
#ifndef M_D3S
#define M_D3S 7
#endif
#ifndef M_D4
#define M_D4 0
#endif
#ifndef M_D5
#define M_D5 10
#endif
#ifndef M_A0
#define M_A0 8
#endif
#ifndef M_A1
#define M_A1 11
#endif
#ifndef M_A2
#define M_A2 17
#endif
#ifndef M_HW1
#define M_HW1 12
#endif
#ifndef M_NP
#define M_NP 2
#endif
#ifndef M_MID
#define M_MID 4
#endif
#ifndef M_HQ
#define M_HQ 16
#endif
constexpr int M_W = 0, M_R = WQF * 1024, M_ST = M_R + 64 * 1024, M_KV = M_ST + 4096, M_LDS = M_KV + 4096;
constexpr int SPIN_MAX = 1 << 12, RETRY_MAX = 64;
constexpr int NW2G = 18;
#ifndef M_W2T
#define M_W2T {101, 103, 105, 107, 109, 111, 113, 115, 117, 119, 121, 123, 125, 127, 204, 210, 216, 222}
#endif
constexpr int W2T[NW2G] = M_W2T;
constexpr int w2_after() {
  int last = 0;
  for (int i = 0; i < NW2G; ++i) last = W2T[i] > last ? W2T[i] : last;
  return (last < 125) + (last < 127);
}
DI unsigned umax3(unsigned a, unsigned b, unsigned c) { return max(max(a, b), c); }
DI unsigned smax(unsigned m, const u32x4& d) { return umax3(umax3(m, d[0], d[1]), d[2], d[3]); }
DI void csum(const u32x4& f, f32x4& c) {
#pragma unroll
  for (int j = 0; j < 4; ++j) c[j] = dot2(f[j], 0x3f803f80u, c[j]);
}
DI void pin4(u32x4& d) { asm volatile("" : "+v"(d)); }
DI void def4(u32x4& d) { asm volatile("" : "=v"(d)); }
DI bool has_sent(unsigned m) { return __builtin_amdgcn_ballot_w64(m == SENT) != 0; }
DI void probe_wait(const char* base, unsigned off) {
#pragma unroll 1
  for (int k = 0; k < SPIN_MAX; ++k) {
    __builtin_amdgcn_s_sleep(M_RS);
    unsigned pb;
    gload1_c<0>(pb, off, base);
    vwait1<0>(pb);
    if (!has_sent(pb)) break;
  }
}
constexpr int hq(int i) { return M_HQ + i < 27 ? M_HQ + i : 27; }

template <bool spare>
DI void mega(char* smem, char* __restrict__ ws_, const unsigned short* __restrict__ b2, unsigned short* __restrict__ out) {
  MS_BEGIN();
#pragma unroll 1
  for (int layer = 0; layer < 2; ++layer) {
    char* ws = ws_;
    int bid = blockIdx.x, tid = threadIdx.x;
    asm volatile("" : "+s"(ws), "+s"(bid), "+v"(tid));
    const int xcd = bid & 7, bi = bid >> 3;
    const int g = 2 * xcd + (bi >> 4), h = bi & 15;
    const int tp = 2 * g + (h >= 7), ns = spare ? h - 14 : h >= 7 ? h - 7 : h;
    const int w8 = __builtin_amdgcn_readfirstlane(tid >> 6), w = w8 & 3, l = tid & 63;
    const int r = l & 31, hh = l >> 5;
    const unsigned l16 = l * 16;
    const bool lag = w8 < 4;
    const int a8 = w8 >> 2;
    const int so = 4 * ns + w;
    const unsigned sm = lds_addr(smem);
    const char* wb = ws + OFF_WQKV + (size_t)h * (WQF * 1024);
    const int nsw = ns;
    const char* w1g = ws + OFF_W1 + (size_t)(8 * nsw + w8) * (KSA * 1024);
    int sg[7];
    {
      const int rot = h >= 14 ? h - 14 : h >= 7 ? h - 7 : h;
#pragma unroll
      for (int j = 0; j < 7; ++j) sg[j] = (rot + j >= 7 ? rot + j - 7 : rot + j) * 4096;
    }
    auto wdma_kv = [&]() {
#pragma unroll
      for (int j = 0; j < 7; ++j) {
        const int o = ((w >> 1) * KSD + 2 * (w & 1)) * 1024 + sg[j];
        glds<2>(l16, wb + o, sm + M_W + o);
      }
    };
    auto wdma_q = [&]() {
      const char* s = wb + (2 * KSD + 7 * w) * 1024;
      const unsigned d = sm + M_W + (2 * KSD + 7 * w) * 1024;
      glds<4>(l16, s, d);
      glds<3>(l16, s + 4096, d + 4096);
    };
    char* ex = ws + OFF_EX + (size_t)layer * EX_BYTES;
    u32x4 wr[KSA];
    auto w1load = [&](auto I_) {
      constexpr int i = decltype(I_)::v;
      if constexpr (!spare) gload<(i & 7) * 1024 - 4096>(wr[i], l16, w1g + (i >> 3) * 8192 + 4096);
    };
    MS(0);
    if (lag) {
      const int tb = 4 * g + w;
      const char* xb = ws + OFF_XF + (size_t)(2 * layer) * XF_BYTES + (size_t)tb * (KSD * 1024);
      u32x4 xr[KSD];
      f32x16 ak, av, aq;
      unsigned wa[7];
#pragma unroll
      for (int j = 0; j < 7; ++j) wa[j] = sm + M_W + l16 + sg[j];
      auto wf = [&](auto S_c, int part) {
        constexpr int s = decltype(S_c)::v;
        return ((lds_u32x4p)wa[s >> 2])[(part * KSD + (s & 3)) * 64];
      };
      auto xseg = [&](auto J_) {
        constexpr int j = decltype(J_)::v;
        return has_sent(max(smax(smax(0u, xr[4 * j]), xr[4 * j + 1]), smax(smax(0u, xr[4 * j + 2]), xr[4 * j + 3])));
      };
      if (layer) __builtin_amdgcn_s_sleep(M_D1);
      sfor<0, KSD>([&](auto S_c) {
        constexpr int s = decltype(S_c)::v;
        gload_c<(s & 3) * 1024>(xr[s], l16, xb + sg[s >> 2]);
      });
      asm volatile("s_barrier" ::: "memory");
      {
        constexpr int PD = 2;
        u32x4 fk[KSD], fv[KSD];
        sfor<0, PD>([&](auto S_c) { fk[decltype(S_c)::v] = wf(S_c, 0); fv[decltype(S_c)::v] = wf(S_c, 1); });
        bool bad;
        auto check = [&](auto J_) {
          constexpr int j = decltype(J_)::v;
          sfor<4 * j, 4 * j + 4>([&](auto S_c) { vwait<24 - 4 * j>(xr[decltype(S_c)::v]); });
          bad = xseg(J_);
        };
        check(IC<0>{});
        MS(1);
        ak = zero16();
        av = zero16();
        sfor<0, KSD>([&](auto S_c) {
          constexpr int s = decltype(S_c)::v, j = s >> 2;
          if constexpr ((s & 3) == 0) {
            if (__builtin_expect(bad, 0)) {
#pragma unroll 1
              for (int it = 0; it < RETRY_MAX; ++it) {
                sfor<4 * j, KSD>([&](auto T_) { vwait<0>(xr[decltype(T_)::v]); });
                if (!xseg(IC<j>{})) break;
                MDBG(0);
                probe_wait(xb, min(l, KSD - 1) * 1024 + 1020);
                sfor<j, 7>([&](auto V_) {
                  constexpr int v = decltype(V_)::v;
                  if (xseg(V_))
                    sfor<4 * v, 4 * v + 4>([&](auto T_) { greload_c<(decltype(T_)::v & 3) * 1024>(xr[decltype(T_)::v], l16, xb + sg[v]); });
                });
              }
              sfor<4 * j, KSD>([&](auto T_) { vwait<0>(xr[decltype(T_)::v]); });
            }
          }
          if constexpr (s + PD == 16) asm volatile("s_barrier" ::: "memory");
          if constexpr (s + PD < KSD) { fk[s + PD] = wf(IC<s + PD>{}, 0); fv[s + PD] = wf(IC<s + PD>{}, 1); }
          ak = MFMA32(asfrag(fk[s]), asfrag(xr[s]), ak);
          PINV(ak);
          SCHED();
          if constexpr ((s & 3) == 3 && j < 6) { check(IC<j + 1>{}); SCHED(); }
          av = MFMA32(asfrag(xr[s]), asfrag(fv[s]), av);
          PINV(av);
          SCHED();
        });
      }
      MS(2);
      float rstd, rs2;
      u32x4 xa;
      constexpr int PD = 4;
      u32x4 f[KSA];
      unsigned qoff = sm + M_W + 3 * KSD * 1024 + l16;
      asm volatile("" : "+v"(qoff));
      const lds_u32x4p wal = (lds_u32x4p)qoff;
      asm volatile("s_barrier" ::: "memory");
      {
        const u32x4 agk = wal[0], agv = wal[64];
        xa = ((const u32x4*)(smem + M_KV))[w * 64 + l];
        rstd = ((const float*)(smem + M_ST))[w * 64 + l];
        sfor<0, PD>([&](auto S_c) { f[decltype(S_c)::v] = wf(S_c, 2); });
        rs2 = rstd * LOG2E;
        ak = MFMA32(asfrag(agk), asfrag(xa), ak);
        av = MFMA32(asfrag(xa), asfrag(agv), av);
        PINV(ak); PINV(av);
      }
      SCHED();
      {
        Smx z;
        u32x4 vfr[2], pa[4];
        char* img = smem + M_R + 32 * 1024 + w * 4096;
        const int i16 = l & 15, q4 = i16 >> 2, p4 = i16 & 3, blk = (l >> 4) & 1;
        f32x4* red = (f32x4*)(smem + M_R) + w * 128 + l;
        auto slot = [&](auto N_, auto Q_) {
          constexpr int n = decltype(N_)::v, q = decltype(Q_)::v;
          smx_slot<n, q>(z, ak, rs2, rstd, hh);
          if constexpr (q >= 17 && ((q - 17) & 1)) {
            constexpr int gg = (q - 17) >> 1;
            const int ch = (2 * gg + hh) ^ ((r >> 1) & 7);
            *(u32x2*)(img + n * 2048 + r * 64 + ch * 8) = z.pk[n][gg];
          }
        };
        aq = MFMA32(asfrag(f[0]), asfrag(xr[0]), zero16());
        PINV(aq);
        smx_pre(z, ak, rs2);
        SCHED();
        sfor<1, KSA>([&](auto S_c) {
          constexpr int s = decltype(S_c)::v;
          if constexpr (s + PD - 1 < KSD) f[s + PD - 1] = wf(IC<s + PD - 1>{}, 2);
          if constexpr (s - 1 < SMX_SLOTS) { slot(IC<0>{}, IC<s - 1>{}); slot(IC<1>{}, IC<s - 1>{}); }
          if constexpr (s == 26) {
#pragma unroll
            for (int i = 0; i < 4; ++i) {
              const int t0 = 16 * (i & 1) + 4 * hh + q4, t1 = t0 + 8;
              const int c0 = (4 * blk + p4) ^ ((t0 >> 1) & 7), c1 = (4 * blk + p4) ^ ((t1 >> 1) & 7);
              pa[i] = join(tr_read(img + (i >> 1) * 2048 + t0 * 64 + c0 * 8), tr_read(img + (i >> 1) * 2048 + t1 * 64 + c1 * 8));
            }
            vfr[0] = pack8(av, 0);
            vfr[1] = pack8(av, 1);
          }
          if constexpr (s == KSD) {
            ak = MFMA32(asfrag(pa[0]), asfrag(vfr[0]), zero16());
            av = MFMA32(asfrag(pa[2]), asfrag(vfr[0]), zero16());
            ak = MFMA32(asfrag(pa[1]), asfrag(vfr[1]), ak);
            av = MFMA32(asfrag(pa[3]), asfrag(vfr[1]), av);
            PINV(ak); PINV(av);
          } else {
            aq = MFMA32(asfrag(f[s]), asfrag(xr[s]), aq);
          }
          PINV(aq);
          SCHED();
        });
#pragma unroll
        for (int gg = 0; gg < 4; ++gg) {
          red[gg * 512] = f32x4{ak[4 * gg], ak[4 * gg + 1], ak[4 * gg + 2], ak[4 * gg + 3]};
          red[gg * 512 + 64] = f32x4{av[4 * gg], av[4 * gg + 1], av[4 * gg + 2], av[4 * gg + 3]};
        }
        BARRIER();
        SCHED();
        aq = MFMA32(asfrag(wal[128]), asfrag(xa), aq);
      }
      MS(3);
      u32x4 pq[4];
      {
        Smx z;
        smx_pre(z, aq, rs2);
        sfor<0, SMX_SLOTS>([&](auto Q_) {
          constexpr int q = decltype(Q_)::v;
          smx_slot<0, q>(z, aq, rs2, 1.f, hh);
          smx_slot<1, q>(z, aq, rs2, 1.f, hh);
        });
#pragma unroll
        for (int i = 0; i < 4; ++i)
          pq[i] = u32x4{z.pk[i >> 1][2 * (i & 1)][0], z.pk[i >> 1][2 * (i & 1)][1], z.pk[i >> 1][2 * (i & 1) + 1][0], z.pk[i >> 1][2 * (i & 1) + 1][1]};
      }
      if constexpr (!spare) {
        sfor<0, 4>([&](auto I_) { pin4(pq[decltype(I_)::v]); });
#pragma unroll
        for (int q = 0; q < 8; ++q)
          glds<2>(l16, ws + OFF_WO + (size_t)((8 * w + q) * NNT + 2 * nsw) * 1024, sm + M_W + w * 16384 + q * 2048);
      }
      MS(4);
      {
        asm volatile("s_barrier" ::: "memory");
        MS(5);
        const u32x4* kl = (const u32x4*)(smem + M_KV) + l;
        f32x16 o = zero16();
#pragma unroll
        for (int k = 0; k < 4; ++k) o = MFMA32(asfrag(kl[k * 64]), asfrag(pq[k]), o);
        float dlo, dhi;
        halves(o[12] + o[13], dlo, dhi);
        const float inv = __builtin_amdgcn_rcpf(dhi + 1e-6f);
#pragma unroll
        for (int i = 0; i < 16; ++i) o[i] *= inv;
        const u32x4 o0 = nosent(pack8(o, 0)), o1 = nosent(pack8(o, 1));
        u32x4* od = (u32x4*)ex + (size_t)(tb * H_ + h) * 128 + l;
        st_wt<0>(od, o0);
        st_wt<1024>(od, o1);
      }
      if constexpr (!spare) sfor<M_A2, KSA>([&](auto I_) { def4(wr[decltype(I_)::v]); });
    } else {
      if (layer == 0) {
        const u32x4 sv = {SENT, SENT, SENT, SENT};
        st_wt<0>((u32x4*)(ex + EX_BYTES + OF_BYTES) + (size_t)((h * 4 + w) * 16 + g) * 64 + l, sv);
        if (w == 0) glds<3>(l16, wb + 3 * KSD * 1024, sm + M_W + 3 * KSD * 1024);
        wdma_kv();
      }
      if (!spare || layer == 0) wdma_q();
      vwait_mem<13>();
      asm volatile("s_barrier" ::: "memory");
      {
        const char* xs = ws + OFF_XS + (size_t)layer * XS_BYTES + (size_t)(4 * g + w) * 7168 + 4096;
        u32x4 st[7];
        sfor<0, 7>([&](auto I_) { gload_c<decltype(I_)::v * 1024 - 4096>(st[decltype(I_)::v], l16, xs); });
        vwait_mem<14>();
        asm volatile("s_barrier" ::: "memory");
#pragma unroll 1
        for (int it = 0; it < RETRY_MAX; ++it) {
          unsigned m = 0;
          sfor<0, 7>([&](auto I_) {
            vwait<0>(st[decltype(I_)::v]);
            m = smax(m, st[decltype(I_)::v]);
          });
          if (!has_sent(m)) break;
          MDBG(1);
          probe_wait(xs - 4096, min(l, KSD - 1) * 256 + 252);
          sfor<0, 7>([&](auto I_) { greload_c<decltype(I_)::v * 1024 - 4096>(st[decltype(I_)::v], l16, xs); });
        }
        sfor<0, 7>([&](auto I_) { vwait<0>(st[decltype(I_)::v]); });
        f32x4 t = __builtin_bit_cast(f32x4, st[0]);
#pragma unroll
        for (int i = 1; i < 7; ++i) t += __builtin_bit_cast(f32x4, st[i]);
        float* tr = (float*)(smem + M_R + 48 * 1024) + w * 256;
        ((f32x4*)tr)[l] = t;
        float s1, s2, mean, rstd, sd;
        halves((tr[l] + tr[64 + l]) + (tr[128 + l] + tr[192 + l]), s1, s2);
        finish_stats(f32x2{s1, s2}, mean, rstd, sd);
        ((u32x4*)(smem + M_KV))[w * 64 + l] = aug_x(mean, sd, hh);
        ((float*)(smem + M_ST))[w * 64 + l] = rstd;
      }
      BARRIER();
      MS(1);
      if (layer == 0) {
        const u32x4 sv = {SENT, SENT, SENT, SENT};
        const int idx = h * 4 + w;
        u32x4* d = (u32x4*)(ws + OFF_HF) + (size_t)(4 * g * KSF + idx * 7) * 64 + l;
        st_wt<0>(d, sv); st_wt<1024>(d, sv); st_wt<2048>(d, sv); st_wt<3072>(d, sv);
        st_wt<0>(d + 256, sv); st_wt<1024>(d + 256, sv); st_wt<2048>(d + 256, sv);
        d = (u32x4*)(ws + OFF_XF + XF_BYTES) + (size_t)(4 * g * KSD + min(idx, 55) * 2) * 64 + l;
        st_wt<0>(d, sv); st_wt<1024>(d, sv);
        st_wt<0>(d + XF_BYTES / 16, sv); st_wt<1024>(d + XF_BYTES / 16, sv);
        d = (u32x4*)(ex + EX_BYTES) + (size_t)(4 * g * H_ + idx) * 128 + l;
        st_wt<0>(d, sv); st_wt<1024>(d, sv);
      }
      sfor<M_A2, KSA>(w1load);
      __builtin_amdgcn_s_setprio(3);
      const f32x4* redr = (const f32x4*)(smem + M_R) + (w & 1) * 1024 + (w >> 1) * 64 + l;
      char* kp = ex + OF_BYTES + (size_t)(h * 4 + w) * 16384;
      BARRIER();
      {
        const f32x4 s0 = (redr[0] + redr[128]) + (redr[256] + redr[384]);
        const f32x4 s1 = (redr[512] + redr[640]) + (redr[768] + redr[896]);
        st_wt<0>((u32x4*)(kp + g * 1024) + l,
                 nosent(u32x4{pack2(s0[0], s0[1]), pack2(s0[2], s0[3]), pack2(s1[0], s1[1]), pack2(s1[2], s1[3])}));
      }
      MS(3);
      {
        u32x4 pv[16];
        float kvs[8];
        __builtin_amdgcn_s_sleep(M_D2);
        sfor<0, 16>([&](auto I_) {
          constexpr int i = decltype(I_)::v;
          gload_c<(i & 7) * 1024 - 4096>(pv[i], l16, kp + (i >> 3) * 8192 + 4096);
        });
#pragma unroll
        for (int j = 0; j < 8; ++j) kvs[j] = 0.f;
        sfor<0, 16>([&](auto I_) {
          constexpr int i = decltype(I_)::v;
          vwait<15 - i>(pv[i]);
          if (__builtin_expect(has_sent(smax(0u, pv[i])), 0)) {
#pragma unroll 1
            for (int it = 0; it < SPIN_MAX; ++it) {
              sfor<i, 16>([&](auto T_) { vwait<0>(pv[decltype(T_)::v]); });
              if (!has_sent(smax(0u, pv[i]))) break;
              MDBG(0);
#if M_KVP
              probe_wait(kp, (l & 15) * 1024 + 1020);
#else
              __builtin_amdgcn_s_sleep(M_RK);
              if (it >= 8) __builtin_amdgcn_s_sleep(32);
#endif
              sfor<i, 16>([&](auto T_) {
                constexpr int t = decltype(T_)::v;
                if (has_sent(smax(0u, pv[t]))) greload_c<(t & 7) * 1024 - 4096>(pv[t], l16, kp + (t >> 3) * 8192 + 4096);
              });
            }
            sfor<i, 16>([&](auto T_) { vwait<0>(pv[decltype(T_)::v]); });
          }
#pragma unroll
          for (int j = 0; j < 4; ++j) {
            kvs[2 * j] = opq(kvs[2 * j] + bflo(pv[i][j]));
            kvs[2 * j + 1] = opq(kvs[2 * j + 1] + bfhi(pv[i][j]));
          }
          SCHED();
        });
        ((u32x4*)(smem + M_KV))[w * 64 + l] = u32x4{pack2(kvs[0], kvs[1]), pack2(kvs[2], kvs[3]), pack2(kvs[4], kvs[5]), pack2(kvs[6], kvs[7])};
        BARRIER();
      }
      __builtin_amdgcn_s_setprio(0);
      MS(5);
    }
    asm volatile("s_barrier" ::: "memory");
    MS(6);
    if constexpr (spare) {
      if (layer == 0) {
        const u32x4 sv = {SENT, SENT, SENT, SENT};
        const int idx = ns * 8 + w8;
        u32x4* d = (u32x4*)(ws + OFF_HF + HF_BYTES) + (size_t)(4 * g * KSF + idx * 28) * 64 + l;
#pragma unroll 1
        for (int i = 0; i < 7; ++i) {
          st_wt<0>(d, sv); st_wt<1024>(d, sv); st_wt<2048>(d, sv); st_wt<3072>(d, sv);
          d += 256;
          __builtin_amdgcn_s_sleep(16);
        }
        d = (u32x4*)(ws + OFF_XF + 3 * XF_BYTES) + (size_t)(4 * g * KSD + idx * 7) * 64 + l;
        st_wt<0>(d, sv); st_wt<1024>(d, sv); st_wt<2048>(d, sv); st_wt<3072>(d, sv);
        st_wt<0>(d + 256, sv); st_wt<1024>(d + 256, sv); st_wt<2048>(d + 256, sv);
#pragma unroll 1
        for (int k = 0; k < SPIN_MAX; ++k) {
          unsigned pb;
          gload1_c<0>(pb, (l & 15) * 2048, ex + (size_t)(4 * g * H_) * 2048);
          vwait1<0>(pb);
          if (!has_sent(pb)) break;
          __builtin_amdgcn_s_sleep(8);
        }
#pragma unroll 1
        for (int k = 0; k < SPIN_MAX; ++k) {
          __builtin_amdgcn_s_sleep(M_SS);
          unsigned pb;
          gload1_c<0>(pb, min(l, 27) * 1024, ws + OFF_XS + XS_BYTES + (size_t)(4 * g) * 7168);
          vwait1<0>(pb);
          if (__builtin_popcountll(__builtin_amdgcn_ballot_w64(pb == SENT)) < 60) break;
        }
      }
      MS_LAYER();
    } else {
    int pofs[7];
#pragma unroll
    for (int j = 0; j < 7; ++j) pofs[j] = ns + j >= 7 ? ns + j - 7 : ns + j;
    f32x16 c0, c1;
    u32x4 xres;
    auto pwrite = [&](int off) {
      f32x4* pw = (f32x4*)(smem + M_R + off) + w * 512 + l;
#pragma unroll
      for (int gg = 0; gg < 4; ++gg) {
        pw[gg * 64] = f32x4{c0[4 * gg], c0[4 * gg + 1], c0[4 * gg + 2], c0[4 * gg + 3]};
        pw[(4 + gg) * 64] = f32x4{c1[4 * gg], c1[4 * gg + 1], c1[4 * gg + 2], c1[4 * gg + 3]};
      }
    };
    f32x4 sum[2];
    auto padd = [&](int off) {
      const f32x4* pr = (const f32x4*)(smem + M_R + off) + w * 128 + l;
      f32x4 pv[8];
#pragma unroll
      for (int i = 0; i < 8; ++i) pv[i] = pr[((i >> 1) * 8 + (i & 1)) * 64];
      SCHED();
#pragma unroll
      for (int gg = 0; gg < 2; ++gg) sum[gg] += (pv[gg] + pv[2 + gg]) + (pv[4 + gg] + pv[6 + gg]);
      PINV(sum[0]); PINV(sum[1]);
    };
    auto psum = [&]() {
      return nosent(u32x4{pack2(sum[0][0], sum[0][1]), pack2(sum[0][2], sum[0][3]), pack2(sum[1][0], sum[1][1]), pack2(sum[1][2], sum[1][3])});
    };
    {
      const char* og = ex + (size_t)((2 * tp + a8) * H_ + 4 * w) * 2048 + 4096;
      const u32x4* wl = (const u32x4*)(smem + M_W) + w * 1024 + l;
      u32x4 of[8];
      auto oissue = [&]() { sfor<0, 8>([&](auto I_) { gload_c<decltype(I_)::v * 1024 - 4096>(of[decltype(I_)::v], l16, og); }); };
      auto otake = [&](auto N_) {
        constexpr int n = decltype(N_)::v;
        unsigned m = 0;
        u32x4 wf[8][2];
        auto wrd = [&](int i) {
          wf[i][0] = wl[i * 128];
          wf[i][1] = wl[i * 128 + 64];
        };
        wrd(0);
        wrd(1);
        c0 = zero16();
        c1 = zero16();
        sfor<0, 8>([&](auto I_) {
          constexpr int i = decltype(I_)::v;
          if constexpr (i + 2 < 8) wrd(i + 2);
          vwait<n + 7 - i>(of[i]);
          c0 = MFMA32(asfrag(wf[i][0]), asfrag(of[i]), c0);
          c1 = MFMA32(asfrag(wf[i][1]), asfrag(of[i]), c1);
          PINV(c0); PINV(c1);
          m = smax(m, of[i]);
          SCHED();
        });
        return has_sent(m);
      };
      gload_c<0>(xres, l16, ws + OFF_XF + (size_t)(2 * layer) * XF_BYTES + (size_t)((2 * tp + a8) * KSD + so) * 1024);
      sfor<0, M_A0>(w1load);
      __builtin_amdgcn_s_sleep(M_D4);
      oissue();
      sfor<M_A0, M_A1>(w1load);
      vwait_mem<8 + M_A1>();
      asm volatile("s_barrier" ::: "memory");
      if (__builtin_expect(otake(IC<M_A1 - M_A0>{}), 0)) {
        for (int it = 0; it < RETRY_MAX; ++it) {
          MDBG(3);
          probe_wait(og - 4096, (l & 7) * 1024 + 1020);
          oissue();
          if (!otake(IC<0>{})) break;
        }
      }
    }
    MS(7);
    pwrite(a8 * 32768);
    PINV(xres);
#pragma unroll
    for (int gg = 0; gg < 2; ++gg)
      sum[gg] = f32x4{bflo(xres[2 * gg]), bfhi(xres[2 * gg]), bflo(xres[2 * gg + 1]), bfhi(xres[2 * gg + 1])};
    BARRIER();
    padd(a8 * 32768);
    xres = psum();
    char* x1 = ws + OFF_XF + (size_t)(2 * layer + 1) * XF_BYTES + (size_t)((2 * tp + a8) * KSD + w) * 1024;
    st_wt<0>((u32x4*)(x1 + ns * 4096) + l, xres);
    MS(8);
    sfor<M_A1, M_A2>(w1load);
    const unsigned l16b = l16 + KSF * 1024;
    const char* w2g = ws + OFF_W2 + (size_t)(2 * nsw * KSF + 2 * w8) * 1024;
    {
      u32x4* xw = (u32x4*)(smem + M_R) + w * 64 + l;
      float* stw = (float*)(smem + M_ST) + w * 64 + l;
      stw[a8 * 512 + 256] = rstat(xres);
      auto w1aug = [&]() { sfor<0, KSA>([&](auto I_) { pin4(wr[decltype(I_)::v]); }); };
      if (lag) {
        u32x4 xr[12];
        const unsigned l16c = l16 + KSD * 1024;
        auto xissue = [&]() {
          sfor<0, 12>([&](auto I_) {
            constexpr int i = decltype(I_)::v;
            gload_c<0>(xr[i], (i & 1) ? l16c : l16, x1 + pofs[(i >> 1) + 1] * 4096);
          });
        };
        auto xtake = [&]() {
          unsigned m = 0;
          float s1[2] = {0.f, 0.f}, s2[2] = {0.f, 0.f};
          sfor<0, 12>([&](auto I_) {
            constexpr int i = decltype(I_)::v;
            vwait<11 - i>(xr[i]);
            xw[pofs[(i >> 1) + 1] * 256 + (i & 1) * (KSD * 64)] = xr[i];
            m = smax(m, xr[i]);
            rsum(xr[i], s1[i & 1], s2[i & 1]);
            SCHED();
          });
          stw[0] = rjoin(s1[0], s2[0]);
          stw[512] = rjoin(s1[1], s2[1]);
          return has_sent(m);
        };
        __builtin_amdgcn_s_sleep(M_D5);
        xissue();
        asm volatile("s_barrier" ::: "memory");
        xw[ns * 256] = xres;
        if (__builtin_expect(xtake(), 0)) {
          for (int it = 0; it < RETRY_MAX; ++it) {
            MDBG(1);
            probe_wait(x1, min(l >> 1, 6) * 4096 + (l & 1) * (KSD * 1024) + 1020);
            xissue();
            if (!xtake()) break;
          }
        }
        sfor<M_A2, KSA>(w1load);
        MS(9);
        BARRIER();
        MS(10);
        asm volatile("s_barrier\n\ts_waitcnt vmcnt(0)" ::: "memory");
      } else {
        asm volatile("s_barrier" ::: "memory");
        xw[ns * 256 + KSD * 64] = xres;
        sfor<0, 20>([&](auto I_) {
          constexpr int i = NW2G + (decltype(I_)::v >> 1), o = decltype(I_)::v & 1;
          glds<1>((i & 1) ? l16b : l16, w2g + pofs[i >> 2] * 16384 + ((i >> 1) & 1) * 1024 - o * 8192, sm + M_W + (w8 * 10 + i - NW2G - 40 * o) * 1024);
        });
        vwait_mem<20>();
        MS(9);
        BARRIER();
        MS(10);
      }
      w1aug();
    }
    u32x4 bq0 = ((const u32x4*)(b2 + 16 * so))[0], bq1 = ((const u32x4*)(b2 + 16 * so))[1];
    char* hg = ws + OFF_HF + (size_t)layer * HF_BYTES + (size_t)(2 * tp * KSF + 2 * w8) * 1024;
    u32x4 w2r[28];
    auto w2at = [&](auto T_) {
      sfor<0, NW2G>([&](auto I_) {
        constexpr int i = decltype(I_)::v;
        if constexpr (W2T[i] == decltype(T_)::v) {
          gload<((i >> 1) & 1) * 1024>(w2r[i], (i & 1) ? l16b : l16, w2g + pofs[i >> 2] * 16384);
          SCHED();
        }
      });
    };
    unsigned xo0 = sm + M_R + l16;
    asm volatile("" : "+v"(xo0));
    const lds_u32x4p xl = (lds_u32x4p)xo0;
    constexpr int PD = 3;
    u32x4 xb[2][KSA];
#pragma unroll
    for (int s = 0; s < PD; ++s) xb[0][s] = xl[s * 64];
    f32x16 acc1, u;
    u32x4 hown[2][2];
    float k0, k1, sd;
    auto epi = [&](auto A_, auto Q_) {
      constexpr int a = decltype(A_)::v, q = decltype(Q_)::v;
      constexpr int k = q >> 2, gg = q & 3;
      const f32x16& y = acc1;
#pragma unroll
      for (int i = 4 * gg; i < 4 * gg + 4; ++i) {
        if constexpr (k == 0) u[i] = opq(y[i] * y[i]);
        else if constexpr (k == 1) u[i] = opq(__builtin_fmaf(u[i], k1, k0));
        else if constexpr (k == 2) u[i] = opq(u[i] * y[i]);
        else if constexpr (k == 3) u[i] = __builtin_amdgcn_exp2f(u[i]);
        else if constexpr (k == 4) u[i] = opq(__builtin_fmaf(u[i], sd, sd));
        else if constexpr (k == 5) u[i] = __builtin_amdgcn_rcpf(u[i]);
        else if constexpr (k == 6) u[i] = opq(u[i] * y[i]);
      }
      if constexpr (k == 6 && (gg & 1)) {
        hown[a][gg >> 1] = nosent(pack8(u, gg >> 1));
        st_wt<(gg >> 1) * 1024>((u32x4*)(hg + ns * 16384 + a * (KSF * 1024)) + l, hown[a][gg >> 1]);
      }
    };
    u32x4 hr[12];
    auto hload1 = [&](auto A_, auto I_) {
      constexpr int a = decltype(A_)::v, i = decltype(I_)::v;
      gload_c<(i & 1) * 1024>(hr[i], a ? l16b : l16, hg + pofs[1 + (i >> 1)] * 16384);
    };
    auto hload = [&](auto A_, auto J_) {
      hload1(A_, IC<2 * decltype(J_)::v - 2>{});
      hload1(A_, IC<2 * decltype(J_)::v - 1>{});
    };
    sfor<0, 2>([&](auto A_) {
      constexpr int a = decltype(A_)::v;
      acc1 = zero16();
      float sp[8], s1, s2, mean, rstd, v, r1;
      unsigned p1, p2, p3;
      sfor<0, KSA>([&](auto S_c) {
        constexpr int s = decltype(S_c)::v;
        if constexpr (s + PD < KSD) xb[a][s + PD] = xl[(a * KSD + s + PD) * 64];
        if constexpr (s == 2) {
#pragma unroll
          for (int i = 0; i < 8; ++i) sp[i] = ((const float*)(smem + M_ST))[(8 * a + i) * 64 + l];
        }
        if constexpr (s == 5) { sp[0] = opq(opq(sp[0] + sp[1]) + opq(sp[2] + sp[3])); sp[4] = opq(opq(sp[4] + sp[5]) + opq(sp[6] + sp[7])); PINV(sp[0]); PINV(sp[4]); }
        if constexpr (s == 6) { halves(sp[0] + sp[4], s1, s2); PINV(s1); PINV(s2); }
        if constexpr (s == 8) { finish_stats(f32x2{s1, s2}, mean, rstd, sd); PINV(mean); PINV(rstd); PINV(sd); }
        if constexpr (s == 10) {
          const float e0 = -2.f * 0.7978845608028654f * LOG2E;
          k0 = e0 * rstd;
          k1 = (e0 * 0.044715f) * rstd * rstd * rstd;
          v = hh ? sd : -mean;
          PINV(k0); PINV(k1); PINV(v);
        }
        if constexpr (s == 12) { p1 = bfbits(v); r1 = v - bflo(p1); PINV(p1); PINV(r1); }
        if constexpr (s == 14) { p2 = bfbits(r1); p3 = bfbits(r1 - bflo(p2)); PINV(p2); PINV(p3); }
        if constexpr (s == 16) {
          const unsigned ab = p1 | (p2 << 16);
          xb[a][KSD] = u32x4{hh ? ab : (p1 | (p1 << 16)), hh ? p3 : ab, hh ? 0u : (p2 | (p3 << 16)), 0u};
          PINV(xb[a][KSD]);
        }
        w2at(IC<200 * a + s>{});
        acc1 = MFMA32(asfrag(wr[s]), asfrag(xb[a][s]), acc1);
        PINV(acc1);
        SCHED();
      });
      BARRIER();
      SCHED();
      MS(11 + 2 * a);
      if constexpr (a == 1) vwait_mem<NW2G + 2>();
      sfor<0, 28>([&](auto Q_) {
        constexpr int q = decltype(Q_)::v;
        epi(A_, Q_);
        if constexpr (a == 0) w2at(IC<100 + q>{});
        if constexpr (a == 1 && NW2G + q < 28) { w2r[NW2G + q] = ((const u32x4*)(smem + M_W))[(w8 * 10 + q) * 64 + l]; SCHED(); }
        if constexpr (a == 1) sfor<0, 12>([&](auto I_) { if constexpr (hq(decltype(I_)::v) == q) { hload1(IC<0>{}, I_); SCHED(); } });
      });
      SCHED();
      if constexpr (a == 0) {
#pragma unroll
        for (int s = 0; s < PD; ++s) xb[1][s] = xl[(KSD + s) * 64];
      }
      BARRIER();
      SCHED();
      MS(12 + 2 * a);
    });
    vwait<w2_after() + 2 + 12>(w2r[0]);
    sfor<1, NW2G>([&](auto I_) { pin4(w2r[decltype(I_)::v]); });
    auto gemm2 = [&](auto A_) {
      constexpr int a = decltype(A_)::v;
      bool bad;
      auto check = [&](auto J_) {
        constexpr int j = decltype(J_)::v;
        constexpr int n = 12 - 2 * j + (a == 0 ? (hq(2 * j - 1) < 25) + (hq(2 * j - 1) < 27) : 0);
        vwait<n>(hr[2 * j - 2]);
        vwait<n>(hr[2 * j - 1]);
        bad = has_sent(max(smax(0u, hr[2 * j - 2]), smax(0u, hr[2 * j - 1])));
      };
      c0 = MFMA32(asfrag(w2r[0]), asfrag(hown[a][0]), zero16());
      c1 = MFMA32(asfrag(w2r[1]), asfrag(hown[a][0]), zero16());
      PINV(c0); PINV(c1);
      SCHED();
      check(IC<1>{});
      SCHED();
      c0 = MFMA32(asfrag(w2r[2]), asfrag(hown[a][1]), c0);
      c1 = MFMA32(asfrag(w2r[3]), asfrag(hown[a][1]), c1);
      PINV(c0); PINV(c1);
      SCHED();
      sfor<1, 7>([&](auto J_) {
        constexpr int j = decltype(J_)::v;
        u32x4 &h0 = hr[2 * j - 2], &h1 = hr[2 * j - 1];
        if (__builtin_expect(bad, 0)) {
          for (int it = 0; it < SPIN_MAX; ++it) {
            MDBG(2);
            hload(A_, J_);
            vwait<0>(h0);
            vwait<0>(h1);
            if (!has_sent(smax(smax(0u, h0), h1))) break;
          }
        }
        c0 = MFMA32(asfrag(w2r[4 * j]), asfrag(h0), c0);
        c1 = MFMA32(asfrag(w2r[4 * j + 1]), asfrag(h0), c1);
        PINV(c0); PINV(c1);
        SCHED();
        if constexpr (j < 6) check(IC<j + 1>{});
        if constexpr (a == 1 && j == M_MID) { if (lag) asm volatile("s_barrier" ::: "memory"); }
        SCHED();
        c0 = MFMA32(asfrag(w2r[4 * j + 2]), asfrag(h1), c0);
        c1 = MFMA32(asfrag(w2r[4 * j + 3]), asfrag(h1), c1);
        PINV(c0); PINV(c1);
        SCHED();
      });
    };
    gemm2(IC<0>{});
    asm volatile("s_barrier" ::: "memory");
    SCHED();
    MS(15);
    sfor<1, 7>([&](auto J_) { hload(IC<1>{}, J_); });
    pwrite(lag ? 32768 : 0);
    {
      const unsigned b00 = bq0[0], b01 = bq0[1], b02 = bq0[2], b03 = bq0[3], b10 = bq1[0], b11 = bq1[1], b12 = bq1[2], b13 = bq1[3];
      const unsigned m = hh ? ~0u : 0u;
      const u32x2 bs[2] = {u32x2{(b02 & m) | (b00 & ~m), (b03 & m) | (b01 & ~m)}, u32x2{(b12 & m) | (b10 & ~m), (b13 & m) | (b11 & ~m)}};
#pragma unroll
      for (int gg = 0; gg < 2; ++gg) {
        sum[gg][0] = bflo(bs[gg][0]) + bflo(xres[2 * gg]);
        sum[gg][1] = bfhi(bs[gg][0]) + bfhi(xres[2 * gg]);
        sum[gg][2] = bflo(bs[gg][1]) + bflo(xres[2 * gg + 1]);
        sum[gg][3] = bfhi(bs[gg][1]) + bfhi(xres[2 * gg + 1]);
      }
      PINV(sum[0]); PINV(sum[1]);
    }
    if (lag) padd(0);
    BARRIER();
    SCHED();
    MS(16);
    if (lag) padd(32768);
    gemm2(IC<1>{});
    if (!lag) asm volatile("s_barrier" ::: "memory");
    SCHED();
    MS(17);
    pwrite(lag ? 32768 : 0);
    BARRIER();
    SCHED();
    if (!lag) {
      padd(0);
      if (layer == 0) wdma_kv();
      asm volatile("s_barrier" ::: "memory");
      padd(32768);
    }
    SCHED();
    MS(18);
    xres = psum();
    if (layer) {
      unsigned short* o = out + (size_t)((2 * tp + a8) * 32 + r) * D_ + 16 * so + 4 * hh;
      *(u32x2*)(o + 0) = u32x2{xres[0], xres[1]};
      *(u32x2*)(o + 8) = u32x2{xres[2], xres[3]};
    } else {
      st_wt<0>((u32x4*)(ws + OFF_XF + 2 * XF_BYTES) + (size_t)((2 * tp + a8) * KSD + so) * 64 + l, xres);
      st_wt1((float*)(ws + OFF_XS + XS_BYTES) + ((2 * tp + a8) * KSD + so) * 64 + l, rstat(xres));
    }
    MS(19);
    MS_LAYER();
    }
  }
  MS_END();
}

__global__ __launch_bounds__(512, 2) void k_mega(char* __restrict__ ws, const unsigned short* __restrict__ b2,
                                                 unsigned short* __restrict__ out) {
  __shared__ __attribute__((aligned(16))) char smem[M_LDS];
  if (((blockIdx.x >> 3) & 15) >= 14) mega<true>(smem, ws, b2, out);
  else mega<false>(smem, ws, b2, out);
}

}

extern "C" void kernel_launch(void* const* d_in, const int* in_sizes, int n_in, void* d_out, int out_size,
                              void* d_ws, size_t ws_size, hipStream_t stream) {
  const unsigned short* x = (const unsigned short*)d_in[0];
  const unsigned short* wqkv = (const unsigned short*)d_in[1];
  const unsigned short* wphi = (const unsigned short*)d_in[2];
  const unsigned short* wo = (const unsigned short*)d_in[3];
  const unsigned short* w1 = (const unsigned short*)d_in[5];
  const unsigned short* b1 = (const unsigned short*)d_in[6];
  const unsigned short* w2 = (const unsigned short*)d_in[7];
  const unsigned short* b2 = (const unsigned short*)d_in[8];
  char* ws = (char*)d_ws;
  k_prep<<<P_BLOCKS, 256, 0, stream>>>(x, wqkv, wphi, wo, w1, b1, w2, ws);
  k_mega<<<256, 512, 0, stream>>>(ws, b2, (unsigned short*)d_out);
}
```

```cpp
#include <hip/hip_runtime.h>
#include <hip/hip_bf16.h>
#include <stdint.h>

typedef __attribute__((ext_vector_type(8))) short bf16x8;
typedef __attribute__((ext_vector_type(4))) short s16x4;
typedef __attribute__((ext_vector_type(16))) float f32x16;
typedef __attribute__((ext_vector_type(4))) float f32x4;
typedef __attribute__((ext_vector_type(2))) float f32x2;
typedef __attribute__((ext_vector_type(4))) unsigned u32x4;
typedef __attribute__((ext_vector_type(2))) unsigned u32x2;
typedef __attribute__((ext_vector_type(2))) __bf16 bf16x2_t;

#define DI __device__ __forceinline__
#define SCHED() __builtin_amdgcn_sched_barrier(0)
#define PIN(x) asm volatile("" : "+a"(x))
#define PINV(x) asm volatile("" : "+v"(x))
#define MFMA32(a, b, c) __builtin_amdgcn_mfma_f32_32x32x16_bf16((a), (b), (c), 0, 0, 0)

#define STAMP_BEGIN()
#define STAMP_AT(kid, i)
#define STAMP_END(kid, n)
#define MS_BEGIN()
#define MS(i)
#define MDBG(i)
#define MS_LAYER()
#define MS_END()

namespace {

constexpr int S_ = 2048, D_ = 448, H_ = 16, DH_ = 28, F_ = 1792;
constexpr int NTB = S_ / 32;
constexpr int KSD = D_ / 16;
constexpr int KSF = F_ / 16;
constexpr int NNT = D_ / 32;
constexpr int NFT = F_ / 32;
constexpr int KSA = KSD + 1;

constexpr size_t XF_BYTES   = (size_t)NTB * KSD * 1024;
constexpr size_t HF_BYTES   = (size_t)NTB * KSF * 1024;
constexpr size_t OF_BYTES   = (size_t)NTB * H_ * 2048;
constexpr size_t KVP_BYTES  = (size_t)H_ * 4 * 16 * 1024;
constexpr size_t EX_BYTES   = OF_BYTES + KVP_BYTES;
constexpr int    WQF        = 3 * KSA;
constexpr size_t OFF_XF     = 0;
constexpr size_t OFF_WQKV   = OFF_XF + 4 * XF_BYTES;
constexpr size_t OFF_WO     = OFF_WQKV + (size_t)H_ * WQF * 1024;
constexpr size_t OFF_W1     = OFF_WO + (size_t)H_ * 2 * NNT * 1024;
constexpr size_t OFF_W2     = OFF_W1 + (size_t)NFT * KSA * 1024;
constexpr size_t OFF_HF     = OFF_W2 + (size_t)NNT * KSF * 1024;
constexpr size_t OFF_EX     = OFF_HF + 2 * HF_BYTES;
constexpr size_t XS_BYTES   = (size_t)NTB * 7 * 1024;
constexpr size_t OFF_XS     = OFF_EX + 2 * EX_BYTES;
constexpr size_t OFF_FLG    = OFF_XS + 2 * XS_BYTES;
constexpr size_t WS_TOTAL   = OFF_FLG + 4096;
constexpr unsigned SENT = 0xffffffffu;

DI unsigned pack2(float a, float b) {
  f32x2 v = {a, b};
  bf16x2_t r = __builtin_convertvector(v, bf16x2_t);
  return __builtin_bit_cast(unsigned, r);
}
DI float bflo(unsigned u) { return __uint_as_float(u << 16); }
DI float bfhi(unsigned u) { return __uint_as_float(u & 0xffff0000u); }
DI float bf1(unsigned short u) { return __uint_as_float(((unsigned)u) << 16); }
DI bf16x8 asfrag(u32x4 v) { return __builtin_bit_cast(bf16x8, v); }
DI u32x4 pack8(const f32x16& x, int sp) {
  u32x4 p;
  p[0] = pack2(x[8 * sp + 0], x[8 * sp + 1]);
  p[1] = pack2(x[8 * sp + 2], x[8 * sp + 3]);
  p[2] = pack2(x[8 * sp + 4], x[8 * sp + 5]);
  p[3] = pack2(x[8 * sp + 6], x[8 * sp + 7]);
  return p;
}
DI float xor32(float v) { return __shfl_xor(v, 32, 64); }
DI void halves(float v, float& lo, float& hi) {
  auto r = __builtin_amdgcn_permlane32_swap(__float_as_uint(v), __float_as_uint(v), false, false);
  lo = __uint_as_float(r[0]);
  hi = __uint_as_float(r[1]);
}
DI float sum32(float v) { float a, b; halves(v, a, b); return a + b; }
DI float max32(float v) { float a, b; halves(v, a, b); return fmaxf(a, b); }
DI float min32(float v) { float a, b; halves(v, a, b); return fminf(a, b); }
DI float opq(float v) { asm("" : "+v"(v)); return v; }
DI f32x16 zero16() {
  f32x16 z;
#pragma unroll
  for (int i = 0; i < 16; ++i) z[i] = 0.f;
  return z;
}
template <int N>
DI void glds(unsigned voff, const void* sbase, unsigned lds) {
  unsigned keep;
  if constexpr (N == 4)
    asm volatile("s_mov_b32 %0, m0\n\ts_mov_b32 m0, %3\n\ts_nop 4\n\t"
                 "global_load_lds_dwordx4 %1, %2\n\tglobal_load_lds_dwordx4 %1, %2 offset:1024\n\t"
                 "global_load_lds_dwordx4 %1, %2 offset:2048\n\tglobal_load_lds_dwordx4 %1, %2 offset:3072\n\t"
                 "s_mov_b32 m0, %0" : "=&s"(keep) : "v"(voff), "s"(sbase), "s"(lds) : "memory");
  else if constexpr (N == 3)
    asm volatile("s_mov_b32 %0, m0\n\ts_mov_b32 m0, %3\n\ts_nop 4\n\t"
                 "global_load_lds_dwordx4 %1, %2\n\tglobal_load_lds_dwordx4 %1, %2 offset:1024\n\t"
                 "global_load_lds_dwordx4 %1, %2 offset:2048\n\t"
                 "s_mov_b32 m0, %0" : "=&s"(keep) : "v"(voff), "s"(sbase), "s"(lds) : "memory");
  else if constexpr (N == 2)
    asm volatile("s_mov_b32 %0, m0\n\ts_mov_b32 m0, %3\n\ts_nop 4\n\t"
                 "global_load_lds_dwordx4 %1, %2\n\tglobal_load_lds_dwordx4 %1, %2 offset:1024\n\t"
                 "s_mov_b32 m0, %0" : "=&s"(keep) : "v"(voff), "s"(sbase), "s"(lds) : "memory");
  else
    asm volatile("s_mov_b32 %0, m0\n\ts_mov_b32 m0, %3\n\ts_nop 4\n\t"
                 "global_load_lds_dwordx4 %1, %2\n\t"
                 "s_mov_b32 m0, %0" : "=&s"(keep) : "v"(voff), "s"(sbase), "s"(lds) : "memory");
}
template <int OFF>
DI void gload(u32x4& d, unsigned voff, const void* sbase) {
  asm volatile("global_load_dwordx4 %0, %1, %2 offset:%3" : "=v"(d) : "v"(voff), "s"(sbase), "i"(OFF));
}
template <int OFF>
DI void gload2(f32x2& d, unsigned voff, const void* sbase) {
  asm volatile("s_nop 4\n\tglobal_load_dwordx2 %0, %1, %2 offset:%3" : "=v"(d) : "v"(voff), "s"(sbase), "i"(OFF));
}
template <int OFF>
DI void gload_c(u32x4& d, unsigned voff, const void* sbase) {
  asm volatile("global_load_dwordx4 %0, %1, %2 offset:%3 sc1" : "=v"(d) : "v"(voff), "s"(sbase), "i"(OFF));
}
template <int OFF>
DI void greload_c(u32x4& d, unsigned voff, const void* sbase) {
  asm volatile("global_load_dwordx4 %0, %1, %2 offset:%3 sc1" : "+v"(d) : "v"(voff), "s"(sbase), "i"(OFF));
}
template <int OFF>
DI void gload2_c(u32x2& d, unsigned voff, const void* sbase) {
  asm volatile("global_load_dwordx2 %0, %1, %2 offset:%3 sc1" : "=v"(d) : "v"(voff), "s"(sbase), "i"(OFF));
}
template <int N> DI void vwait2u(u32x2& d) { asm volatile("s_waitcnt vmcnt(%1)" : "+v"(d) : "i"(N)); }
template <int OFF>
DI void gload1_c(unsigned& d, unsigned voff, const void* sbase) {
  asm volatile("global_load_dword %0, %1, %2 offset:%3 sc1" : "=v"(d) : "v"(voff), "s"(sbase), "i"(OFF));
}
DI void gload1(unsigned& d, unsigned voff, const void* sbase) {
  asm volatile("global_load_dword %0, %1, %2" : "=v"(d) : "v"(voff), "s"(sbase));
}
template <int N> DI void vwait1(unsigned& d) { asm volatile("s_waitcnt vmcnt(%1)" : "+v"(d) : "i"(N)); }
DI u32x4 nosent(u32x4 v) {
  return u32x4{min(v[0], SENT - 1), min(v[1], SENT - 1), min(v[2], SENT - 1), min(v[3], SENT - 1)};
}
template <int OFF = 0>
DI void st_wt(u32x4* p, u32x4 v) {
  asm volatile("global_store_dwordx4 %0, %1, off offset:%2 sc1\n\ts_nop 1" :: "v"(p), "v"(v), "i"(OFF) : "memory");
}
template <int OFF = 0>
DI void st_p(u32x4* p, u32x4 v) {
  st_wt<OFF>(p, v);
}
DI void st_wt2(void* p, u32x2 v) {
  asm volatile("global_store_dwordx2 %0, %1, off sc1\n\ts_nop 1" :: "v"(p), "v"(v) : "memory");
}
DI void st_wt1(float* p, float v) {
  asm volatile("global_store_dword %0, %1, off sc1" :: "v"(p), "v"(v) : "memory");
}
template <int N> DI void vwait(u32x4& d) { asm volatile("s_waitcnt vmcnt(%1)" : "+v"(d) : "i"(N)); }
template <int N> DI void vwait2(f32x2& d) { asm volatile("s_waitcnt vmcnt(%1)" : "+v"(d) : "i"(N)); }
template <int N> DI void vwait_mem() { asm volatile("s_waitcnt vmcnt(%0)" :: "i"(N) : "memory"); }
DI unsigned lds_addr(const void* p) {
  return (unsigned)(uintptr_t)(const __attribute__((address_space(3))) char*)p;
}
DI unsigned bfbits(float v) { return pack2(v, 0.f) & 0xffffu; }
DI void split3(float v, unsigned& p1, unsigned& p2, unsigned& p3) {
  p1 = bfbits(v);
  const float r1 = v - bflo(p1);
  p2 = bfbits(r1);
  p3 = bfbits(r1 - bflo(p2));
}
DI u32x4 aug_w(float cs) {
  unsigned c1, c2, c3;
  split3(cs, c1, c2, c3);
  return u32x4{c1 | (c2 << 16), c3 | (c1 << 16), c2 | (c1 << 16), 0u};
}
DI u32x4 aug_x(float mean, float sd, int hh) {
  unsigned a, b, c;
  split3(hh ? sd : -mean, a, b, c);
  u32x4 lo = {a | (a << 16), a | (b << 16), b | (c << 16), 0u};
  u32x4 hi = {a | (b << 16), c, 0u, 0u};
  return hh ? hi : lo;
}

DI float dot2(unsigned a, unsigned b, float c) {
  return __builtin_amdgcn_fdot2_f32_bf16(__builtin_bit_cast(bf16x2_t, a), __builtin_bit_cast(bf16x2_t, b), c, false);
}
DI void rsum(const u32x4& v, float& s1, float& s2) {
#pragma unroll
  for (int j = 0; j < 4; ++j) {
    s1 = dot2(v[j], 0x3f803f80u, s1);
    s2 = dot2(v[j], v[j], s2);
  }
}
DI float rjoin(float s1, float s2) {
  auto sw = __builtin_amdgcn_permlane32_swap(__float_as_uint(s1), __float_as_uint(s2), false, false);
  return __uint_as_float(sw[0]) + __uint_as_float(sw[1]);
}
DI float rstat(const u32x4& v) {
  float s1 = 0.f, s2 = 0.f;
  rsum(v, s1, s2);
  return __uint_as_float(min(__float_as_uint(rjoin(s1, s2)), SENT - 1));
}

template <int I> struct IC { static constexpr int v = I; };
template <int B, int E, class Fn>
DI void sfor(Fn&& f) {
  if constexpr (B < E) {
    f(IC<B>{});
    sfor<B + 1, E>(f);
  }
}

#define BARRIER() asm volatile("s_waitcnt lgkmcnt(0)\n\ts_barrier" ::: "memory")

typedef __attribute__((address_space(3))) s16x4* lds_s16x4p;
typedef const __attribute__((address_space(3))) u32x4* lds_u32x4p;
DI s16x4 tr_read(const char* p) { return __builtin_amdgcn_ds_read_tr16_b64_v4i16((lds_s16x4p)p); }
DI u32x4 join(s16x4 a, s16x4 b) {
  bf16x8 v = __builtin_shufflevector(a, b, 0, 1, 2, 3, 4, 5, 6, 7);
  return __builtin_bit_cast(u32x4, v);
}

DI u32x4 frag_from_image(const char* img, int row_stride, int s, int l, int cb) {
  const int i16 = l & 15, q4 = i16 >> 2, p4 = i16 & 3, blk = (l >> 4) & 1, hh = l >> 5;
  const char* p = img + (16 * s + 4 * hh + q4) * row_stride + cb + (4 * blk + p4) * 8;
  return join(tr_read(p), tr_read(p + 8 * row_stride));
}
constexpr int P_CS1 = 24, P_QKV = P_CS1 + 28, P_W1 = P_QKV + 56, P_W2 = P_W1 + 56, P_X = P_W2 + 56, P_WO = P_X + 64,
              P_BLOCKS = P_WO + 16;
constexpr int P_RS = 1344 + 16, P_PHI = 16 * P_RS, P_LDS = P_PHI + 8 * 1568 + 256;

__global__ __launch_bounds__(256) void k_prep(const unsigned short* __restrict__ x,
                                              const unsigned short* __restrict__ wqkv,
                                              const unsigned short* __restrict__ wphi,
                                              const unsigned short* __restrict__ wo,
                                              const unsigned short* __restrict__ w1,
                                              const unsigned short* __restrict__ b1,
                                              const unsigned short* __restrict__ w2,
                                              char* __restrict__ ws) {
  __shared__ __attribute__((aligned(16))) char img[P_LDS];
  const int b = blockIdx.x, tid = threadIdx.x, w = tid >> 6, l = tid & 63, r = l & 31, hh = l >> 5;
  STAMP_BEGIN();
  auto fill = [&]() {
    {
      const u32x4 sv = {SENT, SENT, SENT, SENT};
#pragma unroll 1
      for (int fb = b - P_QKV; fb < 256; fb += (fb >= 240 ? 8 : 256)) {
        u32x4* e = (u32x4*)(ws + OFF_EX) + fb * 768 + tid;
        st_p<0>(e, sv); st_p<0>(e + 256, sv); st_p<0>(e + 512, sv);
        if (tid < 113) st_p<0>((u32x4*)(ws + OFF_XS + XS_BYTES) + fb * 113 + tid, sv);
      }
    }
  };

  if (b < P_QKV) {
    const bool q = b < P_CS1;
    const int j = q ? b : b - P_CS1, part = j >> 3, hp = j & 7;
    const int c = q ? min(tid & 7, 6) : tid & 7, ld = q ? 3 * D_ : F_;
    const unsigned short* src = (q ? wqkv + part * D_ + 56 * hp : w1 + 64 * j) + (size_t)(tid >> 3) * ld + 8 * c;
    u32x4 v[14];
#pragma unroll
    for (int i = 0; i < 14; ++i) v[i] = *(const u32x4*)(src + (size_t)32 * i * ld);
    const int h = 2 * hp + (w & 1), cc = r < DH_ ? r : r - DH_;
    unsigned short ph[14];
    unsigned bb = 0;
    if (q) {
#pragma unroll
      for (int e = 0; e < 14; ++e) ph[e] = wphi[(h * DH_ + 14 * hh + e) * DH_ + cc];
    } else {
      bb = b1[64 * j + 32 * (w & 1) + r];
    }
    float cs[8];
#pragma unroll
    for (int k = 0; k < 8; ++k) cs[k] = 0.f;
#pragma unroll
    for (int i = 0; i < 14; ++i)
#pragma unroll
      for (int k = 0; k < 4; ++k) { cs[2 * k] += bflo(v[i][k]); cs[2 * k + 1] += bfhi(v[i][k]); }
    float* red = (float*)img;
    *(f32x4*)(red + (tid >> 3) * 64 + (tid & 7) * 8) = f32x4{cs[0], cs[1], cs[2], cs[3]};
    *(f32x4*)(red + (tid >> 3) * 64 + (tid & 7) * 8 + 4) = f32x4{cs[4], cs[5], cs[6], cs[7]};
    __syncthreads();
    float t = 0.f;
#pragma unroll
    for (int i = 0; i < 8; ++i) t += red[(8 * w + i) * 64 + l];
    red[2048 + w * 64 + l] = t;
    __syncthreads();
    if (w < 2) {
      const float* tot = red + 2048;
      u32x4 hi = {0u, 0u, 0u, 0u};
      float a;
      u32x4* dst;
      if (q) {
        dst = (u32x4*)(ws + OFF_WQKV) + (size_t)(h * WQF + 3 * KSD + (part == 0 ? 2 : part - 1)) * 64;
        if (part < 2) {
          a = 0.f;
#pragma unroll
          for (int e = 0; e < 14; ++e) {
            const int col = DH_ * w + 14 * hh + e;
            a += ((tot[col] + tot[64 + col]) + (tot[128 + col] + tot[192 + col])) * bf1(ph[e]);
          }
          a = sum32(a);
        } else {
          const int col = DH_ * w + cc;
          a = r < DH_ ? (tot[col] + tot[64 + col]) + (tot[128 + col] + tot[192 + col]) : 0.f;
          hi[0] = (r == DH_) ? 0x3f80u : (r == DH_ + 1) ? 0x3f800000u : 0u;
        }
      } else {
        dst = (u32x4*)(ws + OFF_W1) + (size_t)((2 * j + w) * KSA + KSD) * 64;
        const int col = 32 * w + r;
        a = (tot[col] + tot[64 + col]) + (tot[128 + col] + tot[192 + col]);
        hi[0] = bb | (bb << 16);
        hi[1] = bb;
      }
      st_p(dst + l, hh ? hi : aug_w(a));
    }
  } else if (b < P_W1) {
    const int s = (b - P_QKV) >> 1, h0 = 8 * (b & 1);
    u32x4 v[6], p[4];
#pragma unroll
    for (int i = 0; i < 6; ++i) {
      const int idx = min(tid + 256 * i, 1343), rp = idx / 28, c = idx % 28;
      v[i] = *(const u32x4*)(wqkv + (size_t)(16 * s * 3 + rp) * D_ + DH_ * h0 + 8 * c);
    }
#pragma unroll
    for (int i = 0; i < 4; ++i) p[i] = ((const u32x4*)(wphi + h0 * DH_ * DH_))[min(tid + 256 * i, 783)];
    fill();
#pragma unroll
    for (int i = 0; i < 6; ++i) {
      const int idx = min(tid + 256 * i, 1343), row = idx / 84, c = idx % 84;
      *(u32x4*)(img + row * P_RS + c * 16) = v[i];
    }
#pragma unroll
    for (int i = 0; i < 4; ++i) *(u32x4*)(img + P_PHI + min(tid + 256 * i, 783) * 16) = p[i];
    __syncthreads();
    const int i16 = l & 15, q4 = i16 >> 2, cg = (4 * ((l >> 4) & 1) + (i16 & 3)) % 7;
#pragma unroll
    for (int i = 0; i < 2; ++i) {
      const int h = 2 * w + i;
      const char* ar = img + (r & 15) * P_RS + (r < 16 ? 448 : 0) + h * 56 + 8 * hh;
      const char* br = img + P_PHI + h * 1568 + (4 * hh + q4) * 56 + cg * 8;
      const u32x2 a0 = *(const u32x2*)ar, a1 = *(const u32x2*)(ar + 16), a2 = *(const u32x2*)(ar + 32);
      u32x2 a3 = *(const u32x2*)(ar + (hh ? 0 : 48));
      if (hh) a3 = u32x2{0u, 0u};
      const u32x4 b0 = join(tr_read(br), tr_read(br + 8 * 56));
      const u32x4 b1f = join(tr_read(br + 16 * 56), tr_read(br + (hh ? 0 : 24 * 56)));
      u32x4 vf = frag_from_image(img, P_RS, 0, l, 2 * 448 + h * 56);
      if (r >= DH_) vf = u32x4{0u, 0u, 0u, 0u};
      f32x16 acc = MFMA32(asfrag(u32x4{a0[0], a0[1], a1[0], a1[1]}), asfrag(b0), zero16());
      acc = MFMA32(asfrag(u32x4{a2[0], a2[1], a3[0], a3[1]}), asfrag(b1f), acc);
      u32x4* dst = (u32x4*)(ws + OFF_WQKV) + (size_t)((h0 + h) * WQF + s) * 64 + l;
      st_p(dst + KSD * 64, vf);
      st_p(dst, pack8(acc, 0));
      st_p(dst + 2 * KSD * 64, pack8(acc, 1));
    }
  } else if (b < P_X) {
    const bool f1 = b < P_W2;
    const int j = f1 ? b - P_W1 : b - P_W2, cpr = f1 ? 112 : 56, ld = f1 ? F_ : D_;
    const unsigned short* src = f1 ? w1 + (size_t)16 * (j >> 1) * F_ + 896 * (j & 1) : w2 + (size_t)32 * j * D_;
    u32x4 v[7];
#pragma unroll
    for (int i = 0; i < 7; ++i) {
      const int idx = tid + 256 * i;
      v[i] = *(const u32x4*)(src + (size_t)(idx / cpr) * ld + (idx % cpr) * 8);
    }
    fill();
    const int rs = cpr * 16 + 16;
#pragma unroll
    for (int i = 0; i < 7; ++i) {
      const int idx = tid + 256 * i;
      *(u32x4*)(img + (idx / cpr) * rs + (idx % cpr) * 16) = v[i];
    }
    __syncthreads();
#pragma unroll
    for (int i = 0; i < 7; ++i) {
      const int fi = w * 7 + i, ks = f1 ? 0 : w >> 1, ct = f1 ? fi : fi - 14 * ks;
      u32x4* dst = f1 ? (u32x4*)(ws + OFF_W1) + (size_t)((28 * (j & 1) + ct) * KSA + (j >> 1)) * 64
                      : (u32x4*)(ws + OFF_W2) + (size_t)(ct * KSF + 2 * j + ks) * 64;
      st_p(dst + l, frag_from_image(img, rs, ks, l, 64 * ct));
    }
  } else if (b < P_WO) {
    const int tb = b - P_X;
    u32x4 v[7];
#pragma unroll
    for (int i = 0; i < 7; ++i) v[i] = *(const u32x4*)(x + (size_t)tb * 32 * D_ + (tid + 256 * i) * 8);
    fill();
#pragma unroll
    for (int i = 0; i < 7; ++i) {
      const int idx = tid + 256 * i, row = idx / 56, c = idx % 56;
      *(u32x4*)(img + row * 912 + c * 16) = v[i];
    }
    __syncthreads();
    u32x4* dst = (u32x4*)(ws + OFF_XF) + (size_t)tb * KSD * 64;
#pragma unroll
    for (int i = 0; i < 7; ++i) {
      const int s = w * 7 + i;
      const char* p = img + r * 912 + 32 * s + 8 * hh;
      u32x2 lo = *(const u32x2*)p;
      u32x2 hi = *(const u32x2*)(p + 16);
      const u32x4 fr = nosent(u32x4{lo[0], lo[1], hi[0], hi[1]});
      st_p(dst + s * 64 + l, fr);
      st_wt1((float*)(ws + OFF_XS) + (tb * KSD + s) * 64 + l, rstat(fr));
    }
  } else {
    const int h = b - P_WO;
    u32x4 v[7];
#pragma unroll
    for (int i = 0; i < 7; ++i) {
      const int idx = tid + 256 * i;
      v[i] = *(const u32x4*)(wo + (size_t)DH_ * h * D_ + min(idx, 28 * 56 - 1) * 8);
      if (idx >= 28 * 56) v[i] = u32x4{0u, 0u, 0u, 0u};
    }
    fill();
#pragma unroll
    for (int i = 0; i < 7; ++i) *(u32x4*)(img + (tid + 256 * i) * 16) = v[i];
    __syncthreads();
    u32x4* dst = (u32x4*)(ws + OFF_WO) + (size_t)h * 28 * 64;
#pragma unroll
    for (int i = 0; i < 7; ++i) {
      const int fi = w * 7 + i, ks = fi / NNT, nt = fi % NNT;
      st_p(dst + fi * 64 + l, frag_from_image(img, 896, ks, l, 64 * nt));
    }
  }
  STAMP_END(0, 2);
}

DI void finish_stats(f32x2 v, float& mean, float& rstd, float& sd) {
  mean = v[0] * (1.f / D_);
  const float var = fmaxf(v[1] * (1.f / D_) - mean * mean, 0.f) + 1e-5f;
  rstd = rsqrtf(var);
  sd = var * rstd;
}

constexpr float LOG2E = 1.4426950408889634f;
DI float max3(float a, float b, float c) { return fmaxf(fmaxf(a, b), c); }
DI float min3(float a, float b, float c) { return fminf(fminf(a, b), c); }

struct Smx {
  f32x16 e[2];
  float mx, mn, b[2], s[2], inv[2];
  u32x2 pk[2][4];
};
DI void smx_pre(Smx& z, const f32x16& y, float rs2) {
  z.mx = fmaxf(max3(y[0], y[1], y[2]), y[3]);
  z.mn = fminf(min3(y[0], y[1], y[2]), y[3]);
#pragma unroll
  for (int q = 1; q < 4; ++q) {
    z.mx = max3(max3(z.mx, y[4 * q], y[4 * q + 1]), y[4 * q + 2], y[4 * q + 3]);
    z.mn = min3(min3(z.mn, y[4 * q], y[4 * q + 1]), y[4 * q + 2], y[4 * q + 3]);
  }
  z.b[0] = -max32(z.mx) * rs2;
  z.b[1] = min32(z.mn) * rs2;
}
constexpr int SMX_SLOTS = 25;
template <int N, int Q>
DI void smx_slot(Smx& z, const f32x16& y, float rs2, float scale, int hh) {
  f32x16& e = z.e[N];
  if constexpr (Q < 4) {
#pragma unroll
    for (int i = 4 * Q; i < 4 * Q + 4; ++i) e[i] = opq(__builtin_fmaf(y[i], N ? -rs2 : rs2, z.b[N]));
  } else if constexpr (Q < 12) {
    e[2 * Q - 8] = __builtin_amdgcn_exp2f(e[2 * Q - 8]);
    e[2 * Q - 7] = __builtin_amdgcn_exp2f(e[2 * Q - 7]);
  } else if constexpr (Q < 16) {
    constexpr int g = Q - 12;
    float a = opq(opq(e[4 * g] + e[4 * g + 1]) + opq(e[4 * g + 2] + e[4 * g + 3]));
    if constexpr (g == 3) a = hh ? 0.f : a;
    if constexpr (g == 0) z.s[N] = a;
    else z.s[N] = opq(z.s[N] + a);
  } else if constexpr (Q == 16) {
    z.inv[N] = __builtin_amdgcn_rcpf(sum32(z.s[N])) * scale;
  } else {
    constexpr int g = (Q - 17) >> 1;
    if constexpr ((Q - 17) & 1) {
      z.pk[N][g] = u32x2{pack2(e[4 * g], e[4 * g + 1]), pack2(e[4 * g + 2], e[4 * g + 3])};
    } else {
      const float f = (g == 3 && hh) ? 0.f : z.inv[N];
#pragma unroll
      for (int i = 4 * g; i < 4 * g + 4; ++i) e[i] = opq(e[i] * f);
    }
  }
}

#ifndef M_D1
#define M_D1 15
#endif
#ifndef M_D2
#define M_D2 13
#endif
#ifndef M_KVP
#define M_KVP 0
#endif
#ifndef M_RK
#define M_RK 3
#endif
#ifndef M_CSV
#define M_CSV 0
#endif
#ifndef M_SS
#define M_SS 8
#endif
#ifndef M_RS
#define M_RS 4
#endif
#ifndef M_D3
#define M_D3 30
#endif
#ifndef M_D3S
#define M_D3S 7
#endif
#ifndef M_D4
#define M_D4 2
#endif
#ifndef M_D5
#define M_D5 17
#endif
#ifndef M_A0
#define M_A0 8
#endif
#ifndef M_A1
#define M_A1 11
#endif
#ifndef M_A2
#define M_A2 17
#endif
#ifndef M_HW1
#define M_HW1 12
#endif
#ifndef M_NP
#define M_NP 2
#endif
#ifndef M_MID
#define M_MID 4
#endif
#ifndef M_HQ
#define M_HQ 16
#endif
#ifndef M_FE
#define M_FE 0
#endif
#ifndef M_HS
#define M_HS 2
#endif
#ifndef M_HSL
#define M_HSL 0
#endif
constexpr int NFILL = 6 + M_HS;
#ifndef M_NVS
#define M_NVS 4
#endif
#ifndef M_QF
#define M_QF 2
#endif
#ifndef M_D6
#define M_D6 4
#endif
#ifndef M_D4H
#define M_D4H 4
#endif
constexpr int M_W = 0, M_R = WQF * 1024, M_ST = M_R + 64 * 1024, M_KV = M_ST + 4096, M_FL = M_KV + 4096, M_LDS = M_FL + 64;
constexpr int SPIN_MAX = 1 << 12, RETRY_MAX = 64;
constexpr int NW2G = 18;
#ifndef M_W2T
#define M_W2T {101, 103, 105, 107, 109, 111, 113, 115, 117, 119, 121, 123, 125, 127, 204, 210, 216, 222}
#endif
constexpr int W2T[NW2G] = M_W2T;
constexpr int w2_after() {
  int last = 0;
  for (int i = 0; i < NW2G; ++i) last = W2T[i] > last ? W2T[i] : last;
  return (last < 125) + (last < 127);
}
DI unsigned umax3(unsigned a, unsigned b, unsigned c) { return max(max(a, b), c); }
DI unsigned smax(unsigned m, const u32x4& d) { return umax3(umax3(m, d[0], d[1]), d[2], d[3]); }
DI void csum(const u32x4& f, f32x4& c) {
#pragma unroll
  for (int j = 0; j < 4; ++j) c[j] = dot2(f[j], 0x3f803f80u, c[j]);
}
DI void pin4(u32x4& d) { asm volatile("" : "+v"(d)); }
DI void def4(u32x4& d) { asm volatile("" : "=v"(d)); }
DI bool has_sent(unsigned m) { return __builtin_amdgcn_ballot_w64(m == SENT) != 0; }
DI void probe_wait(const char* base, unsigned off) {
#pragma unroll 1
  for (int k = 0; k < SPIN_MAX; ++k) {
    __builtin_amdgcn_s_sleep(M_RS);
    unsigned pb;
    gload1_c<0>(pb, off, base);
    vwait1<0>(pb);
    if (!has_sent(pb)) break;
  }
}
constexpr int hq(int i) { return M_HQ + i < 27 ? M_HQ + i : 27; }

template <bool spare>
DI void mega(char* smem, char* __restrict__ ws_, const unsigned short* __restrict__ b2, unsigned short* __restrict__ out) {
  MS_BEGIN();
#pragma unroll 1
  for (int layer = 0; layer < 2; ++layer) {
    char* ws = ws_;
    int bid = blockIdx.x, tid = threadIdx.x;
    asm volatile("" : "+s"(ws), "+s"(bid), "+v"(tid));
    const int xcd = bid & 7, bi = bid >> 3;
    const int g = 2 * xcd + (bi >> 4), h = bi & 15;
    const int tp = 2 * g + (h >= 7), ns = spare ? h - 14 : h >= 7 ? h - 7 : h;
    const int w8 = __builtin_amdgcn_readfirstlane(tid >> 6), w = w8 & 3, l = tid & 63;
    const int r = l & 31, hh = l >> 5;
    const unsigned l16 = l * 16;
    const bool lag = w8 < 4;
    const int a8 = w8 >> 2;
    const int so = 4 * ns + w;
    const unsigned sm = lds_addr(smem);
    const char* wb = ws + OFF_WQKV + (size_t)h * (WQF * 1024);
    const int nsw = ns;
    const char* w1g = ws + OFF_W1 + (size_t)(8 * nsw + w8) * (KSA * 1024);
    int sg[7];
    {
      const int rot = h >= 14 ? h - 14 : h >= 7 ? h - 7 : h;
#pragma unroll
      for (int j = 0; j < 7; ++j) sg[j] = (rot + j >= 7 ? rot + j - 7 : rot + j) * 4096;
    }
    auto wdma_kv = [&]() {
#pragma unroll
      for (int j = 0; j < 7; ++j) {
        const int o = ((w >> 1) * KSD + 2 * (w & 1)) * 1024 + sg[j];
        glds<2>(l16, wb + o, sm + M_W + o);
      }
    };
    auto wdma_q = [&]() {
      const char* s = wb + (2 * KSD + 7 * w) * 1024;
      const unsigned d = sm + M_W + (2 * KSD + 7 * w) * 1024;
      glds<4>(l16, s, d);
      glds<3>(l16, s + 4096, d + 4096);
    };
    char* ex = ws + OFF_EX + (size_t)layer * EX_BYTES;
    u32x4 wr[KSA];
    auto w1load = [&](auto I_) {
      constexpr int i = decltype(I_)::v;
      if constexpr (!spare) gload<(i & 7) * 1024 - 4096>(wr[i], l16, w1g + (i >> 3) * 8192 + 4096);
    };
    f32x16 c0, c1;
    u32x4 xres;
    auto c1head = [&](auto L_) {
      constexpr bool lg = decltype(L_)::v;
      const char* og = ex + (size_t)((2 * tp + a8) * H_ + 4 * w) * 2048 + 4096;
      const u32x4* wl = (const u32x4*)(smem + M_W) + w * 1024 + l;
      u32x4 of[8];
      auto oissue = [&]() { sfor<0, 8>([&](auto I_) { gload_c<decltype(I_)::v * 1024 - 4096>(of[decltype(I_)::v], l16, og); }); };
      auto otake = [&](auto N_) {
        constexpr int n = decltype(N_)::v;
        unsigned m = 0;
        u32x4 wf[8][2];
        auto wrd = [&](int i) {
          wf[i][0] = wl[i * 128];
          wf[i][1] = wl[i * 128 + 64];
        };
        wrd(0);
        wrd(1);
        c0 = zero16();
        c1 = zero16();
        sfor<0, 8>([&](auto I_) {
          constexpr int i = decltype(I_)::v;
          if constexpr (i + 2 < 8) wrd(i + 2);
          vwait<n + 7 - i>(of[i]);
          c0 = MFMA32(asfrag(wf[i][0]), asfrag(of[i]), c0);
          c1 = MFMA32(asfrag(wf[i][1]), asfrag(of[i]), c1);
          PINV(c0); PINV(c1);
          m = smax(m, of[i]);
          SCHED();
        });
        return has_sent(m);
      };
      gload_c<0>(xres, l16, ws + OFF_XF + (size_t)(2 * layer) * XF_BYTES + (size_t)((2 * tp + a8) * KSD + so) * 1024);
      if constexpr (lg) sfor<0, M_A0>(w1load);
      __builtin_amdgcn_s_sleep(lg ? M_D4 : M_D4H);
      oissue();
      if constexpr (lg) sfor<M_A0, M_A1>(w1load);
      vwait_mem<lg ? 8 + M_A1 : 8>();
      asm volatile("s_barrier" ::: "memory");
      if (__builtin_expect(otake(IC<lg ? M_A1 - M_A0 : 0>{}), 0)) {
        for (int it = 0; it < RETRY_MAX; ++it) {
          MDBG(3);
          probe_wait(og - 4096, (l & 7) * 1024 + 1020);
          oissue();
          if (!otake(IC<0>{})) break;
        }
      }
    };
    volatile __attribute__((address_space(3))) unsigned* flg = (volatile __attribute__((address_space(3))) unsigned*)(sm + M_FL);
    auto flag_wait = [&]() {
#pragma unroll 1
      for (int k = 0; k < (1 << 20); ++k) {
        const unsigned f = flg[l & 3];
        if (__builtin_amdgcn_ballot_w64(f != (unsigned)layer + 1) == 0) break;
        if (!lag) __builtin_amdgcn_s_sleep(2);
      }
      asm volatile("" ::: "memory");
    };
    char* kp = ex + OF_BYTES + (size_t)(h * 4 + w) * 16384;
    MS(0);
    if (lag) {
      const int tb = 4 * g + w;
      const char* xb = ws + OFF_XF + (size_t)(2 * layer) * XF_BYTES + (size_t)tb * (KSD * 1024);
      u32x4 xr[KSD];
      f32x16 ak, av, aq;
      unsigned wa[7];
#pragma unroll
      for (int j = 0; j < 7; ++j) wa[j] = sm + M_W + l16 + sg[j];
      auto wf = [&](auto S_c, int part) {
        constexpr int s = decltype(S_c)::v;
        return ((lds_u32x4p)wa[s >> 2])[(part * KSD + (s & 3)) * 64];
      };
      auto xseg = [&](auto J_) {
        constexpr int j = decltype(J_)::v;
        return has_sent(max(smax(smax(0u, xr[4 * j]), xr[4 * j + 1]), smax(smax(0u, xr[4 * j + 2]), xr[4 * j + 3])));
      };
      if (layer) __builtin_amdgcn_s_sleep(M_D1);
      sfor<0, KSD>([&](auto S_c) {
        constexpr int s = decltype(S_c)::v;
        gload_c<(s & 3) * 1024>(xr[s], l16, xb + sg[s >> 2]);
      });
      asm volatile("s_barrier" ::: "memory");
      if (layer) asm volatile("s_barrier" ::: "memory");
      constexpr int NV = 4 * M_NVS;
      {
        constexpr int PK = 4, PV = 2;
        u32x4 fk[KSD], fv[KSD];
        sfor<0, PK>([&](auto S_c) {
          fk[decltype(S_c)::v] = wf(S_c, 0);
          if constexpr (decltype(S_c)::v < PV) fv[decltype(S_c)::v] = wf(S_c, 1);
        });
        bool bad;
        auto check = [&](auto J_) {
          constexpr int j = decltype(J_)::v;
          sfor<4 * j, 4 * j + 4>([&](auto S_c) { vwait<24 - 4 * j>(xr[decltype(S_c)::v]); });
          bad = xseg(J_);
        };
        check(IC<0>{});
        MS(1);
        ak = zero16();
        av = zero16();
        sfor<0, KSD>([&](auto S_c) {
          constexpr int s = decltype(S_c)::v, j = s >> 2;
          if constexpr ((s & 3) == 0) {
            if (__builtin_expect(bad, 0)) {
#pragma unroll 1
              for (int it = 0; it < RETRY_MAX; ++it) {
                sfor<4 * j, KSD>([&](auto T_) { vwait<0>(xr[decltype(T_)::v]); });
                if (!xseg(IC<j>{})) break;
                MDBG(0);
                probe_wait(xb, min(l, KSD - 1) * 1024 + 1020);
                sfor<j, 7>([&](auto V_) {
                  constexpr int v = decltype(V_)::v;
                  if (xseg(V_))
                    sfor<4 * v, 4 * v + 4>([&](auto T_) { greload_c<(decltype(T_)::v & 3) * 1024>(xr[decltype(T_)::v], l16, xb + sg[v]); });
                });
              }
              sfor<4 * j, KSD>([&](auto T_) { vwait<0>(xr[decltype(T_)::v]); });
            }
          }
          if constexpr (s + PK == 16) { if (!layer) asm volatile("s_barrier" ::: "memory"); }
          if constexpr (s + PK < KSD) fk[s + PK] = wf(IC<s + PK>{}, 0);
          if constexpr (s + PV < NV) fv[s + PV] = wf(IC<s + PV>{}, 1);
          ak = MFMA32(asfrag(fk[s]), asfrag(xr[s]), ak);
          PINV(ak);
          SCHED();
          if constexpr ((s & 3) == (s < NV ? 3 : 2) && j < 6) { check(IC<j + 1>{}); SCHED(); }
          if constexpr (s < NV) {
            av = MFMA32(asfrag(xr[s]), asfrag(fv[s]), av);
            PINV(av);
            SCHED();
          }
        });
      }
      MS(2);
      float rstd, rs2;
      u32x4 xa;
      constexpr int PD = 4, NR = KSD - NV, NT = NR + 1 + KSA, T1 = 27;
      u32x4 f[NT];
      unsigned qoff = sm + M_W + 3 * KSD * 1024 + l16;
      asm volatile("" : "+v"(qoff));
      const lds_u32x4p wal = (lds_u32x4p)qoff;
      auto fld = [&](auto T_) {
        constexpr int t = decltype(T_)::v;
        if constexpr (t < NR) f[t] = wf(IC<NV + t>{}, 1);
        else if constexpr (t == NR) f[t] = wal[64];
        else if constexpr (t < NT - 1) f[t] = wf(IC<t - NR - 1>{}, 2);
        else if constexpr (t == NT - 1) f[t] = wal[128];
      };
      auto fmm = [&](auto T_) {
        constexpr int t = decltype(T_)::v;
        if constexpr (t < NR) { av = MFMA32(asfrag(xr[NV + t]), asfrag(f[t]), av); PINV(av); }
        else if constexpr (t == NR) { av = MFMA32(asfrag(xa), asfrag(f[t]), av); PINV(av); }
        else if constexpr (t == NR + 1) { aq = MFMA32(asfrag(f[t]), asfrag(xr[0]), zero16()); PINV(aq); }
        else if constexpr (t < NT - 1) { aq = MFMA32(asfrag(f[t]), asfrag(xr[t - NR - 1]), aq); PINV(aq); }
        else { aq = MFMA32(asfrag(f[t]), asfrag(xa), aq); PINV(aq); }
      };
      asm volatile("s_barrier" ::: "memory");
      {
        const u32x4 agk = wal[0];
        xa = ((const u32x4*)(smem + M_KV))[w * 64 + l];
        rstd = ((const float*)(smem + M_ST))[w * 64 + l];
        sfor<0, PD>(fld);
        rs2 = rstd * LOG2E;
        ak = MFMA32(asfrag(agk), asfrag(xa), ak);
        PINV(ak);
      }
      SCHED();
      {
        Smx z;
        u32x4 vfr[2], pa[4];
        char* img = smem + M_R + 32 * 1024 + w * 4096;
        const int i16 = l & 15, q4 = i16 >> 2, p4 = i16 & 3, blk = (l >> 4) & 1;
        f32x4* red = (f32x4*)(smem + M_R) + w * 128 + l;
        auto slot = [&](auto N_, auto Q_) {
          constexpr int n = decltype(N_)::v, q = decltype(Q_)::v;
          smx_slot<n, q>(z, ak, rs2, rstd, hh);
          if constexpr (q >= 17 && ((q - 17) & 1)) {
            constexpr int gg = (q - 17) >> 1;
            const int ch = (2 * gg + hh) ^ ((r >> 1) & 7);
            *(u32x2*)(img + n * 2048 + r * 64 + ch * 8) = z.pk[n][gg];
          }
        };
        fmm(IC<0>{});
        smx_pre(z, ak, rs2);
        SCHED();
        sfor<1, T1 + 1>([&](auto T_) {
          constexpr int t = decltype(T_)::v;
          fld(IC<t + PD - 1>{});
          if constexpr (t - 1 < SMX_SLOTS) { slot(IC<0>{}, IC<t - 1>{}); slot(IC<1>{}, IC<t - 1>{}); }
          if constexpr (t == 26) {
#pragma unroll
            for (int i = 0; i < 4; ++i) {
              const int t0 = 16 * (i & 1) + 4 * hh + q4, t1 = t0 + 8;
              const int c0 = (4 * blk + p4) ^ ((t0 >> 1) & 7), c1 = (4 * blk + p4) ^ ((t1 >> 1) & 7);
              pa[i] = join(tr_read(img + (i >> 1) * 2048 + t0 * 64 + c0 * 8), tr_read(img + (i >> 1) * 2048 + t1 * 64 + c1 * 8));
            }
            vfr[0] = pack8(av, 0);
            vfr[1] = pack8(av, 1);
          }
          fmm(T_);
          SCHED();
        });
        ak = MFMA32(asfrag(pa[0]), asfrag(vfr[0]), zero16());
        av = MFMA32(asfrag(pa[2]), asfrag(vfr[0]), zero16());
        ak = MFMA32(asfrag(pa[1]), asfrag(vfr[1]), ak);
        av = MFMA32(asfrag(pa[3]), asfrag(vfr[1]), av);
        PINV(ak); PINV(av);
        fld(IC<T1 + PD>{});
        fmm(IC<T1 + 1>{});
        SCHED();
#pragma unroll
        for (int gg = 0; gg < 4; ++gg) {
          red[gg * 512] = f32x4{ak[4 * gg], ak[4 * gg + 1], ak[4 * gg + 2], ak[4 * gg + 3]};
          red[gg * 512 + 64] = f32x4{av[4 * gg], av[4 * gg + 1], av[4 * gg + 2], av[4 * gg + 3]};
        }
        asm volatile("" ::: "memory");
        flg[w] = layer + 1;
        SCHED();
        sfor<T1 + 2, T1 + 2 + M_QF>([&](auto T_) { fld(IC<decltype(T_)::v + PD - 1>{}); fmm(T_); SCHED(); });
        flag_wait();
        const f32x4* redr = (const f32x4*)(smem + M_R) + (w & 1) * 1024 + (w >> 1) * 64 + l;
        f32x4 pv[8];
#pragma unroll
        for (int i = 0; i < 8; ++i) pv[i] = redr[i * 128];
        SCHED();
        sfor<T1 + 2 + M_QF, T1 + 4 + M_QF>([&](auto T_) { fld(IC<decltype(T_)::v + PD - 1>{}); fmm(T_); SCHED(); });
        const f32x4 s0 = (pv[0] + pv[1]) + (pv[2] + pv[3]), s1 = (pv[4] + pv[5]) + (pv[6] + pv[7]);
        st_wt<0>((u32x4*)(kp + g * 1024) + l,
                 nosent(u32x4{pack2(s0[0], s0[1]), pack2(s0[2], s0[3]), pack2(s1[0], s1[1]), pack2(s1[2], s1[3])}));
        SCHED();
        MS(3);
        sfor<T1 + 4 + M_QF, NT>([&](auto T_) { fld(IC<decltype(T_)::v + PD - 1>{}); fmm(T_); SCHED(); });
      }
      u32x4 pq[4];
      {
        Smx z;
        smx_pre(z, aq, rs2);
        sfor<0, SMX_SLOTS>([&](auto Q_) {
          constexpr int q = decltype(Q_)::v;
          smx_slot<0, q>(z, aq, rs2, 1.f, hh);
          smx_slot<1, q>(z, aq, rs2, 1.f, hh);
        });
#pragma unroll
        for (int i = 0; i < 4; ++i)
          pq[i] = u32x4{z.pk[i >> 1][2 * (i & 1)][0], z.pk[i >> 1][2 * (i & 1)][1], z.pk[i >> 1][2 * (i & 1) + 1][0], z.pk[i >> 1][2 * (i & 1) + 1][1]};
      }
      if constexpr (!spare) {
        sfor<0, 4>([&](auto I_) { pin4(pq[decltype(I_)::v]); });
#pragma unroll
        for (int q = 0; q < 7; ++q)
          glds<2>(l16, ws + OFF_WO + (size_t)((7 * w + q) * NNT + 2 * nsw) * 1024, sm + M_W + (7 * w + q) * 2048);
      }
      MS(4);
      {
        asm volatile("s_barrier" ::: "memory");
        MS(5);
        const u32x4* kl = (const u32x4*)(smem + M_KV) + l;
        f32x16 o = zero16();
#pragma unroll
        for (int k = 0; k < 4; ++k) o = MFMA32(asfrag(kl[k * 64]), asfrag(pq[k]), o);
        float dlo, dhi;
        halves(o[12] + o[13], dlo, dhi);
        const float inv = __builtin_amdgcn_rcpf(dhi + 1e-6f);
#pragma unroll
        for (int i = 0; i < 16; ++i) o[i] *= inv;
        const u32x4 o0 = nosent(pack8(o, 0)), o1 = nosent(pack8(o, 1));
        u32x4* od = (u32x4*)ex + (size_t)(tb * H_ + h) * 128 + l;
        st_wt<0>(od, o0);
        st_wt<1024>(od, o1);
      }
      asm volatile("s_barrier" ::: "memory");
      MS(6);
      if constexpr (!spare) {
        c1head(IC<1>{});
        sfor<M_A1, KSA>([&](auto I_) { def4(wr[decltype(I_)::v]); });
      }
    } else {
      const char* xs = ws + OFF_XS + (size_t)layer * XS_BYTES + (size_t)(4 * g + w) * 7168 + 4096;
      u32x4 st[7];
      auto fills = [&]() {
        const u32x4 sv = {SENT, SENT, SENT, SENT};
        const int idx = h * 4 + w;
        u32x4* d = (u32x4*)(ws + OFF_HF) + (size_t)(4 * g * KSF + idx * M_HS) * 64 + l;
        sfor<0, M_HS>([&](auto I_) { st_wt<(decltype(I_)::v & 3) * 1024>(d + (decltype(I_)::v >> 2) * 256, sv); });
        d = (u32x4*)(ws + OFF_XF + XF_BYTES) + (size_t)(4 * g * KSD + min(idx, 55) * 2) * 64 + l;
        st_wt<0>(d, sv); st_wt<1024>(d, sv);
        st_wt<0>(d + XF_BYTES / 16, sv); st_wt<1024>(d + XF_BYTES / 16, sv);
        d = (u32x4*)(ex + EX_BYTES) + (size_t)(4 * g * H_ + idx) * 128 + l;
        st_wt<0>(d, sv); st_wt<1024>(d, sv);
      };
      if (layer == 0) {
        sfor<0, 7>([&](auto I_) { gload_c<decltype(I_)::v * 1024 - 4096>(st[decltype(I_)::v], l16, xs); });
        flg[w] = 0;
        const u32x4 sv = {SENT, SENT, SENT, SENT};
        st_wt<0>((u32x4*)(kp + EX_BYTES) + g * 64 + l, sv);
        if (w == 0) glds<3>(l16, wb + 3 * KSD * 1024, sm + M_W + 3 * KSD * 1024);
        wdma_kv();
        wdma_q();
#if M_FE == 1
        fills();
#endif
        vwait_mem<13 + (M_FE == 1 ? NFILL : 0)>();
        BARRIER();
#if M_FE == 2
        fills();
#endif
      } else {
        if constexpr (!spare) wdma_q();
        vwait_mem<7>();
        asm volatile("s_barrier\n\ts_barrier" ::: "memory");
        __builtin_amdgcn_s_sleep(M_D6);
        sfor<0, 7>([&](auto I_) { gload_c<decltype(I_)::v * 1024 - 4096>(st[decltype(I_)::v], l16, xs); });
#pragma unroll 1
        for (int it = 0; it < RETRY_MAX; ++it) {
          unsigned m = 0;
          sfor<0, 7>([&](auto I_) {
            vwait<0>(st[decltype(I_)::v]);
            m = smax(m, st[decltype(I_)::v]);
          });
          if (!has_sent(m)) break;
          MDBG(1);
          probe_wait(xs - 4096, min(l, KSD - 1) * 256 + 252);
          sfor<0, 7>([&](auto I_) { greload_c<decltype(I_)::v * 1024 - 4096>(st[decltype(I_)::v], l16, xs); });
        }
        vwait_mem<0>();
      }
      {
        sfor<0, 7>([&](auto I_) { pin4(st[decltype(I_)::v]); });
        f32x4 t = __builtin_bit_cast(f32x4, st[0]);
#pragma unroll
        for (int i = 1; i < 7; ++i) t += __builtin_bit_cast(f32x4, st[i]);
        float* tr = (float*)(smem + M_R + 48 * 1024) + w * 256;
        ((f32x4*)tr)[l] = t;
        float s1, s2, mean, rstd, sd;
        halves((tr[l] + tr[64 + l]) + (tr[128 + l] + tr[192 + l]), s1, s2);
        finish_stats(f32x2{s1, s2}, mean, rstd, sd);
        ((u32x4*)(smem + M_KV))[w * 64 + l] = aug_x(mean, sd, hh);
        ((float*)(smem + M_ST))[w * 64 + l] = rstd;
      }
      if (layer == 0) {
        vwait_mem<7 + (M_FE == 1 || M_FE == 2 ? NFILL : 0)>();
        asm volatile("s_barrier" ::: "memory");
#if M_FE == 3
        fills();
#endif
        vwait_mem<M_FE ? NFILL : 0>();
      }
      BARRIER();
      MS(1);
      sfor<0, KSA>(w1load);
#if M_FE == 0
      if (layer == 0) fills();
#endif
      MS(2);
      flag_wait();
      __builtin_amdgcn_s_setprio(3);
      MS(3);
      {
        u32x4 pv[16];
        float kvs[8];
        __builtin_amdgcn_s_sleep(M_D2);
        sfor<0, 16>([&](auto I_) {
          constexpr int i = decltype(I_)::v;
          gload_c<(i & 7) * 1024 - 4096>(pv[i], l16, kp + (i >> 3) * 8192 + 4096);
        });
#pragma unroll
        for (int j = 0; j < 8; ++j) kvs[j] = 0.f;
        sfor<0, 16>([&](auto I_) {
          constexpr int i = decltype(I_)::v;
          vwait<15 - i>(pv[i]);
          if (__builtin_expect(has_sent(smax(0u, pv[i])), 0)) {
#pragma unroll 1
            for (int it = 0; it < SPIN_MAX; ++it) {
              sfor<i, 16>([&](auto T_) { vwait<0>(pv[decltype(T_)::v]); });
              if (!has_sent(smax(0u, pv[i]))) break;
              MDBG(0);
              __builtin_amdgcn_s_sleep(M_RK);
              if (it >= 8) __builtin_amdgcn_s_sleep(32);
              sfor<i, 16>([&](auto T_) {
                constexpr int t = decltype(T_)::v;
                if (has_sent(smax(0u, pv[t]))) greload_c<(t & 7) * 1024 - 4096>(pv[t], l16, kp + (t >> 3) * 8192 + 4096);
              });
            }
            sfor<i, 16>([&](auto T_) { vwait<0>(pv[decltype(T_)::v]); });
          }
#pragma unroll
          for (int j = 0; j < 4; ++j) {
            kvs[2 * j] = opq(kvs[2 * j] + bflo(pv[i][j]));
            kvs[2 * j + 1] = opq(kvs[2 * j + 1] + bfhi(pv[i][j]));
          }
          SCHED();
        });
        ((u32x4*)(smem + M_KV))[w * 64 + l] = u32x4{pack2(kvs[0], kvs[1]), pack2(kvs[2], kvs[3]), pack2(kvs[4], kvs[5]), pack2(kvs[6], kvs[7])};
        BARRIER();
      }
      __builtin_amdgcn_s_setprio(0);
      MS(5);
      if constexpr (!spare) glds<2>(l16, ws + OFF_WO + (size_t)((28 + w) * NNT + 2 * nsw) * 1024, sm + M_W + (28 + w) * 2048);
      asm volatile("s_barrier" ::: "memory");
      MS(6);
      if constexpr (!spare) c1head(IC<0>{});
    }
    if constexpr (spare) {
      if (layer == 0) {
        const u32x4 sv = {SENT, SENT, SENT, SENT};
        const int idx = ns * 8 + w8;
        u32x4* d = (u32x4*)(ws + OFF_HF) + (size_t)(4 * g * KSF + 64 * M_HS + idx * (28 - 4 * M_HS)) * 64 + l;
#pragma unroll 1
        for (int i = 0; i < 7 - M_HS; ++i) {
          st_wt<0>(d, sv); st_wt<1024>(d, sv); st_wt<2048>(d, sv); st_wt<3072>(d, sv);
          d += 256;
          __builtin_amdgcn_s_sleep(M_HSL);
        }
        vwait_mem<0>();
        if (l == 0) st_wt1((float*)(ws + OFF_FLG) + g * 64 + idx, 0.f);
        d = (u32x4*)(ws + OFF_HF + HF_BYTES) + (size_t)(4 * g * KSF + idx * 28) * 64 + l;
#pragma unroll 1
        for (int i = 0; i < 7; ++i) {
          st_wt<0>(d, sv); st_wt<1024>(d, sv); st_wt<2048>(d, sv); st_wt<3072>(d, sv);
          d += 256;
          __builtin_amdgcn_s_sleep(16);
        }
        d = (u32x4*)(ws + OFF_XF + 3 * XF_BYTES) + (size_t)(4 * g * KSD + idx * 7) * 64 + l;
        st_wt<0>(d, sv); st_wt<1024>(d, sv); st_wt<2048>(d, sv); st_wt<3072>(d, sv);
        st_wt<0>(d + 256, sv); st_wt<1024>(d + 256, sv); st_wt<2048>(d + 256, sv);
#pragma unroll 1
        for (int k = 0; k < SPIN_MAX; ++k) {
          unsigned pb;
          gload1_c<0>(pb, (l & 15) * 2048, ex + (size_t)(4 * g * H_) * 2048);
          vwait1<0>(pb);
          if (!has_sent(pb)) break;
          __builtin_amdgcn_s_sleep(8);
        }
#pragma unroll 1
        for (int k = 0; k < SPIN_MAX; ++k) {
          __builtin_amdgcn_s_sleep(M_SS);
          unsigned pb;
          gload1_c<0>(pb, min(l, 27) * 1024, ws + OFF_XS + XS_BYTES + (size_t)(4 * g) * 7168);
          vwait1<0>(pb);
          if (__builtin_popcountll(__builtin_amdgcn_ballot_w64(pb == SENT)) < 60) break;
        }
      }
      MS_LAYER();
    } else {
    int pofs[7];
#pragma unroll
    for (int j = 0; j < 7; ++j) pofs[j] = ns + j >= 7 ? ns + j - 7 : ns + j;
    auto pwrite = [&](int off) {
      f32x4* pw = (f32x4*)(smem + M_R + off) + w * 512 + l;
#pragma unroll
      for (int gg = 0; gg < 4; ++gg) {
        pw[gg * 64] = f32x4{c0[4 * gg], c0[4 * gg + 1], c0[4 * gg + 2], c0[4 * gg + 3]};
        pw[(4 + gg) * 64] = f32x4{c1[4 * gg], c1[4 * gg + 1], c1[4 * gg + 2], c1[4 * gg + 3]};
      }
    };
    f32x4 sum[2];
    auto padd = [&](int off) {
      const f32x4* pr = (const f32x4*)(smem + M_R + off) + w * 128 + l;
      f32x4 pv[8];
#pragma unroll
      for (int i = 0; i < 8; ++i) pv[i] = pr[((i >> 1) * 8 + (i & 1)) * 64];
      SCHED();
#pragma unroll
      for (int gg = 0; gg < 2; ++gg) sum[gg] += (pv[gg] + pv[2 + gg]) + (pv[4 + gg] + pv[6 + gg]);
      PINV(sum[0]); PINV(sum[1]);
    };
    auto psum = [&]() {
      return nosent(u32x4{pack2(sum[0][0], sum[0][1]), pack2(sum[0][2], sum[0][3]), pack2(sum[1][0], sum[1][1]), pack2(sum[1][2], sum[1][3])});
    };
    MS(7);
    pwrite(a8 * 32768);
    PINV(xres);
#pragma unroll
    for (int gg = 0; gg < 2; ++gg)
      sum[gg] = f32x4{bflo(xres[2 * gg]), bfhi(xres[2 * gg]), bflo(xres[2 * gg + 1]), bfhi(xres[2 * gg + 1])};
    BARRIER();
    padd(a8 * 32768);
    xres = psum();
    char* x1 = ws + OFF_XF + (size_t)(2 * layer + 1) * XF_BYTES + (size_t)((2 * tp + a8) * KSD + w) * 1024;
    st_wt<0>((u32x4*)(x1 + ns * 4096) + l, xres);
    MS(8);
    const unsigned l16b = l16 + KSF * 1024;
    const char* w2g = ws + OFF_W2 + (size_t)(2 * nsw * KSF + 2 * w8) * 1024;
    {
      u32x4* xw = (u32x4*)(smem + M_R) + w * 64 + l;
      float* stw = (float*)(smem + M_ST) + w * 64 + l;
      stw[a8 * 512 + 256] = rstat(xres);
      auto w1aug = [&]() { sfor<0, KSA>([&](auto I_) { pin4(wr[decltype(I_)::v]); }); };
      if (lag) {
        u32x4 xr[12];
        const unsigned l16c = l16 + KSD * 1024;
        auto xissue = [&]() {
          sfor<0, 12>([&](auto I_) {
            constexpr int i = decltype(I_)::v;
            gload_c<0>(xr[i], (i & 1) ? l16c : l16, x1 + pofs[(i >> 1) + 1] * 4096);
          });
        };
        auto xtake = [&]() {
          unsigned m = 0;
          float s1[2] = {0.f, 0.f}, s2[2] = {0.f, 0.f};
          sfor<0, 12>([&](auto I_) {
            constexpr int i = decltype(I_)::v;
            vwait<11 - i>(xr[i]);
            xw[pofs[(i >> 1) + 1] * 256 + (i & 1) * (KSD * 64)] = xr[i];
            m = smax(m, xr[i]);
            rsum(xr[i], s1[i & 1], s2[i & 1]);
            SCHED();
          });
          stw[0] = rjoin(s1[0], s2[0]);
          stw[512] = rjoin(s1[1], s2[1]);
          return has_sent(m);
        };
        sfor<M_A1, M_A2>(w1load);
        __builtin_amdgcn_s_sleep(M_D5);
        xissue();
        asm volatile("s_barrier" ::: "memory");
        xw[ns * 256] = xres;
        if (__builtin_expect(xtake(), 0)) {
          for (int it = 0; it < RETRY_MAX; ++it) {
            MDBG(1);
            probe_wait(x1, min(l >> 1, 6) * 4096 + (l & 1) * (KSD * 1024) + 1020);
            xissue();
            if (!xtake()) break;
          }
        }
        unsigned pb;
        gload1_c<0>(pb, (l & 15) * 4, ws + OFF_FLG + g * 256);
        sfor<M_A2, KSA>(w1load);
        MS(9);
        BARRIER();
        MS(10);
        vwait1<KSA - M_A2>(pb);
        if (__builtin_expect(has_sent(pb), 0)) { MDBG(2); probe_wait(ws + OFF_FLG + g * 256, (l & 15) * 4); }
        asm volatile("s_barrier\n\ts_waitcnt vmcnt(0)" ::: "memory");
      } else {
        asm volatile("s_barrier" ::: "memory");
        xw[ns * 256 + KSD * 64] = xres;
        sfor<0, 20>([&](auto I_) {
          constexpr int i = NW2G + (decltype(I_)::v >> 1), o = decltype(I_)::v & 1;
          glds<1>((i & 1) ? l16b : l16, w2g + pofs[i >> 2] * 16384 + ((i >> 1) & 1) * 1024 - o * 8192, sm + M_W + (w8 * 10 + i - NW2G - 40 * o) * 1024);
        });
        vwait_mem<20>();
        MS(9);
        BARRIER();
        MS(10);
      }
      w1aug();
    }
    u32x4 bq0 = ((const u32x4*)(b2 + 16 * so))[0], bq1 = ((const u32x4*)(b2 + 16 * so))[1];
    char* hg = ws + OFF_HF + (size_t)layer * HF_BYTES + (size_t)(2 * tp * KSF + 2 * w8) * 1024;
    u32x4 w2r[28];
    auto w2at = [&](auto T_) {
      sfor<0, NW2G>([&](auto I_) {
        constexpr int i = decltype(I_)::v;
        if constexpr (W2T[i] == decltype(T_)::v) {
          gload<((i >> 1) & 1) * 1024>(w2r[i], (i & 1) ? l16b : l16, w2g + pofs[i >> 2] * 16384);
          SCHED();
        }
      });
    };
    unsigned xo0 = sm + M_R + l16;
    asm volatile("" : "+v"(xo0));
    const lds_u32x4p xl = (lds_u32x4p)xo0;
    constexpr int PD = 3;
    u32x4 xb[2][KSA];
#pragma unroll
    for (int s = 0; s < PD; ++s) xb[0][s] = xl[s * 64];
    f32x16 acc1, u;
    u32x4 hown[2][2];
    float k0, k1, sd;
    auto epi = [&](auto A_, auto Q_) {
      constexpr int a = decltype(A_)::v, q = decltype(Q_)::v;
      constexpr int k = q >> 2, gg = q & 3;
      const f32x16& y = acc1;
#pragma unroll
      for (int i = 4 * gg; i < 4 * gg + 4; ++i) {
        if constexpr (k == 0) u[i] = opq(y[i] * y[i]);
        else if constexpr (k == 1) u[i] = opq(__builtin_fmaf(u[i], k1, k0));
        else if constexpr (k == 2) u[i] = opq(u[i] * y[i]);
        else if constexpr (k == 3) u[i] = __builtin_amdgcn_exp2f(u[i]);
        else if constexpr (k == 4) u[i] = opq(__builtin_fmaf(u[i], sd, sd));
        else if constexpr (k == 5) u[i] = __builtin_amdgcn_rcpf(u[i]);
        else if constexpr (k == 6) u[i] = opq(u[i] * y[i]);
      }
      if constexpr (k == 6 && (gg & 1)) {
        hown[a][gg >> 1] = nosent(pack8(u, gg >> 1));
        st_wt<(gg >> 1) * 1024>((u32x4*)(hg + ns * 16384 + a * (KSF * 1024)) + l, hown[a][gg >> 1]);
      }
    };
    u32x4 hr[12];
    auto hload1 = [&](auto A_, auto I_) {
      constexpr int a = decltype(A_)::v, i = decltype(I_)::v;
      gload_c<(i & 1) * 1024>(hr[i], a ? l16b : l16, hg + pofs[1 + (i >> 1)] * 16384);
    };
    auto hload = [&](auto A_, auto J_) {
      hload1(A_, IC<2 * decltype(J_)::v - 2>{});
      hload1(A_, IC<2 * decltype(J_)::v - 1>{});
    };
    sfor<0, 2>([&](auto A_) {
      constexpr int a = decltype(A_)::v;
      acc1 = zero16();
      float sp[8], s1, s2, mean, rstd, v, r1;
      unsigned p1, p2, p3;
      sfor<0, KSA>([&](auto S_c) {
        constexpr int s = decltype(S_c)::v;
        if constexpr (s + PD < KSD) xb[a][s + PD] = xl[(a * KSD + s + PD) * 64];
        if constexpr (s == 2) {
#pragma unroll
          for (int i = 0; i < 8; ++i) sp[i] = ((const float*)(smem + M_ST))[(8 * a + i) * 64 + l];
        }
        if constexpr (s == 5) { sp[0] = opq(opq(sp[0] + sp[1]) + opq(sp[2] + sp[3])); sp[4] = opq(opq(sp[4] + sp[5]) + opq(sp[6] + sp[7])); PINV(sp[0]); PINV(sp[4]); }
        if constexpr (s == 6) { halves(sp[0] + sp[4], s1, s2); PINV(s1); PINV(s2); }
        if constexpr (s == 8) { finish_stats(f32x2{s1, s2}, mean, rstd, sd); PINV(mean); PINV(rstd); PINV(sd); }
        if constexpr (s == 10) {
          const float e0 = -2.f * 0.7978845608028654f * LOG2E;
          k0 = e0 * rstd;
          k1 = (e0 * 0.044715f) * rstd * rstd * rstd;
          v = hh ? sd : -mean;
          PINV(k0); PINV(k1); PINV(v);
        }
        if constexpr (s == 12) { p1 = bfbits(v); r1 = v - bflo(p1); PINV(p1); PINV(r1); }
        if constexpr (s == 14) { p2 = bfbits(r1); p3 = bfbits(r1 - bflo(p2)); PINV(p2); PINV(p3); }
        if constexpr (s == 16) {
          const unsigned ab = p1 | (p2 << 16);
          xb[a][KSD] = u32x4{hh ? ab : (p1 | (p1 << 16)), hh ? p3 : ab, hh ? 0u : (p2 | (p3 << 16)), 0u};
          PINV(xb[a][KSD]);
        }
        w2at(IC<200 * a + s>{});
        acc1 = MFMA32(asfrag(wr[s]), asfrag(xb[a][s]), acc1);
        PINV(acc1);
        SCHED();
      });
      BARRIER();
      SCHED();
      MS(11 + 2 * a);
      if constexpr (a == 1) vwait_mem<NW2G + 2>();
      sfor<0, 28>([&](auto Q_) {
        constexpr int q = decltype(Q_)::v;
        epi(A_, Q_);
        if constexpr (a == 0) w2at(IC<100 + q>{});
        if constexpr (a == 1 && NW2G + q < 28) { w2r[NW2G + q] = ((const u32x4*)(smem + M_W))[(w8 * 10 + q) * 64 + l]; SCHED(); }
        if constexpr (a == 1) sfor<0, 12>([&](auto I_) { if constexpr (hq(decltype(I_)::v) == q) { hload1(IC<0>{}, I_); SCHED(); } });
      });
      SCHED();
      if constexpr (a == 0) {
#pragma unroll
        for (int s = 0; s < PD; ++s) xb[1][s] = xl[(KSD + s) * 64];
      }
      BARRIER();
      SCHED();
      MS(12 + 2 * a);
    });
    vwait<w2_after() + 2 + 12>(w2r[0]);
    sfor<1, NW2G>([&](auto I_) { pin4(w2r[decltype(I_)::v]); });
    auto gemm2 = [&](auto A_) {
      constexpr int a = decltype(A_)::v;
      bool bad;
      auto check = [&](auto J_) {
        constexpr int j = decltype(J_)::v;
        constexpr int n = 12 - 2 * j + (a == 0 ? (hq(2 * j - 1) < 25) + (hq(2 * j - 1) < 27) : 0);
        vwait<n>(hr[2 * j - 2]);
        vwait<n>(hr[2 * j - 1]);
        bad = has_sent(max(smax(0u, hr[2 * j - 2]), smax(0u, hr[2 * j - 1])));
      };
      c0 = MFMA32(asfrag(w2r[0]), asfrag(hown[a][0]), zero16());
      c1 = MFMA32(asfrag(w2r[1]), asfrag(hown[a][0]), zero16());
      PINV(c0); PINV(c1);
      SCHED();
      check(IC<1>{});
      SCHED();
      c0 = MFMA32(asfrag(w2r[2]), asfrag(hown[a][1]), c0);
      c1 = MFMA32(asfrag(w2r[3]), asfrag(hown[a][1]), c1);
      PINV(c0); PINV(c1);
      SCHED();
      sfor<1, 7>([&](auto J_) {
        constexpr int j = decltype(J_)::v;
        u32x4 &h0 = hr[2 * j - 2], &h1 = hr[2 * j - 1];
        if (__builtin_expect(bad, 0)) {
          for (int it = 0; it < SPIN_MAX; ++it) {
            MDBG(2);
            hload(A_, J_);
            vwait<0>(h0);
            vwait<0>(h1);
            if (!has_sent(smax(smax(0u, h0), h1))) break;
          }
        }
        c0 = MFMA32(asfrag(w2r[4 * j]), asfrag(h0), c0);
        c1 = MFMA32(asfrag(w2r[4 * j + 1]), asfrag(h0), c1);
        PINV(c0); PINV(c1);
        SCHED();
        if constexpr (j < 6) check(IC<j + 1>{});
        if constexpr (a == 1 && j == M_MID) { if (lag) asm volatile("s_barrier" ::: "memory"); }
        SCHED();
        c0 = MFMA32(asfrag(w2r[4 * j + 2]), asfrag(h1), c0);
        c1 = MFMA32(asfrag(w2r[4 * j + 3]), asfrag(h1), c1);
        PINV(c0); PINV(c1);
        SCHED();
      });
    };
    gemm2(IC<0>{});
    asm volatile("s_barrier" ::: "memory");
    SCHED();
    MS(15);
    sfor<1, 7>([&](auto J_) { hload(IC<1>{}, J_); });
    pwrite(lag ? 32768 : 0);
    {
      const unsigned b00 = bq0[0], b01 = bq0[1], b02 = bq0[2], b03 = bq0[3], b10 = bq1[0], b11 = bq1[1], b12 = bq1[2], b13 = bq1[3];
      const unsigned m = hh ? ~0u : 0u;
      const u32x2 bs[2] = {u32x2{(b02 & m) | (b00 & ~m), (b03 & m) | (b01 & ~m)}, u32x2{(b12 & m) | (b10 & ~m), (b13 & m) | (b11 & ~m)}};
#pragma unroll
      for (int gg = 0; gg < 2; ++gg) {
        sum[gg][0] = bflo(bs[gg][0]) + bflo(xres[2 * gg]);
        sum[gg][1] = bfhi(bs[gg][0]) + bfhi(xres[2 * gg]);
        sum[gg][2] = bflo(bs[gg][1]) + bflo(xres[2 * gg + 1]);
        sum[gg][3] = bfhi(bs[gg][1]) + bfhi(xres[2 * gg + 1]);
      }
      PINV(sum[0]); PINV(sum[1]);
    }
    if (lag) padd(0);
    BARRIER();
    SCHED();
    MS(16);
    if (lag) padd(32768);
    gemm2(IC<1>{});
    if (!lag) asm volatile("s_barrier" ::: "memory");
    SCHED();
    MS(17);
    pwrite(lag ? 32768 : 0);
    BARRIER();
    SCHED();
    if (!lag) {
      padd(0);
      if (layer == 0) wdma_kv();
      asm volatile("s_barrier" ::: "memory");
      padd(32768);
    }
    SCHED();
    MS(18);
    xres = psum();
    if (layer) {
      unsigned short* o = out + (size_t)((2 * tp + a8) * 32 + r) * D_ + 16 * so + 4 * hh;
      *(u32x2*)(o + 0) = u32x2{xres[0], xres[1]};
      *(u32x2*)(o + 8) = u32x2{xres[2], xres[3]};
    } else {
      st_wt<0>((u32x4*)(ws + OFF_XF + 2 * XF_BYTES) + (size_t)((2 * tp + a8) * KSD + so) * 64 + l, xres);
      st_wt1((float*)(ws + OFF_XS + XS_BYTES) + ((2 * tp + a8) * KSD + so) * 64 + l, rstat(xres));
    }
    MS(19);
    MS_LAYER();
    }
  }
  MS_END();
}

__global__ __launch_bounds__(512, 2) void k_mega(char* __restrict__ ws, const unsigned short* __restrict__ b2,
                                                 unsigned short* __restrict__ out) {
  __shared__ __attribute__((aligned(16))) char smem[M_LDS];
  if (((blockIdx.x >> 3) & 15) >= 14) mega<true>(smem, ws, b2, out);
  else mega<false>(smem, ws, b2, out);
}

}

extern "C" void kernel_launch(void* const* d_in, const int* in_sizes, int n_in, void* d_out, int out_size,
                              void* d_ws, size_t ws_size, hipStream_t stream) {
  const unsigned short* x = (const unsigned short*)d_in[0];
  const unsigned short* wqkv = (const unsigned short*)d_in[1];
  const unsigned short* wphi = (const unsigned short*)d_in[2];
  const unsigned short* wo = (const unsigned short*)d_in[3];
  const unsigned short* w1 = (const unsigned short*)d_in[5];
  const unsigned short* b1 = (const unsigned short*)d_in[6];
  const unsigned short* w2 = (const unsigned short*)d_in[7];
  const unsigned short* b2 = (const unsigned short*)d_in[8];
  char* ws = (char*)d_ws;
  k_prep<<<P_BLOCKS, 256, 0, stream>>>(x, wqkv, wphi, wo, w1, b1, w2, ws);
  k_mega<<<256, 512, 0, stream>>>(ws, b2, (unsigned short*)d_out);
}
```

```cpp
#include <hip/hip_runtime.h>
#include <hip/hip_bf16.h>
#include <stdint.h>

typedef __attribute__((ext_vector_type(8))) short bf16x8;
typedef __attribute__((ext_vector_type(4))) short s16x4;
typedef __attribute__((ext_vector_type(16))) float f32x16;
typedef __attribute__((ext_vector_type(4))) float f32x4;
typedef __attribute__((ext_vector_type(2))) float f32x2;
typedef __attribute__((ext_vector_type(4))) unsigned u32x4;
typedef __attribute__((ext_vector_type(2))) unsigned u32x2;
typedef __attribute__((ext_vector_type(2))) __bf16 bf16x2_t;

#define DI __device__ __forceinline__
#define SCHED() __builtin_amdgcn_sched_barrier(0)
#define PIN(x) asm volatile("" : "+a"(x))
#define PINV(x) asm volatile("" : "+v"(x))
#define MFMA32(a, b, c) __builtin_amdgcn_mfma_f32_32x32x16_bf16((a), (b), (c), 0, 0, 0)

#define STAMP_BEGIN()
#define STAMP_AT(kid, i)
#define STAMP_NW(kid, i)
#define STAMP_END(kid, n)
#define MS_BEGIN()
#define MS(i)
#define MDBG(i)
#define MS_LAYER()
#define MS_END()

namespace {

constexpr int S_ = 2048, D_ = 448, H_ = 16, DH_ = 28, F_ = 1792;
constexpr int NTB = S_ / 32;
constexpr int KSD = D_ / 16;
constexpr int KSF = F_ / 16;
constexpr int NNT = D_ / 32;
constexpr int NFT = F_ / 32;
constexpr int KSA = KSD + 1;

constexpr size_t XF_BYTES   = (size_t)NTB * KSD * 1024;
constexpr size_t HF_BYTES   = (size_t)NTB * KSF * 1024;
constexpr size_t OF_BYTES   = (size_t)NTB * H_ * 2048;
constexpr size_t KVP_BYTES  = (size_t)H_ * 4 * 16 * 1024;
constexpr size_t EX_BYTES   = OF_BYTES + KVP_BYTES;
constexpr int    WQF        = 3 * KSA;
constexpr size_t OFF_XF     = 0;
constexpr size_t OFF_WQKV   = OFF_XF + 4 * XF_BYTES;
constexpr size_t OFF_WO     = OFF_WQKV + (size_t)H_ * WQF * 1024;
constexpr size_t OFF_W1     = OFF_WO + (size_t)H_ * 2 * NNT * 1024;
constexpr size_t OFF_W2     = OFF_W1 + (size_t)NFT * KSA * 1024;
constexpr size_t OFF_HF     = OFF_W2 + (size_t)NNT * KSF * 1024;
constexpr size_t OFF_EX     = OFF_HF + 2 * HF_BYTES;
constexpr size_t XS_BYTES   = (size_t)NTB * 7 * 1024;
constexpr size_t OFF_XS     = OFF_EX + 2 * EX_BYTES;
constexpr size_t OFF_FLG    = OFF_XS + 2 * XS_BYTES;
constexpr size_t OFF_CSQ    = OFF_FLG + 4096;
constexpr size_t OFF_B2     = OFF_CSQ + (size_t)H_ * 3 * 4096;
constexpr size_t WS_TOTAL   = OFF_B2 + 1024;
constexpr unsigned SENT = 0xffffffffu;

DI unsigned pack2(float a, float b) {
  f32x2 v = {a, b};
  bf16x2_t r = __builtin_convertvector(v, bf16x2_t);
  return __builtin_bit_cast(unsigned, r);
}
DI float bflo(unsigned u) { return __uint_as_float(u << 16); }
DI float bfhi(unsigned u) { return __uint_as_float(u & 0xffff0000u); }
DI float bf1(unsigned short u) { return __uint_as_float(((unsigned)u) << 16); }
DI bf16x8 asfrag(u32x4 v) { return __builtin_bit_cast(bf16x8, v); }
DI u32x4 pack8(const f32x16& x, int sp) {
  u32x4 p;
  p[0] = pack2(x[8 * sp + 0], x[8 * sp + 1]);
  p[1] = pack2(x[8 * sp + 2], x[8 * sp + 3]);
  p[2] = pack2(x[8 * sp + 4], x[8 * sp + 5]);
  p[3] = pack2(x[8 * sp + 6], x[8 * sp + 7]);
  return p;
}
DI float xor32(float v) { return __shfl_xor(v, 32, 64); }
DI void halves(float v, float& lo, float& hi) {
  auto r = __builtin_amdgcn_permlane32_swap(__float_as_uint(v), __float_as_uint(v), false, false);
  lo = __uint_as_float(r[0]);
  hi = __uint_as_float(r[1]);
}
DI float sum32(float v) { float a, b; halves(v, a, b); return a + b; }
DI float max32(float v) { float a, b; halves(v, a, b); return fmaxf(a, b); }
DI float min32(float v) { float a, b; halves(v, a, b); return fminf(a, b); }
DI float opq(float v) { asm("" : "+v"(v)); return v; }
DI f32x16 zero16() {
  f32x16 z;
#pragma unroll
  for (int i = 0; i < 16; ++i) z[i] = 0.f;
  return z;
}
template <int N>
DI void glds(unsigned voff, const void* sbase, unsigned lds) {
  unsigned keep;
  if constexpr (N == 4)
    asm volatile("s_mov_b32 %0, m0\n\ts_mov_b32 m0, %3\n\ts_nop 4\n\t"
                 "global_load_lds_dwordx4 %1, %2\n\tglobal_load_lds_dwordx4 %1, %2 offset:1024\n\t"
                 "global_load_lds_dwordx4 %1, %2 offset:2048\n\tglobal_load_lds_dwordx4 %1, %2 offset:3072\n\t"
                 "s_mov_b32 m0, %0" : "=&s"(keep) : "v"(voff), "s"(sbase), "s"(lds) : "memory");
  else if constexpr (N == 3)
    asm volatile("s_mov_b32 %0, m0\n\ts_mov_b32 m0, %3\n\ts_nop 4\n\t"
                 "global_load_lds_dwordx4 %1, %2\n\tglobal_load_lds_dwordx4 %1, %2 offset:1024\n\t"
                 "global_load_lds_dwordx4 %1, %2 offset:2048\n\t"
                 "s_mov_b32 m0, %0" : "=&s"(keep) : "v"(voff), "s"(sbase), "s"(lds) : "memory");
  else if constexpr (N == 2)
    asm volatile("s_mov_b32 %0, m0\n\ts_mov_b32 m0, %3\n\ts_nop 4\n\t"
                 "global_load_lds_dwordx4 %1, %2\n\tglobal_load_lds_dwordx4 %1, %2 offset:1024\n\t"
                 "s_mov_b32 m0, %0" : "=&s"(keep) : "v"(voff), "s"(sbase), "s"(lds) : "memory");
  else
    asm volatile("s_mov_b32 %0, m0\n\ts_mov_b32 m0, %3\n\ts_nop 4\n\t"
                 "global_load_lds_dwordx4 %1, %2\n\t"
                 "s_mov_b32 m0, %0" : "=&s"(keep) : "v"(voff), "s"(sbase), "s"(lds) : "memory");
}
template <int OFF>
DI void gload(u32x4& d, unsigned voff, const void* sbase) {
  asm volatile("global_load_dwordx4 %0, %1, %2 offset:%3" : "=v"(d) : "v"(voff), "s"(sbase), "i"(OFF));
}
template <int OFF>
DI void gload_r(u32x4& d, unsigned voff, const void* sbase) {
  asm volatile("global_load_dwordx4 %0, %1, %2 offset:%3" : "+v"(d) : "v"(voff), "s"(sbase), "i"(OFF));
}
template <int OFF>
DI void gload2(f32x2& d, unsigned voff, const void* sbase) {
  asm volatile("s_nop 4\n\tglobal_load_dwordx2 %0, %1, %2 offset:%3" : "=v"(d) : "v"(voff), "s"(sbase), "i"(OFF));
}
template <int OFF>
DI void gload_c(u32x4& d, unsigned voff, const void* sbase) {
  asm volatile("global_load_dwordx4 %0, %1, %2 offset:%3 sc1" : "=v"(d) : "v"(voff), "s"(sbase), "i"(OFF));
}
template <int OFF>
DI void greload_c(u32x4& d, unsigned voff, const void* sbase) {
  asm volatile("global_load_dwordx4 %0, %1, %2 offset:%3 sc1" : "+v"(d) : "v"(voff), "s"(sbase), "i"(OFF));
}
template <int OFF>
DI void gload2_c(u32x2& d, unsigned voff, const void* sbase) {
  asm volatile("global_load_dwordx2 %0, %1, %2 offset:%3 sc1" : "=v"(d) : "v"(voff), "s"(sbase), "i"(OFF));
}
template <int N> DI void vwait2u(u32x2& d) { asm volatile("s_waitcnt vmcnt(%1)" : "+v"(d) : "i"(N)); }
template <int OFF>
DI void gload1_c(unsigned& d, unsigned voff, const void* sbase) {
  asm volatile("global_load_dword %0, %1, %2 offset:%3 sc1" : "=v"(d) : "v"(voff), "s"(sbase), "i"(OFF));
}
DI void gload1(unsigned& d, unsigned voff, const void* sbase) {
  asm volatile("global_load_dword %0, %1, %2" : "=v"(d) : "v"(voff), "s"(sbase));
}
template <int N> DI void vwait1(unsigned& d) { asm volatile("s_waitcnt vmcnt(%1)" : "+v"(d) : "i"(N)); }
DI u32x4 nosent(u32x4 v) {
  return u32x4{min(v[0], SENT - 1), min(v[1], SENT - 1), min(v[2], SENT - 1), min(v[3], SENT - 1)};
}
template <int OFF = 0>
DI void st_wt(u32x4* p, u32x4 v) {
  asm volatile("global_store_dwordx4 %0, %1, off offset:%2 sc1\n\ts_nop 1" :: "v"(p), "v"(v), "i"(OFF) : "memory");
}
template <int OFF = 0>
DI void st_p(u32x4* p, u32x4 v) {
  st_wt<OFF>(p, v);
}
DI void st_wt2(void* p, u32x2 v) {
  asm volatile("global_store_dwordx2 %0, %1, off sc1\n\ts_nop 1" :: "v"(p), "v"(v) : "memory");
}
DI void st_wt1(float* p, float v) {
  asm volatile("global_store_dword %0, %1, off sc1" :: "v"(p), "v"(v) : "memory");
}
template <int N> DI void vwait(u32x4& d) { asm volatile("s_waitcnt vmcnt(%1)" : "+v"(d) : "i"(N)); }
template <int N> DI void vwait2(f32x2& d) { asm volatile("s_waitcnt vmcnt(%1)" : "+v"(d) : "i"(N)); }
template <int N> DI void vwait_mem() { asm volatile("s_waitcnt vmcnt(%0)" :: "i"(N) : "memory"); }
DI unsigned lds_addr(const void* p) {
  return (unsigned)(uintptr_t)(const __attribute__((address_space(3))) char*)p;
}
DI unsigned bfbits(float v) { return pack2(v, 0.f) & 0xffffu; }
DI void split3(float v, unsigned& p1, unsigned& p2, unsigned& p3) {
  p1 = bfbits(v);
  const float r1 = v - bflo(p1);
  p2 = bfbits(r1);
  p3 = bfbits(r1 - bflo(p2));
}
DI u32x4 aug_w(float cs) {
  unsigned c1, c2, c3;
  split3(cs, c1, c2, c3);
  return u32x4{c1 | (c2 << 16), c3 | (c1 << 16), c2 | (c1 << 16), 0u};
}
DI u32x4 aug_x(float mean, float sd, int hh) {
  unsigned a, b, c;
  split3(hh ? sd : -mean, a, b, c);
  u32x4 lo = {a | (a << 16), a | (b << 16), b | (c << 16), 0u};
  u32x4 hi = {a | (b << 16), c, 0u, 0u};
  return hh ? hi : lo;
}

DI float dot2(unsigned a, unsigned b, float c) {
  return __builtin_amdgcn_fdot2_f32_bf16(__builtin_bit_cast(bf16x2_t, a), __builtin_bit_cast(bf16x2_t, b), c, false);
}
DI void rsum(const u32x4& v, float& s1, float& s2) {
#pragma unroll
  for (int j = 0; j < 4; ++j) {
    s1 = dot2(v[j], 0x3f803f80u, s1);
    s2 = dot2(v[j], v[j], s2);
  }
}
DI float rjoin(float s1, float s2) {
  auto sw = __builtin_amdgcn_permlane32_swap(__float_as_uint(s1), __float_as_uint(s2), false, false);
  return __uint_as_float(sw[0]) + __uint_as_float(sw[1]);
}
DI float rstat(const u32x4& v) {
  float s1 = 0.f, s2 = 0.f;
  rsum(v, s1, s2);
  return __uint_as_float(min(__float_as_uint(rjoin(s1, s2)), SENT - 1));
}

DI const char* code_range(int& len) {
  const char* pc;
  asm volatile("s_getpc_b64 vcc\n\ts_add_u32 vcc_lo, vcc_lo, _ZN12_GLOBAL__N_16k_megaEPcPKtPt@rel32@lo+4\n\t"
               "s_addc_u32 vcc_hi, vcc_hi, _ZN12_GLOBAL__N_16k_megaEPcPKtPt@rel32@hi+12\n\ts_mov_b64 %1, vcc\n\t"
               "s_mov_b32 %0, .Lmega_tail-_ZN12_GLOBAL__N_16k_megaEPcPKtPt"
               : "=s"(len), "=s"(pc) :: "scc", "vcc");
  return pc;
}

template <int I> struct IC { static constexpr int v = I; };
template <int B, int E, class Fn>
DI void sfor(Fn&& f) {
  if constexpr (B < E) {
    f(IC<B>{});
    sfor<B + 1, E>(f);
  }
}

#define BARRIER() asm volatile("s_waitcnt lgkmcnt(0)\n\ts_barrier" ::: "memory")

typedef __attribute__((address_space(3))) s16x4* lds_s16x4p;
typedef const __attribute__((address_space(3))) u32x4* lds_u32x4p;
DI s16x4 tr_read(const char* p) { return __builtin_amdgcn_ds_read_tr16_b64_v4i16((lds_s16x4p)p); }
DI u32x4 join(s16x4 a, s16x4 b) {
  bf16x8 v = __builtin_shufflevector(a, b, 0, 1, 2, 3, 4, 5, 6, 7);
  return __builtin_bit_cast(u32x4, v);
}

DI u32x4 frag_from_image(const char* img, int row_stride, int s, int l, int cb) {
  const int i16 = l & 15, q4 = i16 >> 2, p4 = i16 & 3, blk = (l >> 4) & 1, hh = l >> 5;
  const char* p = img + (16 * s + 4 * hh + q4) * row_stride + cb + (4 * blk + p4) * 8;
  return join(tr_read(p), tr_read(p + 8 * row_stride));
}
#define P_CP 1
constexpr int P_W1 = 56, P_W2 = P_W1 + 56, P_X = P_W2 + 56, P_WO = P_X + 64, P_BLOCKS = P_WO + 16 + 8 * P_CP;
constexpr int P_RS = 1344 + 16, P_PHI = 16 * P_RS, P_LDS = 112 * 320;
DI float fsum(const u32x4& f, float c) {
#pragma unroll
  for (int j = 0; j < 4; ++j) c = dot2(f[j], 0x3f803f80u, c);
  return c;
}

__global__ __launch_bounds__(256) void k_prep(const unsigned short* __restrict__ x,
                                              const unsigned short* __restrict__ wqkv,
                                              const unsigned short* __restrict__ wphi,
                                              const unsigned short* __restrict__ wo,
                                              const unsigned short* __restrict__ w1,
                                              const unsigned short* __restrict__ b1,
                                              const unsigned short* __restrict__ w2,
                                              const unsigned short* __restrict__ b2,
                                              char* __restrict__ ws) {
  __shared__ __attribute__((aligned(16))) char img[P_LDS];
  const int b = blockIdx.x, tid = threadIdx.x, w = tid >> 6, l = tid & 63, r = l & 31, hh = l >> 5;
  STAMP_BEGIN();
  if (P_CP && b >= P_WO + 16) {
    int len;
    const char* pc = code_range(len);
    u32x4 t[16], bv;
    gload<0>(bv, min(tid, 55) * 16, b2);
#pragma unroll
    for (int i = 0; i < 16; ++i) gload<0>(t[i], min(i * 4096 + tid * 16, (len - 16) & ~15), pc);
    vwait<16>(bv);
    if (b == P_WO + 16 && tid < 56) st_wt<0>((u32x4*)(ws + OFF_B2) + tid, bv);
#pragma unroll
    for (int i = 0; i < 16; ++i) vwait<0>(t[i]);
    return;
  }
  const bool tq = b < P_W1, t1 = !tq && b < P_W2, t2 = b >= P_W2 && b < P_X, tx = b >= P_X && b < P_WO;
  const int j = tq ? b : t1 ? b - P_W1 : t2 ? b - P_W2 : tx ? b - P_X : b - P_WO;
  u32x4 v[7], p[4];
  unsigned bb = 0;
  {
    const char* src = (const char*)(tq ? wqkv : t1 ? w1 : t2 ? w2 : tx ? x : wo);
    src += tq ? (j >> 1) * (16 * 3 * D_ * 2) + (j & 1) * (8 * DH_ * 2)
              : t1 ? (j & 3) * (112 * F_ * 2) + (j >> 2) * 256 : j * (t2 || tx ? 32 * D_ * 2 : DH_ * D_ * 2);
    const unsigned ld = tq ? D_ * 2 : t1 ? F_ * 2 : 256, step = tq ? 8 * ld : 16 * ld;
    const unsigned lim = tq ? 0u : b >= P_WO ? DH_ * D_ * 2 - 16 : ~0u;
    const unsigned vo = (tid >> (tq ? 5 : 4)) * ld + min(tid & (tq ? 31 : 15), 27) * 16;
#pragma unroll
    for (int i = 0; i < 6; ++i) v[i] = *(const u32x4*)(src + i * step + vo);
    v[6] = *(const u32x4*)(src + min(6 * step + vo, lim));
    if (tq) {
#pragma unroll
      for (int i = 0; i < 4; ++i) p[i] = ((const u32x4*)(wphi + (j & 1) * 8 * DH_ * DH_))[min(tid + 256 * i, 783)];
    }
    if (t1) bb = b1[128 * (j >> 2) + 32 * w + r];
  }
  SCHED();
  {
    const u32x4 sv = {SENT, SENT, SENT, SENT};
#pragma unroll 1
    for (int fb = b; fb < 256; fb += (fb >= 240 ? 8 : 256)) {
      u32x4* e = (u32x4*)(ws + OFF_EX) + fb * 768 + tid;
      st_p<0>(e, sv); st_p<0>(e + 256, sv); st_p<0>(e + 512, sv);
      if (tid < 113) st_p<0>((u32x4*)(ws + OFF_XS + XS_BYTES) + fb * 113 + tid, sv);
    }
  }
  STAMP_NW(0,2);
  if (tq) {
    const int s = j >> 1, h0 = 8 * (j & 1);
#pragma unroll
    for (int i = 0; i < 6; ++i) {
      const int rp = (tid >> 5) + 8 * i, row = rp / 3, part = rp % 3;
      *(u32x4*)(img + row * P_RS + part * 448 + min(tid & 31, 27) * 16) = v[i];
    }
#pragma unroll
    for (int i = 0; i < 4; ++i) *(u32x4*)(img + P_PHI + min(tid + 256 * i, 783) * 16) = p[i];
    STAMP_NW(0,4); __syncthreads(); STAMP_NW(0,5);
    const int i16 = l & 15, q4 = i16 >> 2, cg = (4 * ((l >> 4) & 1) + (i16 & 3)) % 7;
    float cv[2];
    float* cq = (float*)(ws + OFF_CSQ) + (h0 + 2 * w) * 3072 + s * 32 + r;
#pragma unroll
    for (int i = 0; i < 2; ++i) {
      const int h = 2 * w + i;
      const char* ar = img + (r & 15) * P_RS + (r < 16 ? 448 : 0) + h * 56 + 8 * hh;
      const char* br = img + P_PHI + h * 1568 + (4 * hh + q4) * 56 + cg * 8;
      const u32x2 a0 = *(const u32x2*)ar, a1 = *(const u32x2*)(ar + 16), a2 = *(const u32x2*)(ar + 32);
      u32x2 a3 = *(const u32x2*)(ar + (hh ? 0 : 48));
      if (hh) a3 = u32x2{0u, 0u};
      const u32x4 b0 = join(tr_read(br), tr_read(br + 8 * 56));
      const u32x4 b1f = join(tr_read(br + 16 * 56), tr_read(br + (hh ? 0 : 24 * 56)));
      u32x4 vf = frag_from_image(img, P_RS, 0, l, 2 * 448 + h * 56);
      if (r >= DH_) vf = u32x4{0u, 0u, 0u, 0u};
      f32x16 acc = MFMA32(asfrag(u32x4{a0[0], a0[1], a1[0], a1[1]}), asfrag(b0), zero16());
      acc = MFMA32(asfrag(u32x4{a2[0], a2[1], a3[0], a3[1]}), asfrag(b1f), acc);
      u32x4* dst = (u32x4*)(ws + OFF_WQKV) + (size_t)((h0 + h) * WQF + s) * 64 + l;
      const u32x4 kf = pack8(acc, 0), qf = pack8(acc, 1);
      st_p(dst + KSD * 64, vf);
      st_p(dst, kf);
      st_p(dst + 2 * KSD * 64, qf);
      st_wt1(cq + i * 3072 + hh * 2048, rjoin(fsum(kf, 0.f), fsum(qf, 0.f)));
      cv[i] = fsum(vf, 0.f);
    }
    st_wt1(cq + hh * 3072 + 1024, rjoin(cv[0], cv[1]));
  } else if (t1) {
    const int rg = j & 3, ft = 4 * (j >> 2) + w;
#pragma unroll
    for (int i = 0; i < 7; ++i) *(u32x4*)(img + ((tid >> 4) + 16 * i) * 320 + (tid & 15) * 16) = v[i];
    STAMP_NW(0,4); __syncthreads(); STAMP_NW(0,5);
    u32x4* dst = (u32x4*)(ws + OFF_W1) + (size_t)(ft * KSA) * 64 + l;
    float cs = 0.f;
#pragma unroll
    for (int i = 0; i < 7; ++i) {
      const u32x4 f = frag_from_image(img, 320, i, l, 64 * w);
      st_p(dst + (7 * rg + i) * 64, f);
      cs = fsum(f, cs);
    }
    cs = sum32(cs);
    const unsigned hi = rg == 0 ? bb | (bb << 16) : rg == 1 ? bb : 0u;
    st_wt1((float*)(dst + KSD * 64) + rg, hh ? __uint_as_float(hi) : cs);
  } else if (t2) {
#pragma unroll
    for (int i = 0; i < 7; ++i) {
      const int idx = tid + 256 * i;
      *(u32x4*)(img + (idx / 56) * 912 + (idx % 56) * 16) = v[i];
    }
    STAMP_NW(0,4); __syncthreads(); STAMP_NW(0,5);
#pragma unroll
    for (int i = 0; i < 7; ++i) {
      const int ks = w >> 1, ct = w * 7 + i - 14 * ks;
      st_p((u32x4*)(ws + OFF_W2) + (size_t)(ct * KSF + 2 * j + ks) * 64 + l, frag_from_image(img, 912, ks, l, 64 * ct));
    }
  } else if (tx) {
#pragma unroll
    for (int i = 0; i < 7; ++i) {
      const int idx = tid + 256 * i, row = idx / 56, c = idx % 56;
      *(u32x4*)(img + row * 912 + c * 16) = v[i];
    }
    STAMP_NW(0,4); __syncthreads(); STAMP_NW(0,5);
    u32x4* dst = (u32x4*)(ws + OFF_XF) + (size_t)j * KSD * 64;
#pragma unroll
    for (int i = 0; i < 7; ++i) {
      const int s = w * 7 + i;
      const char* q = img + r * 912 + 32 * s + 8 * hh;
      u32x2 lo = *(const u32x2*)q;
      u32x2 hi = *(const u32x2*)(q + 16);
      const u32x4 fr = nosent(u32x4{lo[0], lo[1], hi[0], hi[1]});
      st_p(dst + s * 64 + l, fr);
      st_wt1((float*)(ws + OFF_XS) + (j * KSD + s) * 64 + l, rstat(fr));
    }
  } else {
    if (tid >= 32) v[6] = u32x4{0u, 0u, 0u, 0u};
#pragma unroll
    for (int i = 0; i < 7; ++i) *(u32x4*)(img + (tid + 256 * i) * 16) = v[i];
    STAMP_NW(0,4); __syncthreads(); STAMP_NW(0,5);
    u32x4* dst = (u32x4*)(ws + OFF_WO) + (size_t)j * 28 * 64;
#pragma unroll
    for (int i = 0; i < 7; ++i) {
      const int fi = w * 7 + i, ks = fi / NNT, nt = fi % NNT;
      st_p(dst + fi * 64 + l, frag_from_image(img, 896, ks, l, 64 * nt));
    }
  }
  STAMP_END(0, 2);
}

DI void finish_stats(f32x2 v, float& mean, float& rstd, float& sd) {
  mean = v[0] * (1.f / D_);
  const float var = fmaxf(v[1] * (1.f / D_) - mean * mean, 0.f) + 1e-5f;
  rstd = rsqrtf(var);
  sd = var * rstd;
}

constexpr float LOG2E = 1.4426950408889634f;
DI float max3(float a, float b, float c) { return fmaxf(fmaxf(a, b), c); }
DI float min3(float a, float b, float c) { return fminf(fminf(a, b), c); }

struct Smx {
  f32x16 e[2];
  float mx, mn, b[2], s[2], inv[2];
  u32x2 pk[2][4];
};
DI void smx_pre(Smx& z, const f32x16& y, float rs2) {
  z.mx = fmaxf(max3(y[0], y[1], y[2]), y[3]);
  z.mn = fminf(min3(y[0], y[1], y[2]), y[3]);
#pragma unroll
  for (int q = 1; q < 4; ++q) {
    z.mx = max3(max3(z.mx, y[4 * q], y[4 * q + 1]), y[4 * q + 2], y[4 * q + 3]);
    z.mn = min3(min3(z.mn, y[4 * q], y[4 * q + 1]), y[4 * q + 2], y[4 * q + 3]);
  }
  z.b[0] = -max32(z.mx) * rs2;
  z.b[1] = min32(z.mn) * rs2;
}
constexpr int SMX_SLOTS = 25;
template <int N, int Q>
DI void smx_slot(Smx& z, const f32x16& y, float rs2, float scale, int hh) {
  f32x16& e = z.e[N];
  if constexpr (Q < 4) {
#pragma unroll
    for (int i = 4 * Q; i < 4 * Q + 4; ++i) e[i] = opq(__builtin_fmaf(y[i], N ? -rs2 : rs2, z.b[N]));
  } else if constexpr (Q < 12) {
    e[2 * Q - 8] = __builtin_amdgcn_exp2f(e[2 * Q - 8]);
    e[2 * Q - 7] = __builtin_amdgcn_exp2f(e[2 * Q - 7]);
  } else if constexpr (Q < 16) {
    constexpr int g = Q - 12;
    float a = opq(opq(e[4 * g] + e[4 * g + 1]) + opq(e[4 * g + 2] + e[4 * g + 3]));
    if constexpr (g == 3) a = hh ? 0.f : a;
    if constexpr (g == 0) z.s[N] = a;
    else z.s[N] = opq(z.s[N] + a);
  } else if constexpr (Q == 16) {
    z.inv[N] = __builtin_amdgcn_rcpf(sum32(z.s[N])) * scale;
  } else {
    constexpr int g = (Q - 17) >> 1;
    if constexpr ((Q - 17) & 1) {
      z.pk[N][g] = u32x2{pack2(e[4 * g], e[4 * g + 1]), pack2(e[4 * g + 2], e[4 * g + 3])};
    } else {
      const float f = (g == 3 && hh) ? 0.f : z.inv[N];
#pragma unroll
      for (int i = 4 * g; i < 4 * g + 4; ++i) e[i] = opq(e[i] * f);
    }
  }
}

#ifndef M_D1
#define M_D1 20
#endif
#ifndef M_D2
#define M_D2 21
#endif
#ifndef M_KVP
#define M_KVP 0
#endif
#ifndef M_RK
#define M_RK 3
#endif
#ifndef M_CSV
#define M_CSV 0
#endif
#ifndef M_SS
#define M_SS 8
#endif
#ifndef M_RS
#define M_RS 4
#endif
#ifndef M_D3
#define M_D3 30
#endif
#ifndef M_D3S
#define M_D3S 7
#endif
#ifndef M_D4
#define M_D4 7
#endif
#ifndef M_D5
#define M_D5 25
#endif
#ifndef M_A0
#define M_A0 8
#endif
#ifndef M_A1
#define M_A1 11
#endif
#ifndef M_A2
#define M_A2 17
#endif
#ifndef M_HW1
#define M_HW1 12
#endif
#ifndef M_NP
#define M_NP 2
#endif
#ifndef M_MID
#define M_MID 4
#endif
#ifndef M_HQ
#define M_HQ 16
#endif
#ifndef M_FE
#define M_FE 0
#endif
#ifndef M_HS
#define M_HS 2
#endif
#ifndef M_HSL
#define M_HSL 0
#endif
constexpr int NFILL = 6 + M_HS;
#ifndef M_NVS
#define M_NVS 4
#endif
#ifndef M_QF
#define M_QF 2
#endif
#ifndef M_D6
#define M_D6 4
#endif
#ifndef M_W1E
#define M_W1E 0
#endif
#ifndef M_F2
#define M_F2 0
#endif
#ifndef M_SE
#define M_SE 0
#endif
#ifndef M_HP
#define M_HP 0
#endif
#ifndef M_AO
#define M_AO 1
#endif
#ifndef M_W1D
#define M_W1D 8
#endif
#ifndef M_AE
#define M_AE 0
#endif
#ifndef M_AES
#define M_AES 0
#endif
#ifndef M_PD
#define M_PD 3
#endif
#ifndef M_B2E
#define M_B2E 1
#endif
#ifndef M_SR
#define M_SR 7
#endif
#ifndef M_CPE
#define M_CPE 0
#endif
#ifndef M_OT
#define M_OT 0
#endif
#ifndef M_B2W
#define M_B2W 1
#endif
#ifndef M_D4H
#define M_D4H 9
#endif
constexpr int M_W = 0, M_R = WQF * 1024, M_ST = M_R + 64 * 1024, M_KV = M_ST + 4096, M_FL = M_KV + 4096, M_LDS = M_FL + 64;
constexpr int SPIN_MAX = 1 << 12, RETRY_MAX = 64;
constexpr int NW2G = 18;
#ifndef M_W2T
#define M_W2T {101, 103, 105, 107, 109, 111, 113, 115, 117, 119, 121, 123, 125, 127, 204, 210, 216, 222}
#endif
constexpr int W2T[NW2G] = M_W2T;
constexpr int w2_early() {
  int n = 0;
  for (int i = 0; i < NW2G; ++i) n += W2T[i] < 0;
  return n;
}
constexpr int W2E = w2_early();
constexpr int w2_after() {
  int last = 0;
  for (int i = 0; i < NW2G; ++i) last = W2T[i] > last ? W2T[i] : last;
  return (last < 125) + (last < 127);
}
DI unsigned umax3(unsigned a, unsigned b, unsigned c) { return max(max(a, b), c); }
DI unsigned smax(unsigned m, const u32x4& d) { return umax3(umax3(m, d[0], d[1]), d[2], d[3]); }
DI void csum(const u32x4& f, f32x4& c) {
#pragma unroll
  for (int j = 0; j < 4; ++j) c[j] = dot2(f[j], 0x3f803f80u, c[j]);
}
DI void pin4(u32x4& d) { asm volatile("" : "+v"(d)); }
DI void def4(u32x4& d) { asm volatile("" : "=v"(d)); }
DI bool has_sent(unsigned m) { return __builtin_amdgcn_ballot_w64(m == SENT) != 0; }
DI void probe_wait(const char* base, unsigned off) {
#pragma unroll 1
  for (int k = 0; k < SPIN_MAX; ++k) {
    __builtin_amdgcn_s_sleep(M_RS);
    unsigned pb;
    gload1_c<0>(pb, off, base);
    vwait1<0>(pb);
    if (!has_sent(pb)) break;
  }
}
constexpr int hq(int i) { return M_HQ + i < 27 ? M_HQ + i : 27; }

template <bool spare>
DI void mega(char* smem, char* __restrict__ ws_, const unsigned short* __restrict__ b2, unsigned short* __restrict__ out) {
#ifdef M_CPF
  constexpr int NPF = spare ? 3 : 0;
#else
  constexpr int NPF = 0;
#endif
  constexpr int NW1 = spare ? 0 : KSA;
  MS_BEGIN();
#pragma unroll 1
  for (int layer = 0; layer < 2; ++layer) {
    char* ws = ws_;
    int bid = blockIdx.x, tid = threadIdx.x;
    asm volatile("" : "+s"(ws), "+s"(bid), "+v"(tid));
    const int xcd = bid & 7, bi = bid >> 3;
    const int g = 2 * xcd + (bi >> 4), h = bi & 15;
    const int tp = 2 * g + (h >= 7), ns = spare ? h - 14 : h >= 7 ? h - 7 : h;
    const int w8 = __builtin_amdgcn_readfirstlane(tid >> 6), w = w8 & 3, l = tid & 63;
    const int r = l & 31, hh = l >> 5;
    const unsigned l16 = l * 16;
    const bool lag = w8 < 4;
    const int a8 = w8 >> 2;
    const int so = 4 * ns + w;
    const unsigned sm = lds_addr(smem);
    const char* wb = ws + OFF_WQKV + (size_t)h * (WQF * 1024);
    const int nsw = ns;
    const char* w1g = ws + OFF_W1 + (size_t)(8 * nsw + w8) * (KSA * 1024);
    int sg[7];
    {
      const int rot = h >= 14 ? h - 14 : h >= 7 ? h - 7 : h;
#pragma unroll
      for (int j = 0; j < 7; ++j) sg[j] = (rot + j >= 7 ? rot + j - 7 : rot + j) * 4096;
    }
    auto wdma_kv = [&]() {
#pragma unroll
      for (int j = 0; j < 7; ++j) {
        const int o = ((w >> 1) * KSD + 2 * (w & 1)) * 1024 + sg[j];
        glds<2>(l16, wb + o, sm + M_W + o);
      }
    };
    auto wdma_q = [&]() {
      const char* s = wb + (2 * KSD + 7 * w) * 1024;
      const unsigned d = sm + M_W + (2 * KSD + 7 * w) * 1024;
      glds<4>(l16, s, d);
      glds<3>(l16, s + 4096, d + 4096);
    };
    char* ex = ws + OFF_EX + (size_t)layer * EX_BYTES;
    u32x4 wr[KSA];
    auto w1load = [&](auto I_) {
      constexpr int i = decltype(I_)::v;
      if constexpr (!spare) gload<(i & 7) * 1024 - 4096>(wr[i], l16, w1g + (i >> 3) * 8192 + 4096);
    };
    auto w1rl = [&](auto I_) {
      constexpr int i = decltype(I_)::v;
      if constexpr (!spare) gload_r<(i & 7) * 1024 - 4096>(wr[i], l16, w1g + (i >> 3) * 8192 + 4096);
    };
    f32x16 c0, c1;
    u32x4 xres;
    u32x2 bs[2];
    unsigned otouch;
    auto bload = [&]() {
      if constexpr (!spare)
        asm volatile("global_load_dwordx2 %0, %2, %3\n\tglobal_load_dwordx2 %1, %2, %3 offset:16"
                     : "=&v"(bs[0]), "=&v"(bs[1]) : "v"(8 * hh), "s"(M_B2W ? ws + OFF_B2 + 32 * so : (const char*)(b2 + 16 * so)));
      if constexpr (!spare && M_OT) gload1(otouch, r * (D_ * 2), out + (size_t)(2 * tp + a8) * 32 * D_ + 16 * so);
    };
    auto c1head = [&](auto L_) {
      constexpr bool lg = decltype(L_)::v;
      const char* og = ex + (size_t)((2 * tp + a8) * H_ + 4 * w) * 2048 + 4096;
      const u32x4* wl = (const u32x4*)(smem + M_W) + w * 1024 + l;
      u32x4 of[8];
      auto oissue = [&]() { sfor<0, 8>([&](auto I_) { gload_c<decltype(I_)::v * 1024 - 4096>(of[decltype(I_)::v], l16, og); }); };
      auto otake = [&](auto N_) {
        constexpr int n = decltype(N_)::v;
        unsigned m = 0;
        u32x4 wf[8][2];
        auto wrd = [&](int i) {
          wf[i][0] = wl[i * 128];
          wf[i][1] = wl[i * 128 + 64];
        };
        wrd(0);
        wrd(1);
        c0 = zero16();
        c1 = zero16();
        sfor<0, 8>([&](auto I_) {
          constexpr int i = decltype(I_)::v;
          if constexpr (i + 2 < 8) wrd(i + 2);
          vwait<n + 7 - i>(of[i]);
          c0 = MFMA32(asfrag(wf[i][0]), asfrag(of[i]), c0);
          c1 = MFMA32(asfrag(wf[i][1]), asfrag(of[i]), c1);
          PINV(c0); PINV(c1);
          m = smax(m, of[i]);
          SCHED();
        });
        return has_sent(m);
      };
      if constexpr (!M_B2E) bload();
      gload_c<0>(xres, l16, ws + OFF_XF + (size_t)(2 * layer) * XF_BYTES + (size_t)((2 * tp + a8) * KSD + so) * 1024);
      if constexpr (lg) sfor<M_AE, M_A0>(w1load);
      __builtin_amdgcn_s_sleep(lg ? M_D4 : M_D4H);
      oissue();
      if constexpr (lg) sfor<M_A0, M_A1>(w1load);
      vwait_mem<lg ? 8 + M_A1 - M_AE : 8>();
      asm volatile("s_barrier" ::: "memory");
      if (__builtin_expect(otake(IC<lg ? M_A1 - M_A0 : 0>{}), 0)) {
#if M_SR & 1
#pragma unroll 1
        for (int it = 0; it < SPIN_MAX; ++it) {
          unsigned m = 0;
          sfor<0, 8>([&](auto I_) { vwait<0>(of[decltype(I_)::v]); m = smax(m, of[decltype(I_)::v]); });
          if (!has_sent(m)) break;
          MDBG(3);
          if (it >= 8) __builtin_amdgcn_s_sleep(32);
          sfor<0, 8>([&](auto I_) {
            constexpr int i = decltype(I_)::v;
            if (has_sent(smax(0u, of[i]))) greload_c<i * 1024 - 4096>(of[i], l16, og);
          });
        }
        sfor<0, 8>([&](auto I_) { vwait<0>(of[decltype(I_)::v]); });
        otake(IC<0>{});
#else
        for (int it = 0; it < RETRY_MAX; ++it) {
          MDBG(3);
          probe_wait(og - 4096, (l & 7) * 1024 + 1020);
          oissue();
          if (!otake(IC<0>{})) break;
        }
#endif
      }
    };
    volatile __attribute__((address_space(3))) unsigned* flg = (volatile __attribute__((address_space(3))) unsigned*)(sm + M_FL);
    auto flag_wait = [&]() {
#pragma unroll 1
      for (int k = 0; k < (1 << 20); ++k) {
        const unsigned f = flg[l & 3];
        if (__builtin_amdgcn_ballot_w64(f != (unsigned)layer + 1) == 0) break;
        if (!lag) __builtin_amdgcn_s_sleep(2);
      }
      asm volatile("" ::: "memory");
    };
    char* kp = ex + OF_BYTES + (size_t)(h * 4 + w) * 16384;
    MS(0);
    if (lag) {
      const int tb = 4 * g + w;
      const char* xb = ws + OFF_XF + (size_t)(2 * layer) * XF_BYTES + (size_t)tb * (KSD * 1024);
      u32x4 xr[KSD];
      f32x16 ak, av, aq;
      unsigned wa[7];
#pragma unroll
      for (int j = 0; j < 7; ++j) wa[j] = sm + M_W + l16 + sg[j];
      auto wf = [&](auto S_c, int part) {
        constexpr int s = decltype(S_c)::v;
        return ((lds_u32x4p)wa[s >> 2])[(part * KSD + (s & 3)) * 64];
      };
      auto xseg = [&](auto J_) {
        constexpr int j = decltype(J_)::v;
        return has_sent(max(smax(smax(0u, xr[4 * j]), xr[4 * j + 1]), smax(smax(0u, xr[4 * j + 2]), xr[4 * j + 3])));
      };
      if (layer) __builtin_amdgcn_s_sleep(M_D1);
      sfor<0, KSD>([&](auto S_c) {
        constexpr int s = decltype(S_c)::v;
        gload_c<(s & 3) * 1024>(xr[s], l16, xb + sg[s >> 2]);
      });
#if M_AO
      auto lvl = [&](unsigned n) {
#pragma unroll 1
        for (int k = 0; k < (1 << 20); ++k) {
          const unsigned f = flg[8 + (l & 3)];
          if (__builtin_amdgcn_ballot_w64(f < n) == 0) break;
          __builtin_amdgcn_s_sleep(1);
        }
        asm volatile("" ::: "memory");
      };
      asm volatile("s_barrier" ::: "memory");
      if (!layer) lvl(1);
#else
      asm volatile("s_barrier" ::: "memory");
      if (layer) asm volatile("s_barrier" ::: "memory");
#endif
      constexpr int NV = 4 * M_NVS;
      {
        constexpr int PK = 4, PV = 2;
        u32x4 fk[KSD], fv[KSD];
        sfor<0, PK>([&](auto S_c) {
          fk[decltype(S_c)::v] = wf(S_c, 0);
          if constexpr (decltype(S_c)::v < PV) fv[decltype(S_c)::v] = wf(S_c, 1);
        });
        bool bad;
        auto check = [&](auto J_) {
          constexpr int j = decltype(J_)::v;
          sfor<4 * j, 4 * j + 4>([&](auto S_c) { vwait<24 - 4 * j>(xr[decltype(S_c)::v]); });
          bad = xseg(J_);
        };
        check(IC<0>{});
        MS(1);
        ak = zero16();
        av = zero16();
        sfor<0, KSD>([&](auto S_c) {
          constexpr int s = decltype(S_c)::v, j = s >> 2;
          if constexpr ((s & 3) == 0) {
            if (__builtin_expect(bad, 0)) {
#pragma unroll 1
              for (int it = 0; it < (M_SR & 4 ? SPIN_MAX : RETRY_MAX); ++it) {
                sfor<4 * j, KSD>([&](auto T_) { vwait<0>(xr[decltype(T_)::v]); });
                if (!xseg(IC<j>{})) break;
                MDBG(0);
                if constexpr (M_SR & 4) { if (it >= 8) __builtin_amdgcn_s_sleep(32); }
                else probe_wait(xb, min(l, KSD - 1) * 1024 + 1020);
                sfor<j, 7>([&](auto V_) {
                  constexpr int v = decltype(V_)::v;
                  if (xseg(V_))
                    sfor<4 * v, 4 * v + 4>([&](auto T_) { greload_c<(decltype(T_)::v & 3) * 1024>(xr[decltype(T_)::v], l16, xb + sg[v]); });
                });
              }
              sfor<4 * j, KSD>([&](auto T_) { vwait<0>(xr[decltype(T_)::v]); });
            }
          }
#if M_AO
          if constexpr (s + PK == 16) { if (!layer) lvl(2); }
#else
          if constexpr (s + PK == 16) { if (!layer) asm volatile("s_barrier" ::: "memory"); }
#endif
          if constexpr (s + PK < KSD) fk[s + PK] = wf(IC<s + PK>{}, 0);
          if constexpr (s + PV < NV) fv[s + PV] = wf(IC<s + PV>{}, 1);
          ak = MFMA32(asfrag(fk[s]), asfrag(xr[s]), ak);
          PINV(ak);
          SCHED();
          if constexpr ((s & 3) == (s < NV ? 3 : 2) && j < 6) { check(IC<j + 1>{}); SCHED(); }
          if constexpr (s < NV) {
            av = MFMA32(asfrag(xr[s]), asfrag(fv[s]), av);
            PINV(av);
            SCHED();
          }
        });
      }
      MS(2);
      float rstd, rs2;
      u32x4 xa;
      constexpr int PD = 4, NR = KSD - NV, NT = NR + 1 + KSA, T1 = 27;
      u32x4 f[NT];
      unsigned qoff = sm + M_W + 3 * KSD * 1024 + l16;
      asm volatile("" : "+v"(qoff));
      const lds_u32x4p wal = (lds_u32x4p)qoff;
      auto fld = [&](auto T_) {
        constexpr int t = decltype(T_)::v;
        if constexpr (t < NR) f[t] = wf(IC<NV + t>{}, 1);
        else if constexpr (t == NR) f[t] = wal[64];
        else if constexpr (t < NT - 1) f[t] = wf(IC<t - NR - 1>{}, 2);
        else if constexpr (t == NT - 1) f[t] = wal[128];
      };
      auto fmm = [&](auto T_) {
        constexpr int t = decltype(T_)::v;
        if constexpr (t < NR) { av = MFMA32(asfrag(xr[NV + t]), asfrag(f[t]), av); PINV(av); }
        else if constexpr (t == NR) { av = MFMA32(asfrag(xa), asfrag(f[t]), av); PINV(av); }
        else if constexpr (t == NR + 1) { aq = MFMA32(asfrag(f[t]), asfrag(xr[0]), zero16()); PINV(aq); }
        else if constexpr (t < NT - 1) { aq = MFMA32(asfrag(f[t]), asfrag(xr[t - NR - 1]), aq); PINV(aq); }
        else { aq = MFMA32(asfrag(f[t]), asfrag(xa), aq); PINV(aq); }
      };
#if M_F2
#pragma unroll 1
      for (int k = 0; k < (1 << 20); ++k) {
        const unsigned f = flg[4 + (l & 3)];
        if (__builtin_amdgcn_ballot_w64(f != (unsigned)layer + 1) == 0) break;
      }
      asm volatile("" ::: "memory");
#else
      asm volatile("s_barrier" ::: "memory");
#endif
      {
        const u32x4 agk = wal[0];
        xa = ((const u32x4*)(smem + M_KV))[w * 64 + l];
        rstd = ((const float*)(smem + M_ST))[w * 64 + l];
        sfor<0, PD>(fld);
        rs2 = rstd * LOG2E;
        ak = MFMA32(asfrag(agk), asfrag(xa), ak);
        PINV(ak);
      }
      SCHED();
      {
        Smx z;
        u32x4 vfr[2], pa[4];
        char* img = smem + M_R + 32 * 1024 + w * 4096;
        const int i16 = l & 15, q4 = i16 >> 2, p4 = i16 & 3, blk = (l >> 4) & 1;
        f32x4* red = (f32x4*)(smem + M_R) + w * 128 + l;
        auto slot = [&](auto N_, auto Q_) {
          constexpr int n = decltype(N_)::v, q = decltype(Q_)::v;
          smx_slot<n, q>(z, ak, rs2, rstd, hh);
          if constexpr (q >= 17 && ((q - 17) & 1)) {
            constexpr int gg = (q - 17) >> 1;
            const int ch = (2 * gg + hh) ^ ((r >> 1) & 7);
            *(u32x2*)(img + n * 2048 + r * 64 + ch * 8) = z.pk[n][gg];
          }
        };
        fmm(IC<0>{});
        smx_pre(z, ak, rs2);
        SCHED();
        sfor<1, T1 + 1>([&](auto T_) {
          constexpr int t = decltype(T_)::v;
          fld(IC<t + PD - 1>{});
          if constexpr (t - 1 < SMX_SLOTS) { slot(IC<0>{}, IC<t - 1>{}); slot(IC<1>{}, IC<t - 1>{}); }
          if constexpr (t == 26) {
#pragma unroll
            for (int i = 0; i < 4; ++i) {
              const int t0 = 16 * (i & 1) + 4 * hh + q4, t1 = t0 + 8;
              const int c0 = (4 * blk + p4) ^ ((t0 >> 1) & 7), c1 = (4 * blk + p4) ^ ((t1 >> 1) & 7);
              pa[i] = join(tr_read(img + (i >> 1) * 2048 + t0 * 64 + c0 * 8), tr_read(img + (i >> 1) * 2048 + t1 * 64 + c1 * 8));
            }
            vfr[0] = pack8(av, 0);
            vfr[1] = pack8(av, 1);
          }
          fmm(T_);
          SCHED();
        });
        ak = MFMA32(asfrag(pa[0]), asfrag(vfr[0]), zero16());
        av = MFMA32(asfrag(pa[2]), asfrag(vfr[0]), zero16());
        ak = MFMA32(asfrag(pa[1]), asfrag(vfr[1]), ak);
        av = MFMA32(asfrag(pa[3]), asfrag(vfr[1]), av);
        PINV(ak); PINV(av);
        fld(IC<T1 + PD>{});
        fmm(IC<T1 + 1>{});
        SCHED();
#pragma unroll
        for (int gg = 0; gg < 4; ++gg) {
          red[gg * 512] = f32x4{ak[4 * gg], ak[4 * gg + 1], ak[4 * gg + 2], ak[4 * gg + 3]};
          red[gg * 512 + 64] = f32x4{av[4 * gg], av[4 * gg + 1], av[4 * gg + 2], av[4 * gg + 3]};
        }
        asm volatile("" ::: "memory");
        flg[w] = layer + 1;
        SCHED();
        sfor<T1 + 2, T1 + 2 + M_QF>([&](auto T_) { fld(IC<decltype(T_)::v + PD - 1>{}); fmm(T_); SCHED(); });
        flag_wait();
        const f32x4* redr = (const f32x4*)(smem + M_R) + (w & 1) * 1024 + (w >> 1) * 64 + l;
        f32x4 pv[8];
#pragma unroll
        for (int i = 0; i < 8; ++i) pv[i] = redr[i * 128];
        SCHED();
        sfor<T1 + 2 + M_QF, T1 + 4 + M_QF>([&](auto T_) { fld(IC<decltype(T_)::v + PD - 1>{}); fmm(T_); SCHED(); });
        const f32x4 s0 = (pv[0] + pv[1]) + (pv[2] + pv[3]), s1 = (pv[4] + pv[5]) + (pv[6] + pv[7]);
        st_wt<0>((u32x4*)(kp + g * 1024) + l,
                 nosent(u32x4{pack2(s0[0], s0[1]), pack2(s0[2], s0[3]), pack2(s1[0], s1[1]), pack2(s1[2], s1[3])}));
        SCHED();
        MS(3);
        sfor<T1 + 4 + M_QF, NT>([&](auto T_) { fld(IC<decltype(T_)::v + PD - 1>{}); fmm(T_); SCHED(); });
      }
      u32x4 pq[4];
      {
        Smx z;
        smx_pre(z, aq, rs2);
        sfor<0, SMX_SLOTS>([&](auto Q_) {
          constexpr int q = decltype(Q_)::v;
          smx_slot<0, q>(z, aq, rs2, 1.f, hh);
          smx_slot<1, q>(z, aq, rs2, 1.f, hh);
        });
#pragma unroll
        for (int i = 0; i < 4; ++i)
          pq[i] = u32x4{z.pk[i >> 1][2 * (i & 1)][0], z.pk[i >> 1][2 * (i & 1)][1], z.pk[i >> 1][2 * (i & 1) + 1][0], z.pk[i >> 1][2 * (i & 1) + 1][1]};
      }
      if constexpr (!spare) {
        sfor<0, 4>([&](auto I_) { pin4(pq[decltype(I_)::v]); });
        if constexpr (M_B2E) bload();
#pragma unroll
        for (int q = 0; q < 7; ++q)
          glds<2>(l16, ws + OFF_WO + (size_t)((7 * w + q) * NNT + 2 * nsw) * 1024, sm + M_W + (7 * w + q) * 2048);
        sfor<0, M_AE>([&](auto I_) { if constexpr (M_AES) __builtin_amdgcn_s_sleep(M_AES); w1load(I_); });
      }
      MS(4);
      {
        asm volatile("s_barrier" ::: "memory");
        MS(5);
        const u32x4* kl = (const u32x4*)(smem + M_KV) + l;
        f32x16 o = zero16();
#pragma unroll
        for (int k = 0; k < 4; ++k) o = MFMA32(asfrag(kl[k * 64]), asfrag(pq[k]), o);
        float dlo, dhi;
        halves(o[12] + o[13], dlo, dhi);
        const float inv = __builtin_amdgcn_rcpf(dhi + 1e-6f);
#pragma unroll
        for (int i = 0; i < 16; ++i) o[i] *= inv;
        const u32x4 o0 = nosent(pack8(o, 0)), o1 = nosent(pack8(o, 1));
        u32x4* od = (u32x4*)ex + (size_t)(tb * H_ + h) * 128 + l;
        st_wt<0>(od, o0);
        st_wt<1024>(od, o1);
      }
      asm volatile("s_barrier" ::: "memory");
      MS(6);
      if constexpr (!spare) {
        c1head(IC<1>{});
        sfor<M_A1, KSA>([&](auto I_) { def4(wr[decltype(I_)::v]); });
      }
    } else {
      const char* xs = ws + OFF_XS + (size_t)layer * XS_BYTES + (size_t)(4 * g + w) * 7168 + 4096;
      u32x4 st[7];
      auto fills = [&]() {
        const u32x4 sv = {SENT, SENT, SENT, SENT};
        const int idx = h * 4 + w;
        u32x4* d = (u32x4*)(ws + OFF_HF) + (size_t)(4 * g * KSF + idx * M_HS) * 64 + l;
        sfor<0, M_HS>([&](auto I_) { st_wt<(decltype(I_)::v & 3) * 1024>(d + (decltype(I_)::v >> 2) * 256, sv); });
        d = (u32x4*)(ws + OFF_XF + XF_BYTES) + (size_t)(4 * g * KSD + min(idx, 55) * 2) * 64 + l;
        st_wt<0>(d, sv); st_wt<1024>(d, sv);
        st_wt<0>(d + XF_BYTES / 16, sv); st_wt<1024>(d + XF_BYTES / 16, sv);
        d = (u32x4*)(ex + EX_BYTES) + (size_t)(4 * g * H_ + idx) * 128 + l;
        st_wt<0>(d, sv); st_wt<1024>(d, sv);
      };
      if constexpr (!spare) sfor<0, KSA>([&](auto I_) { def4(wr[decltype(I_)::v]); });
#if M_AO
      if (layer == 0) {
        wdma_kv();
        flg[w] = 0;
        flg[8 + w] = 0;
        sfor<0, 7>([&](auto I_) { gload_c<decltype(I_)::v * 1024 - 4096>(st[decltype(I_)::v], l16, xs); });
        const int part = min(w, 2);
        u32x4 cp[4];
        sfor<0, 4>([&](auto I_) { gload<decltype(I_)::v * 1024>(cp[decltype(I_)::v], l16, ws + OFF_CSQ + (h * 3 + part) * 4096); });
        wdma_q();
        const u32x4 sv = {SENT, SENT, SENT, SENT};
        st_wt<0>((u32x4*)(kp + EX_BYTES) + g * 64 + l, sv);
        if constexpr (M_CPE) {
          int len;
          const char* pc = code_range(len);
          glds<1>(l16, pc + min(((bi * 4 + w) & 63) * 1024, (len - 1024) & ~255), sm + M_R + 56 * 1024);
        }
        BARRIER();
        vwait_mem<25 + M_CPE>();
        flg[8 + w] = 1;
        vwait_mem<19 + M_CPE>();
        flg[8 + w] = 2;
        vwait_mem<8 + M_CPE>();
#else
      if (layer == 0) {
        sfor<0, 7>([&](auto I_) { gload_c<decltype(I_)::v * 1024 - 4096>(st[decltype(I_)::v], l16, xs); });
        flg[w] = 0;
        flg[4 + w] = 0;
        const u32x4 sv = {SENT, SENT, SENT, SENT};
        st_wt<0>((u32x4*)(kp + EX_BYTES) + g * 64 + l, sv);
#ifdef M_OLDP
        if (w == 0) glds<3>(l16, wb + 3 * KSD * 1024, sm + M_W + 3 * KSD * 1024);
#else
        const int part = min(w, 2);
        u32x4 cp[4];
        sfor<0, 4>([&](auto I_) { gload<decltype(I_)::v * 1024>(cp[decltype(I_)::v], l16, ws + OFF_CSQ + (h * 3 + part) * 4096); });
#endif
        wdma_kv();
        wdma_q();
        if constexpr (NPF > 0) {
          const char* pc;
          asm volatile("s_getpc_b64 vcc\n\ts_add_u32 vcc_lo, vcc_lo, _ZN12_GLOBAL__N_16k_megaEPcPKtPt@rel32@lo+4\n\t"
                       "s_addc_u32 vcc_hi, vcc_hi, _ZN12_GLOBAL__N_16k_megaEPcPKtPt@rel32@hi+12\n\ts_mov_b64 %0, vcc" : "=s"(pc) :: "scc", "vcc");
          pc += (((bi >> 4) * 2 + ns) * 4 + w) * 3072;
          glds<3>(l16, pc, sm + M_R + 56 * 1024);
        }
#if M_FE == 1
        fills();
#endif
        vwait_mem<13 + NPF + (M_FE == 1 ? NFILL : 0)>();
        BARRIER();
#endif
#ifndef M_OLDP
        {
          sfor<0, 4>([&](auto I_) { pin4(cp[decltype(I_)::v]); });
          f32x4 t = __builtin_bit_cast(f32x4, cp[3]);
          if (hh) t = f32x4{0.f, 0.f, 0.f, 0.f};
          t += (__builtin_bit_cast(f32x4, cp[0]) + __builtin_bit_cast(f32x4, cp[1])) + __builtin_bit_cast(f32x4, cp[2]);
          float* tr = (float*)(smem + M_R + 52 * 1024) + w * 256;
          ((f32x4*)tr)[l] = t;
          const float a = sum32((tr[l] + tr[64 + l]) + (tr[128 + l] + tr[192 + l]));
          const unsigned hi = part != 1 ? 0u : r == DH_ ? 0x3f80u : r == DH_ + 1 ? 0x3f800000u : 0u;
          ((u32x4*)(smem + M_W))[(3 * KSD + part) * 64 + l] = hh ? u32x4{hi, 0u, 0u, 0u} : aug_w(a);
        }
#endif
        if constexpr (M_W1E & 1) sfor<0, KSA>(w1rl);
#if M_FE == 2
        fills();
#endif
      } else {
        if constexpr (!spare) wdma_q();
#if M_SE
        __builtin_amdgcn_s_sleep(M_SE);
        sfor<0, 7>([&](auto I_) { gload_c<decltype(I_)::v * 1024 - 4096>(st[decltype(I_)::v], l16, xs); });
        vwait_mem<spare ? 14 : 16>();
        asm volatile("s_barrier\n\ts_barrier" ::: "memory");
#else
        vwait_mem<7>();
        if (M_AO) asm volatile("s_barrier" ::: "memory");
        else asm volatile("s_barrier\n\ts_barrier" ::: "memory");
        __builtin_amdgcn_s_sleep(M_D6);
        sfor<0, 7>([&](auto I_) { gload_c<decltype(I_)::v * 1024 - 4096>(st[decltype(I_)::v], l16, xs); });
#endif
#pragma unroll 1
        for (int it = 0; it < (M_SR & 4 ? SPIN_MAX : RETRY_MAX); ++it) {
          unsigned m = 0;
          sfor<0, 7>([&](auto I_) {
            vwait<0>(st[decltype(I_)::v]);
            m = smax(m, st[decltype(I_)::v]);
          });
          if (!has_sent(m)) break;
          MDBG(1);
          if constexpr (M_SR & 4) { if (it >= 8) __builtin_amdgcn_s_sleep(32); }
          else probe_wait(xs - 4096, min(l, KSD - 1) * 256 + 252);
          sfor<0, 7>([&](auto I_) { greload_c<decltype(I_)::v * 1024 - 4096>(st[decltype(I_)::v], l16, xs); });
        }
        vwait_mem<0>();
        if constexpr (M_W1E & 4) sfor<0, KSA>(w1rl);
      }
      if constexpr (M_W1E & 8) sfor<0, KSA>(w1rl);
      if constexpr (M_HP) __builtin_amdgcn_s_setprio(M_HP);
      {
        sfor<0, 7>([&](auto I_) { pin4(st[decltype(I_)::v]); });
        f32x4 t = __builtin_bit_cast(f32x4, st[0]);
#pragma unroll
        for (int i = 1; i < 7; ++i) t += __builtin_bit_cast(f32x4, st[i]);
        float* tr = (float*)(smem + M_R + 48 * 1024) + w * 256;
        ((f32x4*)tr)[l] = t;
        float s1, s2, mean, rstd, sd;
        halves((tr[l] + tr[64 + l]) + (tr[128 + l] + tr[192 + l]), s1, s2);
        finish_stats(f32x2{s1, s2}, mean, rstd, sd);
        ((u32x4*)(smem + M_KV))[w * 64 + l] = aug_x(mean, sd, hh);
        ((float*)(smem + M_ST))[w * 64 + l] = rstd;
      }
      if (M_AO) vwait_mem<0>();
      else if (layer == 0) {
        vwait_mem<7 + NPF + (M_W1E & 9 ? NW1 : 0)>();
        asm volatile("s_barrier" ::: "memory");
        if constexpr (M_W1E & 2) sfor<0, KSA>(w1rl);
#if M_FE == 3
        fills();
#endif
        vwait_mem<NPF + (M_W1E & 11 ? NW1 : 0)>();
      }
#if M_F2
      asm volatile("" ::: "memory");
      flg[4 + w] = layer + 1;
      asm volatile("" ::: "memory");
      if constexpr (M_HP) __builtin_amdgcn_s_setprio(0);
#else
      BARRIER();
#endif
      MS(1);
#ifndef M_NOCPF
      if constexpr (spare) {
        if (layer == 0) {
          const char* pc;
          unsigned here;
          asm volatile("s_getpc_b64 vcc\n\ts_add_u32 %0, vcc_lo, _ZN12_GLOBAL__N_16k_megaEPcPKtPt@rel32@lo+4\n\t"
                       "s_addc_u32 vcc_hi, vcc_hi, _ZN12_GLOBAL__N_16k_megaEPcPKtPt@rel32@hi+12\n\t"
                       "s_sub_u32 %0, vcc_lo, %0\n\ts_sub_u32 vcc_lo, vcc_lo, %0\n\ts_mov_b64 %1, vcc"
                       : "=&s"(here), "=s"(pc) :: "scc", "vcc");
          const int lim = max((int)here - 3072, 0) & ~1023;
          glds<3>(l16, pc + min((((bi >> 4) * 2 + ns) * 4 + w) * 3072, lim), sm + M_R + 56 * 1024);
        }
      }
#endif
      if constexpr (M_B2E) bload();
      if constexpr (M_W1D) { if (layer == 0) __builtin_amdgcn_s_sleep(M_W1D); }
#if M_FE == 4
      if (layer == 0) fills();
#endif
      if constexpr (M_W1E == 0) sfor<0, KSA>(w1rl);
      else if constexpr (M_W1E & 8) {}
      else if constexpr (!(M_W1E & 3)) { if (layer == 0) sfor<0, KSA>(w1rl); }
      else if constexpr (!(M_W1E & 4)) { if (layer) sfor<0, KSA>(w1rl); }
#if M_FE == 0
      if (layer == 0) fills();
#endif
      MS(2);
      flag_wait();
      __builtin_amdgcn_s_setprio(3);
      MS(3);
      {
        u32x4 pv[16];
        float kvs[8];
        __builtin_amdgcn_s_sleep(M_D2);
        sfor<0, 16>([&](auto I_) {
          constexpr int i = decltype(I_)::v;
          gload_c<(i & 7) * 1024 - 4096>(pv[i], l16, kp + (i >> 3) * 8192 + 4096);
        });
#pragma unroll
        for (int j = 0; j < 8; ++j) kvs[j] = 0.f;
        sfor<0, 16>([&](auto I_) {
          constexpr int i = decltype(I_)::v;
          vwait<15 - i>(pv[i]);
          if (__builtin_expect(has_sent(smax(0u, pv[i])), 0)) {
#pragma unroll 1
            for (int it = 0; it < SPIN_MAX; ++it) {
              sfor<i, 16>([&](auto T_) { vwait<0>(pv[decltype(T_)::v]); });
              if (!has_sent(smax(0u, pv[i]))) break;
              MDBG(0);
              __builtin_amdgcn_s_sleep(M_RK);
              if (it >= 8) __builtin_amdgcn_s_sleep(32);
              sfor<i, 16>([&](auto T_) {
                constexpr int t = decltype(T_)::v;
                if (has_sent(smax(0u, pv[t]))) greload_c<(t & 7) * 1024 - 4096>(pv[t], l16, kp + (t >> 3) * 8192 + 4096);
              });
            }
            sfor<i, 16>([&](auto T_) { vwait<0>(pv[decltype(T_)::v]); });
          }
#pragma unroll
          for (int j = 0; j < 4; ++j) {
            kvs[2 * j] = opq(kvs[2 * j] + bflo(pv[i][j]));
            kvs[2 * j + 1] = opq(kvs[2 * j + 1] + bfhi(pv[i][j]));
          }
          SCHED();
        });
        ((u32x4*)(smem + M_KV))[w * 64 + l] = u32x4{pack2(kvs[0], kvs[1]), pack2(kvs[2], kvs[3]), pack2(kvs[4], kvs[5]), pack2(kvs[6], kvs[7])};
        BARRIER();
      }
      __builtin_amdgcn_s_setprio(0);
      MS(5);
      if constexpr (!spare) glds<2>(l16, ws + OFF_WO + (size_t)((28 + w) * NNT + 2 * nsw) * 1024, sm + M_W + (28 + w) * 2048);
      asm volatile("s_barrier" ::: "memory");
      MS(6);
      if constexpr (!spare) c1head(IC<0>{});
    }
    if constexpr (spare) {
      if (layer == 0) {
        const u32x4 sv = {SENT, SENT, SENT, SENT};
        const int idx = ns * 8 + w8;
        u32x4* d = (u32x4*)(ws + OFF_HF) + (size_t)(4 * g * KSF + 64 * M_HS + idx * (28 - 4 * M_HS)) * 64 + l;
#pragma unroll 1
        for (int i = 0; i < 7 - M_HS; ++i) {
          st_wt<0>(d, sv); st_wt<1024>(d, sv); st_wt<2048>(d, sv); st_wt<3072>(d, sv);
          d += 256;
          __builtin_amdgcn_s_sleep(M_HSL);
        }
        vwait_mem<0>();
        if (l == 0) st_wt1((float*)(ws + OFF_FLG) + g * 64 + idx, 0.f);
        d = (u32x4*)(ws + OFF_HF + HF_BYTES) + (size_t)(4 * g * KSF + idx * 28) * 64 + l;
#pragma unroll 1
        for (int i = 0; i < 7; ++i) {
          st_wt<0>(d, sv); st_wt<1024>(d, sv); st_wt<2048>(d, sv); st_wt<3072>(d, sv);
          d += 256;
          __builtin_amdgcn_s_sleep(16);
        }
        d = (u32x4*)(ws + OFF_XF + 3 * XF_BYTES) + (size_t)(4 * g * KSD + idx * 7) * 64 + l;
        st_wt<0>(d, sv); st_wt<1024>(d, sv); st_wt<2048>(d, sv); st_wt<3072>(d, sv);
        st_wt<0>(d + 256, sv); st_wt<1024>(d + 256, sv); st_wt<2048>(d + 256, sv);
#pragma unroll 1
        for (int k = 0; k < SPIN_MAX; ++k) {
          unsigned pb;
          gload1_c<0>(pb, (l & 15) * 2048, ex + (size_t)(4 * g * H_) * 2048);
          vwait1<0>(pb);
          if (!has_sent(pb)) break;
          __builtin_amdgcn_s_sleep(8);
        }
        asm volatile(".Lmega_tail:");
#pragma unroll 1
        for (int k = 0; k < SPIN_MAX; ++k) {
          __builtin_amdgcn_s_sleep(M_SS);
          unsigned pb;
          gload1_c<0>(pb, min(l, 27) * 1024, ws + OFF_XS + XS_BYTES + (size_t)(4 * g) * 7168);
          vwait1<0>(pb);
          if (__builtin_popcountll(__builtin_amdgcn_ballot_w64(pb == SENT)) < 60) break;
        }
      }
      MS_LAYER();
    } else {
    int pofs[7];
#pragma unroll
    for (int j = 0; j < 7; ++j) pofs[j] = ns + j >= 7 ? ns + j - 7 : ns + j;
    auto pwrite = [&](int off) {
      f32x4* pw = (f32x4*)(smem + M_R + off) + w * 512 + l;
#pragma unroll
      for (int gg = 0; gg < 4; ++gg) {
        pw[gg * 64] = f32x4{c0[4 * gg], c0[4 * gg + 1], c0[4 * gg + 2], c0[4 * gg + 3]};
        pw[(4 + gg) * 64] = f32x4{c1[4 * gg], c1[4 * gg + 1], c1[4 * gg + 2], c1[4 * gg + 3]};
      }
    };
    f32x4 sum[2];
    auto padd = [&](int off) {
      const f32x4* pr = (const f32x4*)(smem + M_R + off) + w * 128 + l;
      f32x4 pv[8];
#pragma unroll
      for (int i = 0; i < 8; ++i) pv[i] = pr[((i >> 1) * 8 + (i & 1)) * 64];
      SCHED();
#pragma unroll
      for (int gg = 0; gg < 2; ++gg) sum[gg] += (pv[gg] + pv[2 + gg]) + (pv[4 + gg] + pv[6 + gg]);
      PINV(sum[0]); PINV(sum[1]);
    };
    auto psum = [&]() {
      return nosent(u32x4{pack2(sum[0][0], sum[0][1]), pack2(sum[0][2], sum[0][3]), pack2(sum[1][0], sum[1][1]), pack2(sum[1][2], sum[1][3])});
    };
    MS(7);
    pwrite(a8 * 32768);
    PINV(xres); PINV(bs[0]); PINV(bs[1]);
    if constexpr (M_OT) PINV(otouch);
#pragma unroll
    for (int gg = 0; gg < 2; ++gg)
      sum[gg] = f32x4{bflo(xres[2 * gg]), bfhi(xres[2 * gg]), bflo(xres[2 * gg + 1]), bfhi(xres[2 * gg + 1])};
    BARRIER();
    padd(a8 * 32768);
    xres = psum();
    char* x1 = ws + OFF_XF + (size_t)(2 * layer + 1) * XF_BYTES + (size_t)((2 * tp + a8) * KSD + w) * 1024;
    st_wt<0>((u32x4*)(x1 + ns * 4096) + l, xres);
    MS(8);
    const unsigned l16b = l16 + KSF * 1024;
    const char* w2g = ws + OFF_W2 + (size_t)(2 * nsw * KSF + 2 * w8) * 1024;
    u32x4 w2r[28];
    auto w2at = [&](auto T_) {
      sfor<0, NW2G>([&](auto I_) {
        constexpr int i = decltype(I_)::v;
        if constexpr (W2T[i] == decltype(T_)::v) {
          gload<((i >> 1) & 1) * 1024>(w2r[i], (i & 1) ? l16b : l16, w2g + pofs[i >> 2] * 16384);
          SCHED();
        }
      });
    };
    {
      u32x4* xw = (u32x4*)(smem + M_R) + w * 64 + l;
      float* stw = (float*)(smem + M_ST) + w * 64 + l;
      stw[a8 * 512 + 256] = rstat(xres);
      auto w1aug = [&]() { sfor<0, KSA>([&](auto I_) { pin4(wr[decltype(I_)::v]); }); };
      if (lag) {
        u32x4 xr[12];
        const unsigned l16c = l16 + KSD * 1024;
        auto xissue = [&]() {
          sfor<0, 12>([&](auto I_) {
            constexpr int i = decltype(I_)::v;
            gload_c<0>(xr[i], (i & 1) ? l16c : l16, x1 + pofs[(i >> 1) + 1] * 4096);
          });
        };
        auto xtake = [&]() {
          unsigned m = 0;
          float s1[2] = {0.f, 0.f}, s2[2] = {0.f, 0.f};
          sfor<0, 12>([&](auto I_) {
            constexpr int i = decltype(I_)::v;
            vwait<11 - i>(xr[i]);
            xw[pofs[(i >> 1) + 1] * 256 + (i & 1) * (KSD * 64)] = xr[i];
            m = smax(m, xr[i]);
            rsum(xr[i], s1[i & 1], s2[i & 1]);
            SCHED();
          });
          stw[0] = rjoin(s1[0], s2[0]);
          stw[512] = rjoin(s1[1], s2[1]);
          return has_sent(m);
        };
        sfor<M_A1, M_A2>(w1load);
        __builtin_amdgcn_s_sleep(M_D5);
        xissue();
        asm volatile("s_barrier" ::: "memory");
        xw[ns * 256] = xres;
        if (__builtin_expect(xtake(), 0)) {
#if M_SR & 2
#pragma unroll 1
          for (int it = 0; it < SPIN_MAX; ++it) {
            unsigned m = 0;
            sfor<0, 12>([&](auto I_) { vwait<0>(xr[decltype(I_)::v]); m = smax(m, xr[decltype(I_)::v]); });
            if (!has_sent(m)) break;
            MDBG(1);
            if (it >= 8) __builtin_amdgcn_s_sleep(32);
            sfor<0, 12>([&](auto I_) {
              constexpr int i = decltype(I_)::v;
              if (has_sent(smax(0u, xr[i]))) greload_c<0>(xr[i], (i & 1) ? l16c : l16, x1 + pofs[(i >> 1) + 1] * 4096);
            });
          }
          sfor<0, 12>([&](auto I_) { vwait<0>(xr[decltype(I_)::v]); });
          xtake();
#else
          for (int it = 0; it < RETRY_MAX; ++it) {
            MDBG(1);
            probe_wait(x1, min(l >> 1, 6) * 4096 + (l & 1) * (KSD * 1024) + 1020);
            xissue();
            if (!xtake()) break;
          }
#endif
        }
        unsigned pb;
        gload1_c<0>(pb, (l & 15) * 4, ws + OFF_FLG + g * 256);
        sfor<M_A2, KSA>(w1load);
        w2at(IC<-1>{});
        MS(9);
        BARRIER();
        MS(10);
        vwait1<KSA - M_A2 + W2E>(pb);
        if (__builtin_expect(has_sent(pb), 0)) { MDBG(2); probe_wait(ws + OFF_FLG + g * 256, (l & 15) * 4); }
        asm volatile("s_barrier\n\ts_waitcnt vmcnt(%0)" :: "i"(W2E) : "memory");
      } else {
        asm volatile("s_barrier" ::: "memory");
        xw[ns * 256 + KSD * 64] = xres;
        sfor<0, 20>([&](auto I_) {
          constexpr int i = NW2G + (decltype(I_)::v >> 1), o = decltype(I_)::v & 1;
          glds<1>((i & 1) ? l16b : l16, w2g + pofs[i >> 2] * 16384 + ((i >> 1) & 1) * 1024 - o * 8192, sm + M_W + (w8 * 10 + i - NW2G - 40 * o) * 1024);
        });
        w2at(IC<-1>{});
        vwait_mem<20 + W2E>();
        MS(9);
        BARRIER();
        MS(10);
      }
      w1aug();
    }
    char* hg = ws + OFF_HF + (size_t)layer * HF_BYTES + (size_t)(2 * tp * KSF + 2 * w8) * 1024;
    unsigned xo0 = sm + M_R + l16;
    asm volatile("" : "+v"(xo0));
    const lds_u32x4p xl = (lds_u32x4p)xo0;
    constexpr int PD = M_PD;
    u32x4 xb[2][KSA];
#pragma unroll
    for (int s = 0; s < PD; ++s) xb[0][s] = xl[s * 64];
    f32x16 acc1, u;
    u32x4 hown[2][2];
    float k0, k1, sd;
    auto epi = [&](auto A_, auto Q_) {
      constexpr int a = decltype(A_)::v, q = decltype(Q_)::v;
      constexpr int k = q >> 2, gg = q & 3;
      const f32x16& y = acc1;
#pragma unroll
      for (int i = 4 * gg; i < 4 * gg + 4; ++i) {
        if constexpr (k == 0) u[i] = opq(y[i] * y[i]);
        else if constexpr (k == 1) u[i] = opq(__builtin_fmaf(u[i], k1, k0));
        else if constexpr (k == 2) u[i] = opq(u[i] * y[i]);
        else if constexpr (k == 3) u[i] = __builtin_amdgcn_exp2f(u[i]);
        else if constexpr (k == 4) u[i] = opq(__builtin_fmaf(u[i], sd, sd));
        else if constexpr (k == 5) u[i] = __builtin_amdgcn_rcpf(u[i]);
        else if constexpr (k == 6) u[i] = opq(u[i] * y[i]);
      }
      if constexpr (k == 6 && (gg & 1)) {
        hown[a][gg >> 1] = nosent(pack8(u, gg >> 1));
        st_wt<(gg >> 1) * 1024>((u32x4*)(hg + ns * 16384 + a * (KSF * 1024)) + l, hown[a][gg >> 1]);
      }
    };
    u32x4 hr[12];
    auto hload1 = [&](auto A_, auto I_) {
      constexpr int a = decltype(A_)::v, i = decltype(I_)::v;
      gload_c<(i & 1) * 1024>(hr[i], a ? l16b : l16, hg + pofs[1 + (i >> 1)] * 16384);
    };
    auto hload = [&](auto A_, auto J_) {
      hload1(A_, IC<2 * decltype(J_)::v - 2>{});
      hload1(A_, IC<2 * decltype(J_)::v - 1>{});
    };
    sfor<0, 2>([&](auto A_) {
      constexpr int a = decltype(A_)::v;
      acc1 = zero16();
      float sp[8], s1, s2, mean, rstd, v, r1;
      unsigned p1, p2, p3;
      sfor<0, KSA>([&](auto S_c) {
        constexpr int s = decltype(S_c)::v;
        if constexpr (s + PD < KSD) xb[a][s + PD] = xl[(a * KSD + s + PD) * 64];
        if constexpr (s == 2) {
#pragma unroll
          for (int i = 0; i < 8; ++i) sp[i] = ((const float*)(smem + M_ST))[(8 * a + i) * 64 + l];
        }
        if constexpr (s == 5) { sp[0] = opq(opq(sp[0] + sp[1]) + opq(sp[2] + sp[3])); sp[4] = opq(opq(sp[4] + sp[5]) + opq(sp[6] + sp[7])); PINV(sp[0]); PINV(sp[4]); }
        if constexpr (s == 6) { halves(sp[0] + sp[4], s1, s2); PINV(s1); PINV(s2); }
        if constexpr (s == 8) { finish_stats(f32x2{s1, s2}, mean, rstd, sd); PINV(mean); PINV(rstd); PINV(sd); }
        if constexpr (s == 10) {
          const float e0 = -2.f * 0.7978845608028654f * LOG2E;
          k0 = e0 * rstd;
          k1 = (e0 * 0.044715f) * rstd * rstd * rstd;
          v = hh ? sd : -mean;
          PINV(k0); PINV(k1); PINV(v);
        }
        if constexpr (s == 12) { p1 = bfbits(v); r1 = v - bflo(p1); PINV(p1); PINV(r1); }
        if constexpr (s == 14) { p2 = bfbits(r1); p3 = bfbits(r1 - bflo(p2)); PINV(p2); PINV(p3); }
        if constexpr (s == 16) {
          const unsigned ab = p1 | (p2 << 16);
          xb[a][KSD] = u32x4{hh ? ab : (p1 | (p1 << 16)), hh ? p3 : ab, hh ? 0u : (p2 | (p3 << 16)), 0u};
          PINV(xb[a][KSD]);
        }
#ifndef M_OLDP
        if constexpr (a == 0 && s == 18) {
          v = opq(__uint_as_float(wr[KSD][0]) + __uint_as_float(wr[KSD][1])) + opq(__uint_as_float(wr[KSD][2]) + __uint_as_float(wr[KSD][3]));
          p1 = bfbits(v); r1 = v - bflo(p1);
          PINV(p1); PINV(r1);
        }
        if constexpr (a == 0 && s == 20) { p2 = bfbits(r1); p3 = bfbits(r1 - bflo(p2)); PINV(p2); PINV(p3); }
        if constexpr (a == 0 && s == 22) {
          wr[KSD] = u32x4{hh ? wr[KSD][0] : p1 | (p2 << 16), hh ? wr[KSD][1] : p3 | (p1 << 16), hh ? 0u : p2 | (p1 << 16), 0u};
          PINV(wr[KSD]);
        }
#endif
        w2at(IC<200 * a + s>{});
        acc1 = MFMA32(asfrag(wr[s]), asfrag(xb[a][s]), acc1);
        PINV(acc1);
        SCHED();
      });
      BARRIER();
      SCHED();
      MS(11 + 2 * a);
      if constexpr (a == 1) vwait_mem<NW2G + 2>();
      sfor<0, 28>([&](auto Q_) {
        constexpr int q = decltype(Q_)::v;
        epi(A_, Q_);
        if constexpr (a == 0) w2at(IC<100 + q>{});
        if constexpr (a == 1 && NW2G + q < 28) { w2r[NW2G + q] = ((const u32x4*)(smem + M_W))[(w8 * 10 + q) * 64 + l]; SCHED(); }
        if constexpr (a == 1) sfor<0, 12>([&](auto I_) { if constexpr (hq(decltype(I_)::v) == q) { hload1(IC<0>{}, I_); SCHED(); } });
      });
      SCHED();
      if constexpr (a == 0) {
#pragma unroll
        for (int s = 0; s < PD; ++s) xb[1][s] = xl[(KSD + s) * 64];
      }
      BARRIER();
      SCHED();
      MS(12 + 2 * a);
    });
    vwait<w2_after() + 2 + 12>(w2r[0]);
    sfor<1, NW2G>([&](auto I_) { pin4(w2r[decltype(I_)::v]); });
    auto gemm2 = [&](auto A_) {
      constexpr int a = decltype(A_)::v;
      bool bad;
      auto check = [&](auto J_) {
        constexpr int j = decltype(J_)::v;
        constexpr int n = 12 - 2 * j + (a == 0 ? (hq(2 * j - 1) < 25) + (hq(2 * j - 1) < 27) : 0);
        vwait<n>(hr[2 * j - 2]);
        vwait<n>(hr[2 * j - 1]);
        bad = has_sent(max(smax(0u, hr[2 * j - 2]), smax(0u, hr[2 * j - 1])));
      };
      c0 = MFMA32(asfrag(w2r[0]), asfrag(hown[a][0]), zero16());
      c1 = MFMA32(asfrag(w2r[1]), asfrag(hown[a][0]), zero16());
      PINV(c0); PINV(c1);
      SCHED();
      check(IC<1>{});
      SCHED();
      c0 = MFMA32(asfrag(w2r[2]), asfrag(hown[a][1]), c0);
      c1 = MFMA32(asfrag(w2r[3]), asfrag(hown[a][1]), c1);
      PINV(c0); PINV(c1);
      SCHED();
      sfor<1, 7>([&](auto J_) {
        constexpr int j = decltype(J_)::v;
        u32x4 &h0 = hr[2 * j - 2], &h1 = hr[2 * j - 1];
        if (__builtin_expect(bad, 0)) {
          for (int it = 0; it < SPIN_MAX; ++it) {
            MDBG(2);
            hload(A_, J_);
            vwait<0>(h0);
            vwait<0>(h1);
            if (!has_sent(smax(smax(0u, h0), h1))) break;
          }
        }
        c0 = MFMA32(asfrag(w2r[4 * j]), asfrag(h0), c0);
        c1 = MFMA32(asfrag(w2r[4 * j + 1]), asfrag(h0), c1);
        PINV(c0); PINV(c1);
        SCHED();
        if constexpr (j < 6) check(IC<j + 1>{});
        if constexpr (a == 1 && j == M_MID) { if (lag) asm volatile("s_barrier" ::: "memory"); }
        SCHED();
        c0 = MFMA32(asfrag(w2r[4 * j + 2]), asfrag(h1), c0);
        c1 = MFMA32(asfrag(w2r[4 * j + 3]), asfrag(h1), c1);
        PINV(c0); PINV(c1);
        SCHED();
      });
    };
    gemm2(IC<0>{});
    asm volatile("s_barrier" ::: "memory");
    SCHED();
    MS(15);
    sfor<1, 7>([&](auto J_) { hload(IC<1>{}, J_); });
    pwrite(lag ? 32768 : 0);
    {
#pragma unroll
      for (int gg = 0; gg < 2; ++gg) {
        sum[gg][0] = bflo(bs[gg][0]) + bflo(xres[2 * gg]);
        sum[gg][1] = bfhi(bs[gg][0]) + bfhi(xres[2 * gg]);
        sum[gg][2] = bflo(bs[gg][1]) + bflo(xres[2 * gg + 1]);
        sum[gg][3] = bfhi(bs[gg][1]) + bfhi(xres[2 * gg + 1]);
      }
      PINV(sum[0]); PINV(sum[1]);
    }
    if (lag) padd(0);
    BARRIER();
    SCHED();
    MS(16);
    if (lag) padd(32768);
    gemm2(IC<1>{});
    if (!lag) asm volatile("s_barrier" ::: "memory");
    SCHED();
    MS(17);
    pwrite(lag ? 32768 : 0);
    BARRIER();
    SCHED();
    if (!lag) {
      padd(0);
      if (layer == 0) wdma_kv();
      asm volatile("s_barrier" ::: "memory");
      padd(32768);
    }
    SCHED();
    MS(18);
    xres = psum();
    if (layer) {
      unsigned short* o = out + (size_t)((2 * tp + a8) * 32 + r) * D_ + 16 * so + 4 * hh;
      *(u32x2*)(o + 0) = u32x2{xres[0], xres[1]};
      *(u32x2*)(o + 8) = u32x2{xres[2], xres[3]};
    } else {
      st_wt<0>((u32x4*)(ws + OFF_XF + 2 * XF_BYTES) + (size_t)((2 * tp + a8) * KSD + so) * 64 + l, xres);
      st_wt1((float*)(ws + OFF_XS + XS_BYTES) + ((2 * tp + a8) * KSD + so) * 64 + l, rstat(xres));
    }
    MS(19);
    MS_LAYER();
    }
  }
  MS_END();
}

__global__ __launch_bounds__(512, 2) void k_mega(char* __restrict__ ws, const unsigned short* __restrict__ b2,
                                                 unsigned short* __restrict__ out) {
  __shared__ __attribute__((aligned(16))) char smem[M_LDS];
  if (((blockIdx.x >> 3) & 15) >= 14) mega<true>(smem, ws, b2, out);
  else mega<false>(smem, ws, b2, out);
}

}

extern "C" void kernel_launch(void* const* d_in, const int* in_sizes, int n_in, void* d_out, int out_size,
                              void* d_ws, size_t ws_size, hipStream_t stream) {
  const unsigned short* x = (const unsigned short*)d_in[0];
  const unsigned short* wqkv = (const unsigned short*)d_in[1];
  const unsigned short* wphi = (const unsigned short*)d_in[2];
  const unsigned short* wo = (const unsigned short*)d_in[3];
  const unsigned short* w1 = (const unsigned short*)d_in[5];
  const unsigned short* b1 = (const unsigned short*)d_in[6];
  const unsigned short* w2 = (const unsigned short*)d_in[7];
  const unsigned short* b2 = (const unsigned short*)d_in[8];
  char* ws = (char*)d_ws;
  k_prep<<<P_BLOCKS, 256, 0, stream>>>(x, wqkv, wphi, wo, w1, b1, w2, b2, ws);
  k_mega<<<256, 512, 0, stream>>>(ws, b2, (unsigned short*)d_out);
}
```
